```python
import torch

def _softmax(x, axis=-1):
    return torch.softmax(x, dim=axis)
def _gelu(x):
    return torch.nn.functional.gelu(x, approximate="tanh")
def _silu(x):
    return torch.nn.functional.silu(x)
def _sigmoid(x):
    return torch.sigmoid(x)
def _layernorm(x, axis=-1, eps=1e-5):
    m = x.mean(dim=axis, keepdim=True)
    v = x.var(dim=axis, keepdim=True, unbiased=False)
    return (x - m) / torch.sqrt(v + eps)
def _scatter_add(tgt, idx, src, axis=0):
    return tgt.clone().index_add_(axis, idx, src)
def _segment_sum(src, seg, n):
    out = torch.zeros((n,) + src.shape[1:], dtype=src.dtype)
    return out.index_add_(0, seg, src)
def _pad(x, pw):
    flat = tuple(v for pair in reversed(pw) for v in pair)
    return torch.nn.functional.pad(x, flat)

kernel_name = 'sequential_preLN__sliding_local_dilated__geglu_ffn__prompt'

def setup_inputs():

    _w = (lambda a: (lambda t: t.to(torch.bfloat16) if t.is_floating_point() else t)(torch.as_tensor(a)))
    def _A_setup():
        B, S, D = 1, 2048, 1024
        return {
            "x": _w((lambda *s: torch.randn(*s, dtype=torch.bfloat16) * 0.05)(B, S, D)),
            "wqkv": _w((lambda *s: torch.randn(*s, dtype=torch.bfloat16) * 0.05)(D, 3 * D)),
            "wo": _w((lambda *s: torch.randn(*s, dtype=torch.bfloat16) * 0.05)(D, D)),
        }
    _A_in = _A_setup()

    def _B_setup():
        B, S, D, F = 1, 2048, 1024, 3584
        return {
            "x": _w((lambda *s: torch.randn(*s, dtype=torch.bfloat16) * 0.05)(B, S, D)),
            "w_gate": _w((lambda *s: torch.randn(*s, dtype=torch.bfloat16) * 0.05)(D, F)),
            "w_up": _w((lambda *s: torch.randn(*s, dtype=torch.bfloat16) * 0.05)(D, F)),
            "w_down": _w((lambda *s: torch.randn(*s, dtype=torch.bfloat16) * 0.05)(F, D)),
        }
    _B_in = _B_setup()

    return {
        "A_x": _A_in["x"],
        "A_wqkv": _A_in["wqkv"],
        "A_wo": _A_in["wo"],
        "B_x": _B_in["x"],
        "B_w_gate": _B_in["w_gate"],
        "B_w_up": _B_in["w_up"],
        "B_w_down": _B_in["w_down"],
    }

def reference(A_x, A_wqkv, A_wo, B_x, B_w_gate, B_w_up, B_w_down):
    def _A_ref(x, wqkv, wo):
        B, S, D = x.shape
        H = 32; Dh = D // H
        W, dil = 8, 2
        qkv = (lambda x, *d: x.permute(*d))((x @ wqkv).reshape(B, S, 3, H, Dh), 2, 0, 3, 1, 4)
        q, k, v = qkv[0], qkv[1], qkv[2]
        scale = Dh ** -0.5
        pos = torch.arange(S)
        off = torch.arange(W) * dil
        idx = pos[:, None] - off[None, :]
        mask = idx >= 0
        idx_c = torch.where(mask, idx, 0).reshape(-1)
        kg = k[:, :, idx_c, :].reshape(B, H, S, W, Dh)
        vg = v[:, :, idx_c, :].reshape(B, H, S, W, Dh)
        logits = torch.einsum("bhsd,bhswd->bhsw", q, kg) * scale
        logits = torch.where(mask[None, None, :, :], logits, -1e9)
        attn = _softmax(logits, axis=-1)
        o = torch.einsum("bhsw,bhswd->bhsd", attn, vg)
        return (lambda x, *d: x.permute(*d))(o, 0, 2, 1, 3).reshape(B, S, D) @ wo

    def _B_ref(x, w_gate, w_up, w_down):
        return (_gelu(x @ w_gate) * (x @ w_up)) @ w_down

    x = A_x
    h = x + _A_ref(_layernorm(x, axis=-1), A_wqkv, A_wo)
    h2 = h + _B_ref(_layernorm(h, axis=-1), B_w_gate, B_w_up, B_w_down)
    return h2
```

```cpp
#include <hip/hip_runtime.h>
#include <hip/hip_bf16.h>
#include <stdint.h>

#pragma clang diagnostic ignored "-Winline-asm"
#pragma clang diagnostic ignored "-Wint-to-pointer-cast"

typedef unsigned short u16;
typedef __bf16 bf16x8 __attribute__((ext_vector_type(8)));
typedef __bf16 bf16x2 __attribute__((ext_vector_type(2)));
typedef short s16x4 __attribute__((ext_vector_type(4)));
typedef float f32x4 __attribute__((ext_vector_type(4)));
typedef float f32x2 __attribute__((ext_vector_type(2)));
typedef unsigned u32x4 __attribute__((ext_vector_type(4)));
typedef unsigned u32x2 __attribute__((ext_vector_type(2)));
#define LAS __attribute__((address_space(3)))
#define DI __device__ __forceinline__

constexpr int S_ = 2048, D_ = 1024, F_ = 3584;
constexpr float LN_EPS = 1e-5f;

constexpr size_t WS_O = 0;
constexpr size_t WS_HC = WS_O + (size_t)32 * 131072;
constexpr size_t WS_ACT = WS_HC + (size_t)33 * 131072;
constexpr size_t WS_MUX = WS_ACT + (size_t)112 * 131072;
constexpr size_t WS_MU = WS_MUX + 8192;
constexpr size_t WS_WTAB = WS_MU + 8192;

DI u32x4 make_srd(const void* p, unsigned bytes) {
  uint64_t a = (uint64_t)p;
  u32x4 r; r.x = (unsigned)a; r.y = (unsigned)(a >> 32) & 0xffffu; r.z = bytes; r.w = 0x00020000u;
  return r;
}
DI void dma(unsigned lds, unsigned voff, u32x4 srd, unsigned soff) {
  asm volatile("s_mov_b32 m0, %0\n\ts_nop 0\n\tbuffer_load_dwordx4 %1, %2, %3 offen lds" ::"s"(lds), "v"(voff), "s"(srd), "s"(soff) : "m0");
}
#define SB() __builtin_amdgcn_sched_barrier(0)
#define MEMFENCE() asm volatile("" ::: "memory")
#define WAIT_VL(V) do { SB(); MEMFENCE(); __builtin_amdgcn_s_waitcnt(((V) & 15) | (((V) >> 4) << 14) | 0x70); MEMFENCE(); SB(); } while (0)
#define WAIT_L0() do { SB(); MEMFENCE(); __builtin_amdgcn_s_waitcnt(0xC07F); MEMFENCE(); SB(); } while (0)
#define BARRIER() do { SB(); MEMFENCE(); __builtin_amdgcn_s_barrier(); MEMFENCE(); SB(); } while (0)

DI u32x4 lds128(unsigned a) { return *(LAS u32x4*)(a); }
DI f32x4 lds128f(unsigned a) { return *(LAS f32x4*)(a); }
DI u32x2 ldstr(unsigned a) { return __builtin_bit_cast(u32x2, __builtin_amdgcn_ds_read_tr16_b64_v4i16((LAS s16x4*)(a))); }
DI f32x4 mfma(u32x4 a, u32x4 b, f32x4 c) {
  return __builtin_amdgcn_mfma_f32_16x16x32_bf16(__builtin_bit_cast(bf16x8, a), __builtin_bit_cast(bf16x8, b), c, 0, 0, 0);
}
DI u32x4 cat(u32x2 lo, u32x2 hi) { u32x4 r; r.x = lo.x; r.y = lo.y; r.z = hi.x; r.w = hi.y; return r; }
DI unsigned pk(float a, float b) { f32x2 v = {a, b}; return __builtin_bit_cast(unsigned, __builtin_convertvector(v, bf16x2)); }
DI float dot2(unsigned a, unsigned b, float c) {
  return __builtin_amdgcn_fdot2_f32_bf16(__builtin_bit_cast(bf16x2, a), __builtin_bit_cast(bf16x2, b), c, false);
}
DI float bflo(unsigned u) { return __uint_as_float(u << 16); }
DI float bfhi(unsigned u) { return __uint_as_float(u & 0xffff0000u); }
DI float red_add(float v) {
  auto a = __builtin_amdgcn_permlane32_swap(__float_as_uint(v), __float_as_uint(v), false, false);
  v = __uint_as_float(a[0]) + __uint_as_float(a[1]);
  auto b = __builtin_amdgcn_permlane16_swap(__float_as_uint(v), __float_as_uint(v), false, false);
  return __uint_as_float(b[0]) + __uint_as_float(b[1]);
}
DI float red_max(float v) {
  auto a = __builtin_amdgcn_permlane32_swap(__float_as_uint(v), __float_as_uint(v), false, false);
  v = fmaxf(__uint_as_float(a[0]), __uint_as_float(a[1]));
  auto b = __builtin_amdgcn_permlane16_swap(__float_as_uint(v), __float_as_uint(v), false, false);
  return fmaxf(__uint_as_float(b[0]), __uint_as_float(b[1]));
}
DI void swap32f(float& a, float& b) {
  auto r = __builtin_amdgcn_permlane32_swap(__float_as_uint(a), __float_as_uint(b), false, false);
  a = __uint_as_float(r[0]); b = __uint_as_float(r[1]);
}
DI void swap32u(unsigned& a, unsigned& b) { auto r = __builtin_amdgcn_permlane32_swap(a, b, false, false); a = r[0]; b = r[1]; }
DI void swap16u(unsigned& a, unsigned& b) { auto r = __builtin_amdgcn_permlane16_swap(a, b, false, false); a = r[0]; b = r[1]; }
constexpr unsigned ONES2 = 0x3F803F80u;
#define STAMP_AT(kern, i)
#define STAMP_RT(kern, i)
#define STAMP_L0()
#define STAMP_L1(kern)
#define STAMP_END(kern)
#define STAMP_BEG(kern)

constexpr int K1_HALO = 0, K1_RING = 32768, K1_STAGE = 40960, K1_LDS = K1_RING + 3 * K1_STAGE;
struct K1Frag { u32x4 a[2]; u32x4 h; u32x2 b[6][2]; };

__global__ __launch_bounds__(512, 2) void k1_qkv_attn(const u16* __restrict__ x, const u16* __restrict__ wqkv, const u16* __restrict__ wo,
                                                      char* __restrict__ ws) {
  __shared__ __attribute__((aligned(1024))) char smem[K1_LDS];
  const unsigned L0 = (unsigned)(size_t)(LAS char*)smem;
  const int tid = threadIdx.x, lane = tid & 63;
  const int wid = __builtin_amdgcn_readfirstlane(tid >> 6);
  const int g = wid & 3, hh = wid >> 2, fr = lane & 15, fq = lane >> 4;
  const int xcd = blockIdx.x & 7, idx = blockIdx.x >> 3;
  const int pm = (xcd >> 1) * 4 + (idx >> 3), pn = (xcd & 1) * 8 + (idx & 7);
  const int s0 = pm * 128;
  STAMP_BEG(0);

  u32x4 wrow0, wrow1;
  {
    const u16* p = wo + (size_t)(blockIdx.x * 4 + g) * D_ + lane * 8;
    asm volatile("global_load_dwordx4 %0, %1, off" : "=v"(wrow0) : "v"(p));
    asm volatile("global_load_dwordx4 %0, %1, off offset:1024" : "=v"(wrow1) : "v"(p));
  }

  const u32x4 srdX = make_srd(x, S_ * D_ * 2);
  u32x4 srdW = make_srd(wqkv, D_ * 3 * D_ * 2);
  u32x4 srdA = srdX;
  const int l3 = lane >> 3, l7 = lane & 7;
  unsigned voffA[2], voffH[2];
#pragma unroll
  for (int i = 0; i < 2; ++i) {
    const unsigned c = (unsigned)(l7 ^ (4 * i + (lane >> 4))) * 16;
    voffA[i] = (unsigned)(s0 + 16 * wid + 8 * i + l3) * 2048 + c;
    const int hr = s0 - 16 + 8 * i + l3;
    voffH[i] = (unsigned)(hr < 0 ? 0 : hr) * 2048 + c;
  }
  const unsigned voffB = (unsigned)(8 * wid + l3) * 6144 + pn * 128 + (unsigned)(l7 ^ ((((lane >> 4) & 1) | ((wid & 1) << 1)) << 1)) * 16;

  auto stage = [&](int kt, unsigned so) {
    const unsigned lb = L0 + K1_RING + so;
    dma(lb + wid * 2048, voffA[0], srdA, kt * 128);
    dma(lb + wid * 2048 + 1024, voffA[1], srdA, kt * 128);
#pragma unroll
    for (int cg = 0; cg < 3; ++cg) dma(lb + 16384 + cg * 8192 + wid * 1024, voffB, srdW, kt * (64 * 6144) + cg * 2048);
  };
#pragma unroll
  for (int q = 0; q < 2; ++q)
#pragma unroll
    for (int i = 0; i < 2; ++i) dma(L0 + K1_HALO + (2 * wid + q) * 2048 + i * 1024, voffH[i], srdX, (2 * wid + q) * 128);
  stage(0, 0); stage(1, K1_STAGE); stage(2, 2 * K1_STAGE);

  unsigned ra[2][2], rh[2], rb[6];
#pragma unroll
  for (int ks = 0; ks < 2; ++ks) {
    const unsigned c = (unsigned)((4 * ks + fq) ^ (fr >> 1)) * 16;
#pragma unroll
    for (int m = 0; m < 2; ++m) ra[m][ks] = L0 + K1_RING + (unsigned)(16 * (2 * g + (m ^ hh)) + fr) * 128 + c;
    rh[ks] = L0 + K1_HALO + fr * 128 + c;
  }
  {
    const int q = fr >> 2, p = lane & 3, f = ((q >> 1) & 1) | ((fq & 1) << 1);
#pragma unroll
    for (int j = 0; j < 6; ++j) {
      const int a = j < 4 ? ((j + g) & 3) : 4 + ((j + g) & 1);
      rb[j] = L0 + K1_RING + 16384 + (a >> 1) * 8192 + (unsigned)(8 * fq + q) * 128 + (unsigned)((2 * hh + (a & 1)) ^ f) * 32 + 8 * p;
    }
  }

  f32x4 acc[2][6], accHT = {0, 0, 0, 0}, accHV = {0, 0, 0, 0};
#pragma unroll
  for (int m = 0; m < 2; ++m)
#pragma unroll
    for (int j = 0; j < 6; ++j) acc[m][j] = (f32x4){0, 0, 0, 0};
  float sx = 0.f, sq = 0.f;
  K1Frag F0, F1;

  auto rd = [&](K1Frag& F, int i, int ks, unsigned so, unsigned ho) {
    if (i < 2) F.a[i] = lds128(ra[i][ks] + so);
    else if (i == 2) F.h = lds128(rh[ks] + ho);
    else { const int e = i - 3, j = e >> 1, t = e & 1; F.b[j][t] = ldstr(rb[j] + so + ks * 4096 + t * 512); }
  };
  auto mm = [&](K1Frag& F, int i) {
    if (i < 12) {
      const int j = i >> 1, m = i & 1;
      const u32x4 b = cat(F.b[j][0], F.b[j][1]);
      if (j < 4) acc[m][j] = mfma(b, F.a[m], acc[m][j]); else acc[m][j] = mfma(F.a[m], b, acc[m][j]);
    } else if (i == 12) accHT = mfma(cat(F.b[0][0], F.b[0][1]), F.h, accHT);
    else accHV = mfma(F.h, cat(F.b[4][0], F.b[4][1]), accHV);
  };
  auto side = [&](K1Frag& F, int i) {
    if (i < 4) { sx = dot2(F.a[0][i], ONES2, sx); sq = dot2(F.a[0][i], F.a[0][i], sq); }
    else if (i < 8) F.h[i - 4] = fr == 0 ? ONES2 : F.h[i - 4];
  };

  WAIT_VL(10); BARRIER();
#pragma unroll
  for (int i = 0; i < 15; ++i) rd(F0, i, 0, 0, 0);

  STAMP_L0();
  unsigned so = 0;
  for (int kt = 0; kt < 16; ++kt) {
    const unsigned ho = kt * 2048;
    const unsigned so1 = so == 2 * K1_STAGE ? 0 : so + K1_STAGE;
#pragma unroll
    for (int i = 0; i < 14; ++i) {
      mm(F0, i); rd(F1, i, 1, so, ho); if (i == 13) rd(F1, 14, 1, so, ho);
      side(F0, i);
      SB();
    }
    WAIT_VL(5); BARRIER();
    if (kt == 13) { srdA.z = 0; srdW.z = 0; }
    const unsigned lb = L0 + K1_RING + so;
    const int k3 = kt + 3;
#pragma unroll
    for (int i = 0; i < 14; ++i) {
      mm(F1, i); rd(F0, i, 0, so1, ho + 2048); if (i == 13) rd(F0, 14, 0, so1, ho + 2048);
      side(F1, i);
      if (i == 3) dma(lb + wid * 2048, voffA[0], srdA, k3 * 128);
      if (i == 5) dma(lb + wid * 2048 + 1024, voffA[1], srdA, k3 * 128);
      if (i == 7) dma(lb + 16384 + wid * 1024, voffB, srdW, k3 * (64 * 6144));
      if (i == 9) dma(lb + 16384 + 8192 + wid * 1024, voffB, srdW, k3 * (64 * 6144) + 2048);
      if (i == 11) dma(lb + 16384 + 16384 + wid * 1024, voffB, srdW, k3 * (64 * 6144) + 4096);
      SB();
    }
    so = so1;
  }
  WAIT_VL(0); BARRIER();
  STAMP_L1(0);

  constexpr unsigned X_OFF = K1_RING;
  constexpr unsigned ST_OFF = K1_RING + 55296;
  constexpr unsigned CT_OFF = ST_OFF + 144 * 8;
  constexpr unsigned CV_OFF = CT_OFF + 512;
  float* mux = (float*)(ws + WS_MUX);
  {
    sx = red_add(sx); sq = red_add(sq);
    const float mean = sx * (1.0f / D_), var = fmaxf(sq * (1.0f / D_) - mean * mean, 0.f);
    const float rstd = __builtin_amdgcn_rsqf(var + LN_EPS);
    const int tl = 16 * (2 * g + hh) + fr;
    if (fq == 0) {
      *(LAS f32x2*)(L0 + ST_OFF + (16 + tl) * 8) = (f32x2){mean, rstd};
      if (pn == 0) mux[s0 + tl] = mean;
    }
    float hx = 0.f, hq = 0.f;
    const int hrow = 2 * wid + (lane >> 5), sub = lane & 31;
#pragma unroll
    for (int i = 0; i < 4; ++i) {
      const u32x4 v = lds128(L0 + K1_HALO + ((sub >> 3) + 4 * i) * 2048 + hrow * 128 + (sub & 7) * 16);
#pragma unroll
      for (int e = 0; e < 4; ++e) { hx = dot2(v[e], ONES2, hx); hq = dot2(v[e], v[e], hq); }
    }
#pragma unroll
    for (int o = 1; o < 32; o <<= 1) { hx += __shfl_xor(hx, o); hq += __shfl_xor(hq, o); }
    const float hm = hx * (1.0f / D_), hv = fmaxf(hq * (1.0f / D_) - hm * hm, 0.f);
    if (sub == 0) *(LAS f32x2*)(L0 + ST_OFF + hrow * 8) = (f32x2){hm, __builtin_amdgcn_rsqf(hv + LN_EPS)};
    if (fr == 0) *(LAS f32x4*)(L0 + CT_OFF + (hh * 4 + g) * 64 + fq * 16) = accHT;
    if (fq == 0) *(LAS float*)(L0 + CV_OFF + (hh * 2 + (g & 1)) * 64 + fr * 4) = accHV[0];
  }
  WAIT_L0(); BARRIER();
  {
    const unsigned xb = L0 + X_OFF + hh * (9 * 3072) + lane * 16;
    f32x4 ct[4]; float cv[2];
#pragma unroll
    for (int j = 0; j < 4; ++j) ct[j] = lds128f(L0 + CT_OFF + (hh * 4 + ((j + g) & 3)) * 64 + fq * 16);
#pragma unroll
    for (int j = 0; j < 2; ++j) cv[j] = *(LAS float*)(L0 + CV_OFF + (hh * 2 + ((j + g) & 1)) * 64 + fr * 4);
#pragma unroll
    for (int m = 0; m < 3; ++m) {
      const int ti = m == 2 ? 0 : 2 * g + (m ^ hh) + 1;
      const f32x2 st = *(LAS f32x2*)(L0 + ST_OFF + (16 * ti + fr) * 8);
      const f32x4 s01 = lds128f(L0 + ST_OFF + (16 * ti + 4 * fq) * 8), s23 = lds128f(L0 + ST_OFF + (16 * ti + 4 * fq + 2) * 8);
#pragma unroll
      for (int j = 0; j < (m == 2 ? 1 : 4); ++j) {
        const f32x4 a = m == 2 ? accHT : acc[m & 1][j];
        const f32x4 v = (a - ct[j] * st.x) * st.y;
        const int at = (j + g) & 3;
        u32x2 w; w.x = pk(v[0], v[1]); w.y = pk(v[2], v[3]);
        *(LAS u32x2*)(xb + ti * 3072 + (at >> 1) * 1024 + (at & 1) * 8) = w;
      }
#pragma unroll
      for (int j = 0; j < (m == 2 ? 1 : 2); ++j) {
        const f32x4 a = m == 2 ? accHV : acc[m & 1][4 + j];
        u32x2 w;
        w.x = pk((a[0] - cv[j] * s01[0]) * s01[1], (a[1] - cv[j] * s01[2]) * s01[3]);
        w.y = pk((a[2] - cv[j] * s23[0]) * s23[1], (a[3] - cv[j] * s23[2]) * s23[3]);
        *(LAS u32x2*)(xb + ti * 3072 + 2048 + ((j + g) & 1) * 8) = w;
      }
    }
  }
  WAIT_L0(); BARRIER();
  {
    const unsigned xb = L0 + X_OFF + hh * (9 * 3072) + 2 * g * 3072 + lane * 16;
    u32x4 kf[3], vf[3], qf[2];
#pragma unroll
    for (int t = 0; t < 3; ++t) { kf[t] = lds128(xb + t * 3072 + 1024); vf[t] = lds128(xb + t * 3072 + 2048); }
#pragma unroll
    for (int t = 0; t < 2; ++t) qf[t] = lds128(xb + (t + 1) * 3072);
    const float c = 0.17677669529663687f * 1.4426950408889634f;
    char* of = ws + WS_O + (size_t)(2 * pn + hh) * 131072 + (size_t)(pm * 8 + 2 * g) * 1024 + lane * 16;
#pragma unroll
    for (int m = 0; m < 2; ++m) {
      const f32x4 z = {0, 0, 0, 0};
      f32x4 sp = mfma(kf[m], qf[m], z), sc = mfma(kf[m + 1], qf[m], z);
      const bool pok = (m == 1) || (pm | g) != 0;
      float s[8]; bool ok[8];
#pragma unroll
      for (int r = 0; r < 4; ++r) {
        const int d = fr - 4 * fq - r;
        ok[r] = pok && ((d & 1) == 0) && d <= -2;
        ok[4 + r] = ((d & 1) == 0) && d >= 0 && d <= 14;
        s[r] = ok[r] ? sp[r] : -INFINITY;
        s[4 + r] = ok[4 + r] ? sc[r] : -INFINITY;
      }
      float mx = fmaxf(fmaxf(fmaxf(s[0], s[1]), fmaxf(s[2], s[3])), fmaxf(fmaxf(s[4], s[5]), fmaxf(s[6], s[7])));
      mx = red_max(mx);
      const float mc = mx * c;
      float sum = 0.f;
#pragma unroll
      for (int e = 0; e < 8; ++e) { s[e] = __builtin_amdgcn_exp2f(s[e] * c - mc); sum += s[e]; }
      sum = red_add(sum);
      const float inv = __builtin_amdgcn_rcpf(sum);
      u32x4 p;
#pragma unroll
      for (int e = 0; e < 4; ++e) p[e] = pk(s[2 * e] * inv, s[2 * e + 1] * inv);
      u32x4 va, vb;
      va.x = vf[m].x; va.y = vf[m].y; va.z = vf[m + 1].x; va.w = vf[m + 1].y;
      vb.x = vf[m].z; vb.y = vf[m].w; vb.z = vf[m + 1].z; vb.w = vf[m + 1].w;
      const f32x4 oa = mfma(va, p, z), ob = mfma(vb, p, z);
      u32x4 w; w.x = pk(oa[0], oa[1]); w.y = pk(oa[2], oa[3]); w.z = pk(ob[0], ob[1]); w.w = pk(ob[2], ob[3]);
      *(u32x4*)(of + m * 1024) = w;
    }
  }
  asm volatile("" : "+v"(wrow0), "+v"(wrow1));
  if (hh == 0) {
    float s = 0.f;
#pragma unroll
    for (int e = 0; e < 4; ++e) { s = dot2(wrow0[e], ONES2, s); s = dot2(wrow1[e], ONES2, s); }
#pragma unroll
    for (int o = 1; o < 64; o <<= 1) s += __shfl_xor(s, o);
    s *= (1.0f / D_);
    const unsigned hi = pk(s, 0.f) & 0xffffu;
    const unsigned lo = pk(s - bflo(hi), 0.f) & 0xffffu;
    const int k = blockIdx.x * 4 + g, dd = k & 31;
    u16* t = (u16*)(ws + WS_WTAB) + (k >> 5) * 96 + ((dd >> 2) & 3) * 24 + 4 * (dd >> 4) + (dd & 3);
    if (lane < 3) t[lane * 8] = (u16)(lane == 0 ? hi : lane == 1 ? lo : 0u);
  }
  STAMP_END(0);
}

constexpr int K2_SLOT = 24576, K2_NS = 6, K2_TAB = K2_SLOT * K2_NS, K2_LDS = K2_TAB + 8192;
struct K2Frag { u32x4 a[2]; u32x2 b[4][2]; u32x4 w; };

template <int MODE>
__global__ __launch_bounds__(512, 2) void k24_proj(const u16* __restrict__ x, const u16* __restrict__ wmat, char* __restrict__ ws,
                                                   u16* __restrict__ out) {
  constexpr int NKS = MODE == 0 ? 32 : 112, NIT = NKS / 2;
  __shared__ __attribute__((aligned(1024))) char smem[MODE == 0 ? K2_LDS : K2_TAB];
  const unsigned L0 = (unsigned)(size_t)(LAS char*)smem;
  const int tid = threadIdx.x, lane = tid & 63;
  const int wid = __builtin_amdgcn_readfirstlane(tid >> 6);
  const int g = wid & 3, ga = wid >> 2, fr = lane & 15, fq = lane >> 4;
  const int xcd = blockIdx.x & 7, idx = blockIdx.x >> 3;
  const int pm = (xcd >> 1) * 4 + (idx >> 3), pn = (xcd & 1) * 8 + (idx & 7);
  const int tok = pm * 128 + 16 * (2 * g + ga) + fr;
  STAMP_BEG(1 + 2 * MODE);

  u32x2 rx[4]; u32x4 rh[2]; float rmu;
  if (MODE == 0) {
    const u16* p = x + (size_t)tok * D_ + pn * 64 + 4 * fq;
    asm volatile("global_load_dwordx2 %0, %1, off" : "=v"(rx[0]) : "v"(p));
    asm volatile("global_load_dwordx2 %0, %1, off offset:32" : "=v"(rx[1]) : "v"(p));
    asm volatile("global_load_dwordx2 %0, %1, off offset:64" : "=v"(rx[2]) : "v"(p));
    asm volatile("global_load_dwordx2 %0, %1, off offset:96" : "=v"(rx[3]) : "v"(p));
    const float* q = (const float*)(ws + WS_MUX) + tok;
    asm volatile("global_load_dword %0, %1, off" : "=v"(rmu) : "v"(q));
  } else {
    const char* p = ws + WS_HC + (size_t)(2 * pn) * 131072 + (size_t)(pm * 8 + 2 * g + ga) * 1024 + lane * 16;
    const char* p1 = p + 131072;
    asm volatile("global_load_dwordx4 %0, %1, off" : "=v"(rh[0]) : "v"(p));
    asm volatile("global_load_dwordx4 %0, %1, off" : "=v"(rh[1]) : "v"(p1));
    const float* q = (const float*)(ws + WS_MU) + tok;
    asm volatile("global_load_dword %0, %1, off" : "=v"(rmu) : "v"(q));
  }

  u32x4 srdA = make_srd(ws + (MODE == 0 ? WS_O : WS_ACT), NKS * 131072);
  u32x4 srdB = make_srd(wmat, NKS * 32 * 2048);
  const unsigned voffA = lane * 16 + g * 2048 + pm * 8192;
  const unsigned voffB = (unsigned)(lane >> 3) * 2048 + pn * 128 + (unsigned)((lane & 7) ^ (((lane >> 4) & 3) << 1)) * 16;
  if (MODE == 0) {
    const u32x4 srdT = make_srd(ws + WS_WTAB, 6144);
    dma(L0 + K2_TAB + wid * 1024, lane * 16, srdT, wid * 1024);
  }
  int kP = 0, kk = ga;
  const int rc0 = 8 * g, rc1 = 56 * (g & 1) + 8 * (g >> 1) - 416, rc2 = 56 * g + 48;
  auto rowbase = [&](int it) -> unsigned {
    if (MODE == 0) return (2 * it + ga) * 32 + 8 * g;
    const int a = 56 * kk + rc0, b = 112 * kk + rc1;
    int r = kk < 4 ? a : b;
    r = kk == 6 ? rc2 : r;
    return kP + r;
  };
  auto adv = [&]() { const int p2 = kP + 224, k5 = kk - 5, k2 = kk + 2; kP = kk >= 5 ? p2 : kP; kk = kk >= 5 ? k5 : k2; };
  const unsigned ldA = L0 + ga * 8192 + g * 2048, ldB = L0 + 16384 + ga * 4096 + g * 1024;
#pragma unroll
  for (int it = 0; it < K2_NS; ++it) {
    dma(ldA + it * K2_SLOT, voffA, srdA, (2 * it + ga) * 131072);
    dma(ldA + it * K2_SLOT + 1024, voffA, srdA, (2 * it + ga) * 131072 + 1024);
    dma(ldB + it * K2_SLOT, voffB, srdB, rowbase(it) * 2048);
    adv();
  }

  const unsigned ra = ldA + lane * 16;
  unsigned rb[4];
  {
    const int q = fr >> 2, p = lane & 3, sw = (2 * fq + (q >> 1)) & 3;
#pragma unroll
    for (int n = 0; n < 4; ++n) rb[n] = L0 + 16384 + ga * 4096 + (unsigned)(4 * fq + q) * 128 + (unsigned)(n ^ sw) * 32 + 8 * p;
  }
  const unsigned rw = L0 + K2_TAB + ga * 192 + fq * 48 + (fr < 2 ? fr : 2) * 16;

  f32x4 acc[2][4], accW[2];
#pragma unroll
  for (int m = 0; m < 2; ++m) {
    accW[m] = (f32x4){0, 0, 0, 0};
#pragma unroll
    for (int n = 0; n < 4; ++n) acc[m][n] = (f32x4){0, 0, 0, 0};
  }
  K2Frag F0, F1;
  auto rd = [&](K2Frag& F, int i, unsigned so, int it) {
    if (i < 2) F.a[i] = lds128(ra + so + i * 1024);
    else if (i < 10) { const int e = i - 2; F.b[e >> 1][e & 1] = ldstr(rb[e >> 1] + so + (e & 1) * 2048); }
    else if (MODE == 0) F.w = lds128(rw + it * 384);
  };
  auto step = [&](K2Frag& F, K2Frag& N, int it, unsigned so, unsigned so1) {
    if (it + K2_NS == NIT) { srdA.z = 0; srdB.z = 0; }
    const int i6 = it + K2_NS;
    const unsigned rbs = rowbase(i6) * 2048;
    adv();
#pragma unroll
    for (int i = 0; i < 8; ++i) {
      const int n = i >> 1, m = i & 1;
      acc[m][n] = mfma(cat(F.b[n][0], F.b[n][1]), F.a[m], acc[m][n]);
      if (i == 0) dma(ldA + so, voffA, srdA, (2 * i6 + ga) * 131072);
      if (i == 1) dma(ldA + so + 1024, voffA, srdA, (2 * i6 + ga) * 131072 + 1024);
      if (i == 2) dma(ldB + so, voffB, srdB, rbs);
      if (i < 5) { rd(N, 2 * i, so1, it + 1); rd(N, 2 * i + 1, so1, it + 1); }
      if (i == 5) rd(N, 10, so1, it + 1);
      SB();
    }
    if (MODE == 0) { accW[0] = mfma(F.w, F.a[0], accW[0]); accW[1] = mfma(F.w, F.a[1], accW[1]); }
    WAIT_VL(12); BARRIER();
  };

  WAIT_VL(15); BARRIER();
#pragma unroll
  for (int i = 0; i < 11; ++i) rd(F0, i, 0, 0);
  WAIT_VL(12); BARRIER();
  STAMP_L0();
  unsigned so = 0;
  for (int it = 0; it < NIT; it += 2) {
    const unsigned so1 = so + K2_SLOT, so2 = so1 == (K2_NS - 1) * K2_SLOT ? 0 : so1 + K2_SLOT;
    step(F0, F1, it, so, so1);
    step(F1, F0, it + 1, so1, so2);
    so = so2;
  }
  WAIT_VL(0); BARRIER();
  STAMP_L1(1 + 2 * MODE);

  {
    const unsigned wb = L0 + wid * 5120 + lane * 16, pb = L0 + (wid ^ 4) * 5120 + lane * 16;
#pragma unroll
    for (int n = 0; n < 4; ++n) *(LAS f32x4*)(wb + n * 1024) = ga ? acc[0][n] : acc[1][n];
    if (MODE == 0) *(LAS f32x4*)(wb + 4096) = ga ? accW[0] : accW[1];
    WAIT_L0(); BARRIER();
    f32x4 y[4];
#pragma unroll
    for (int n = 0; n < 4; ++n) y[n] = (ga ? acc[1][n] : acc[0][n]) + lds128f(pb + n * 1024);
    if (MODE == 0) {
      asm volatile("" : "+v"(rx[0]), "+v"(rx[1]), "+v"(rx[2]), "+v"(rx[3]), "+v"(rmu));
      const f32x4 wv = (ga ? accW[1] : accW[0]) + lds128f(pb + 4096);
      const float mu = rmu + red_add(wv[0] + wv[1]);
      unsigned hp[4][2]; float ss = 0.f;
#pragma unroll
      for (int n = 0; n < 4; ++n) {
        hp[n][0] = pk(y[n][0] + bflo(rx[n].x) - mu, y[n][1] + bfhi(rx[n].x) - mu);
        hp[n][1] = pk(y[n][2] + bflo(rx[n].y) - mu, y[n][3] + bfhi(rx[n].y) - mu);
        ss = dot2(hp[n][0], hp[n][0], ss); ss = dot2(hp[n][1], hp[n][1], ss);
      }
      ss = red_add(ss);
      char* hb = ws + WS_HC + (size_t)(2 * pn) * 131072 + (size_t)(pm * 8 + 2 * g + ga) * 1024 + lane * 16;
      *(u32x4*)(hb) = (u32x4){hp[0][0], hp[0][1], hp[1][0], hp[1][1]};
      *(u32x4*)(hb + 131072) = (u32x4){hp[2][0], hp[2][1], hp[3][0], hp[3][1]};
      if (fq == 0) {
        ((float*)(ws + WS_HC + (size_t)32 * 131072))[tok * 16 + pn] = ss;
        if (pn == 0) ((float*)(ws + WS_MU))[tok] = mu;
      }
    } else {
      asm volatile("" : "+v"(rh[0]), "+v"(rh[1]), "+v"(rmu));
      u16* op = out + (size_t)tok * D_ + pn * 64 + (fq & 1) * 16 + (fq >> 1) * 8;
#pragma unroll
      for (int c = 0; c < 2; ++c) {
        const u32x4 h = rh[c];
        unsigned a0 = pk(y[2 * c][0] + (bflo(h.x) + rmu), y[2 * c][1] + (bfhi(h.x) + rmu));
        unsigned a1 = pk(y[2 * c][2] + (bflo(h.y) + rmu), y[2 * c][3] + (bfhi(h.y) + rmu));
        unsigned b0 = pk(y[2 * c + 1][0] + (bflo(h.z) + rmu), y[2 * c + 1][1] + (bfhi(h.z) + rmu));
        unsigned b1 = pk(y[2 * c + 1][2] + (bflo(h.w) + rmu), y[2 * c + 1][3] + (bfhi(h.w) + rmu));
        swap16u(a0, b0); swap16u(a1, b1);
        *(u32x4*)(op + c * 32) = (u32x4){a0, a1, b0, b1};
      }
    }
  }
  STAMP_END(1 + 2 * MODE);
}

constexpr int K3_G = 16384, K3_U = K3_G + 7168 + 16, K3_SLOT = 30752, K3_NS = 5, K3_SCR = K3_SLOT * K3_NS, K3_LDS = K3_SCR + 1024;
struct K3Frag { u32x4 a[4]; u32x2 b[7][2]; };

DI float geglu(float gt, float up) {
  const float c1 = -2.0f * 0.7978845608028654f * 1.4426950408889634f, c2 = c1 * 0.044715f;
  const float e = __builtin_amdgcn_exp2f(gt * (c1 + c2 * gt * gt));
  return gt * up * __builtin_amdgcn_rcpf(1.0f + e);
}

__global__ __launch_bounds__(512, 2) void k3_gateup(const u16* __restrict__ wg, const u16* __restrict__ wu, char* __restrict__ ws) {
  __shared__ __attribute__((aligned(1024))) char smem[K3_LDS];
  const unsigned L0 = (unsigned)(size_t)(LAS char*)smem;
  const int tid = threadIdx.x, lane = tid & 63;
  const int wid = __builtin_amdgcn_readfirstlane(tid >> 6);
  const int wm = wid & 3, wn = wid >> 2, fr = lane & 15, fq = lane >> 4;
  const int xcd = blockIdx.x & 7, idx = blockIdx.x >> 3;
  const int pm = (xcd >> 2) * 4 + (idx >> 3), pn = (xcd & 3) * 8 + (idx & 7);

  STAMP_BEG(2);
  u32x4 srdA = make_srd(ws + WS_HC, 33 * 131072);
  const unsigned wbytes = wid < 7 ? D_ * F_ * 2 : 0;
  u32x4 srdG = make_srd(wg, wbytes), srdU = make_srd(wu, wbytes);
  const unsigned voffA = lane * 16 + wid * 2048 + pm * 16384;
  unsigned voffB;
  { const int L = (wid < 7 ? wid : 0) * 64 + lane; voffB = (unsigned)(L / 14) * 7168 + pn * 224 + (unsigned)(L % 14) * 16; }
  const unsigned ldA = L0 + wid * 2048;
  const unsigned ldG = wid < 7 ? L0 + K3_G + wid * 1024 : L0 + K3_SCR, ldU = wid < 7 ? L0 + K3_U + wid * 1024 : L0 + K3_SCR;
  const unsigned smask = wid < 7 ? 0xffffffffu : 0u;
#pragma unroll
  for (int j = 0; j < K3_NS; ++j) {
    dma(ldA + j * K3_SLOT, voffA, srdA, j * 131072);
    dma(ldA + j * K3_SLOT + 1024, voffA, srdA, j * 131072 + 1024);
    dma(ldG + ((j * K3_SLOT) & smask), voffB, srdG, j * (32 * 7168));
    dma(ldU + ((j * K3_SLOT) & smask), voffB, srdU, j * (32 * 7168));
  }
  const unsigned ra = L0 + wm * 4096 + lane * 16;
  const unsigned rb = L0 + K3_G + (unsigned)(4 * fq + (fr >> 2)) * 224 + 112 * wn + (lane & 1) * 8 + ((lane >> 1) & 1) * (K3_U - K3_G);

  f32x4 acc[4][7];
#pragma unroll
  for (int m = 0; m < 4; ++m)
#pragma unroll
    for (int n = 0; n < 7; ++n) acc[m][n] = (f32x4){0, 0, 0, 0};
  K3Frag F0, F1;
  auto rd = [&](K3Frag& F, int i, unsigned so) {
    if (i < 4) F.a[i] = lds128(ra + so + i * 1024);
    else { const int e = i - 4; F.b[e >> 1][e & 1] = ldstr(rb + so + (e >> 1) * 16 + (e & 1) * 3584); }
  };
  auto step = [&](K3Frag& F, K3Frag& N, int j, unsigned so, unsigned so1) {
    const int j5 = j + K3_NS;
    if (j5 == 32) { srdG.z = 0; srdU.z = 0; }
    if (j5 == 33) srdA.z = 0;
#pragma unroll
    for (int i = 0; i < 28; ++i) {
      const int n = i >> 2, m = i & 3;
      acc[m][n] = mfma(cat(F.b[n][0], F.b[n][1]), F.a[m], acc[m][n]);
      if (i < 18) rd(N, i, so1);
      if (i == 18) dma(ldA + so, voffA, srdA, j5 * 131072);
      if (i == 20) dma(ldA + so + 1024, voffA, srdA, j5 * 131072 + 1024);
      if (i == 22) dma(ldG + (so & smask), voffB, srdG, j5 * (32 * 7168));
      if (i == 24) dma(ldU + (so & smask), voffB, srdU, j5 * (32 * 7168));
      SB();
    }
    WAIT_VL(12); BARRIER();
  };

  WAIT_VL(16); BARRIER();
#pragma unroll
  for (int i = 0; i < 18; ++i) rd(F0, i, 0);
  WAIT_VL(12); BARRIER();
  STAMP_L0();
  unsigned so = 0;
  for (int j = 0; j < 32; j += 2) {
    const unsigned so1 = so == (K3_NS - 1) * K3_SLOT ? 0 : so + K3_SLOT;
    const unsigned so2 = so1 == (K3_NS - 1) * K3_SLOT ? 0 : so1 + K3_SLOT;
    step(F0, F1, j, so, so1);
    step(F1, F0, j + 1, so1, so2);
    so = so2;
  }
  WAIT_VL(0); BARRIER();
  STAMP_L1(2);

#pragma unroll
  for (int m = 0; m < 4; ++m) {
    const f32x4 p = lds128f(L0 + 2 * K3_SLOT + (unsigned)(64 * wm + 16 * m + fr) * 64 + fq * 16);
    const float rstd = __builtin_amdgcn_rsqf(red_add((p[0] + p[1]) + (p[2] + p[3])) * (1.0f / D_) + LN_EPS);
#pragma unroll
    for (int n = 0; n < 7; ++n) acc[m][n] *= rstd;
  }
  const int P = pn >> 1, w = 2 * (pn & 1) + wn, mt0 = pm * 16 + 4 * wm;
  char* ab = ws + WS_ACT + (size_t)(7 * P) * 131072 + (size_t)mt0 * 1024;
  unsigned xb[4][2];
#pragma unroll
  for (int m = 0; m < 4; ++m) {
    unsigned o[3][2];
#pragma unroll
    for (int c = 0; c < 3; ++c) {
      float v[4];
#pragma unroll
      for (int r = 0; r < 4; ++r) {
        float a = acc[m][2 * c][r], b = acc[m][2 * c + 1][r];
        swap32f(a, b);
        v[r] = geglu(a, b);
      }
      o[c][0] = pk(v[0], v[1]); o[c][1] = pk(v[2], v[3]);
    }
    *(u32x4*)(ab + (size_t)w * 131072 + m * 1024 + lane * 16) = (u32x4){o[0][0], o[0][1], o[1][0], o[1][1]};
    xb[m][0] = o[2][0]; xb[m][1] = o[2][1];
  }
  const int hf = lane >> 5, fq1 = fq & 1;
#pragma unroll
  for (int mp = 0; mp < 2; ++mp) {
    swap32u(xb[2 * mp][0], xb[2 * mp + 1][0]); swap32u(xb[2 * mp][1], xb[2 * mp + 1][1]);
    const unsigned slot = (unsigned)((2 * (w & 1) + fq1) * 16 + fr) * 16 + (2 * mp + hf) * 1024;
    *(u32x4*)(ab + (size_t)(4 + (w >> 1)) * 131072 + slot) = (u32x4){xb[2 * mp][0], xb[2 * mp][1], xb[2 * mp + 1][0], xb[2 * mp + 1][1]};
    float v[4];
#pragma unroll
    for (int r = 0; r < 4; ++r) {
      float a = acc[2 * mp][6][r], b = acc[2 * mp + 1][6][r];
      swap32f(a, b);
      v[r] = geglu(a, b);
    }
    *(u32x2*)(ab + (size_t)6 * 131072 + slot + (w >> 1) * 8) = (u32x2){pk(v[0], v[1]), pk(v[2], v[3])};
  }
  STAMP_END(2);
}

extern "C" void kernel_launch(void* const* d_in, const int* in_sizes, int n_in, void* d_out, int out_size, void* d_ws, size_t ws_size,
                              hipStream_t stream) {
  const u16* x = (const u16*)d_in[0];
  const u16* wqkv = (const u16*)d_in[1];
  const u16* wo = (const u16*)d_in[2];
  const u16* wg = (const u16*)d_in[4];
  const u16* wu = (const u16*)d_in[5];
  const u16* wd = (const u16*)d_in[6];
  char* ws = (char*)d_ws;
  u16* out = (u16*)d_out;
  k1_qkv_attn<<<256, 512, 0, stream>>>(x, wqkv, wo, ws);
  k24_proj<0><<<256, 512, 0, stream>>>(x, wo, ws, out);
  k3_gateup<<<256, 512, 0, stream>>>(wg, wu, ws);
  k24_proj<1><<<256, 512, 0, stream>>>(x, wd, ws, out);
}
```

```cpp
#include <hip/hip_runtime.h>
#include <hip/hip_bf16.h>
#include <stdint.h>

#pragma clang diagnostic ignored "-Winline-asm"
#pragma clang diagnostic ignored "-Wint-to-pointer-cast"

typedef unsigned short u16;
typedef __bf16 bf16x8 __attribute__((ext_vector_type(8)));
typedef __bf16 bf16x2 __attribute__((ext_vector_type(2)));
typedef short s16x4 __attribute__((ext_vector_type(4)));
typedef float f32x4 __attribute__((ext_vector_type(4)));
typedef float f32x2 __attribute__((ext_vector_type(2)));
typedef unsigned u32x4 __attribute__((ext_vector_type(4)));
typedef unsigned u32x2 __attribute__((ext_vector_type(2)));
#define LAS __attribute__((address_space(3)))
#define DI __device__ __forceinline__

constexpr int S_ = 2048, D_ = 1024, F_ = 3584;
constexpr float LN_EPS = 1e-5f;

constexpr size_t WS_O = 0;
constexpr size_t WS_HC = WS_O + (size_t)32 * 131072;
constexpr size_t WS_ACT = WS_HC + (size_t)33 * 131072;
constexpr size_t WS_MUX = WS_ACT + (size_t)112 * 131072;
constexpr size_t WS_MU = WS_MUX + 8192;
constexpr size_t WS_WTAB = WS_MU + 8192;
constexpr size_t WS_XN = WS_WTAB + 8192;

DI u32x4 make_srd(const void* p, unsigned bytes) {
  uint64_t a = (uint64_t)p;
  u32x4 r; r.x = (unsigned)a; r.y = (unsigned)(a >> 32) & 0xffffu; r.z = bytes; r.w = 0x00020000u;
  return r;
}
DI void dma(unsigned lds, unsigned voff, u32x4 srd, unsigned soff) {
  asm volatile("s_mov_b32 m0, %0\n\ts_nop 0\n\tbuffer_load_dwordx4 %1, %2, %3 offen lds" ::"s"(lds), "v"(voff), "s"(srd), "s"(soff) : "m0");
}
DI void dma_nt(unsigned lds, unsigned voff, u32x4 srd, unsigned soff) {
  asm volatile("s_mov_b32 m0, %0\n\ts_nop 0\n\tbuffer_load_dwordx4 %1, %2, %3 offen nt lds" ::"s"(lds), "v"(voff), "s"(srd), "s"(soff) : "m0");
}
#define dma1 dma
DI void touch_g(unsigned lds, unsigned voff, uint64_t base) {
  asm volatile("s_mov_b32 m0, %0\n\ts_nop 0\n\tglobal_load_lds_dword %1, %2" ::"s"(lds), "v"(voff), "s"(base) : "m0");
}
DI void touch_b(unsigned lds, unsigned voff, u32x4 srd, unsigned soff) {
  asm volatile("s_mov_b32 m0, %0\n\ts_nop 0\n\tbuffer_load_dword %1, %2, %3 offen lds" ::"s"(lds), "v"(voff), "s"(srd), "s"(soff) : "m0");
}
DI void touch_v(unsigned lds, uint64_t addr) {
  asm volatile("s_mov_b32 m0, %0\n\ts_nop 0\n\tglobal_load_lds_dword %1, off" ::"s"(lds), "v"(addr) : "m0");
}
#define CODE_ADDR(var, sym) uint64_t var; { int d_; asm volatile("s_getpc_b64 %0\n\ts_mov_b32 %1, " #sym "@rel32@lo+4" : "=s"(var), "=s"(d_)); var += (int64_t)d_; }
#define VADD(v, s) asm volatile("v_add_u32 %0, %1, %0" : "+v"(v) : "s"(s))
#define SB() __builtin_amdgcn_sched_barrier(0)
#define MEMFENCE() asm volatile("" ::: "memory")
#define WAIT_VL(V) do { SB(); MEMFENCE(); __builtin_amdgcn_s_waitcnt(((V) & 15) | (((V) >> 4) << 14) | 0x70); MEMFENCE(); SB(); } while (0)
#define WAIT_V(V) do { SB(); MEMFENCE(); __builtin_amdgcn_s_waitcnt(((V) & 15) | (((V) >> 4) << 14) | 0xF70); MEMFENCE(); SB(); } while (0)
#define WAIT_L0() do { SB(); MEMFENCE(); __builtin_amdgcn_s_waitcnt(0xC07F); MEMFENCE(); SB(); } while (0)
#define BARRIER() do { SB(); MEMFENCE(); __builtin_amdgcn_s_barrier(); MEMFENCE(); SB(); } while (0)
#define BW_DECL()
#define BW_OUT(kern)

DI u32x4 lds128(unsigned a) { return *(LAS u32x4*)(a); }
DI f32x4 lds128f(unsigned a) { return *(LAS f32x4*)(a); }
DI u32x2 ldstr(unsigned a) { return __builtin_bit_cast(u32x2, __builtin_amdgcn_ds_read_tr16_b64_v4i16((LAS s16x4*)(a))); }
DI f32x4 mfma(u32x4 a, u32x4 b, f32x4 c) {
  return __builtin_amdgcn_mfma_f32_16x16x32_bf16(__builtin_bit_cast(bf16x8, a), __builtin_bit_cast(bf16x8, b), c, 0, 0, 0);
}
DI u32x4 cat(u32x2 lo, u32x2 hi) { u32x4 r; r.x = lo.x; r.y = lo.y; r.z = hi.x; r.w = hi.y; return r; }
DI unsigned pk(float a, float b) { f32x2 v = {a, b}; return __builtin_bit_cast(unsigned, __builtin_convertvector(v, bf16x2)); }
DI float dot2(unsigned a, unsigned b, float c) {
  return __builtin_amdgcn_fdot2_f32_bf16(__builtin_bit_cast(bf16x2, a), __builtin_bit_cast(bf16x2, b), c, false);
}
DI float bflo(unsigned u) { return __uint_as_float(u << 16); }
DI float bfhi(unsigned u) { return __uint_as_float(u & 0xffff0000u); }
DI float red_add(float v) {
  auto a = __builtin_amdgcn_permlane32_swap(__float_as_uint(v), __float_as_uint(v), false, false);
  v = __uint_as_float(a[0]) + __uint_as_float(a[1]);
  auto b = __builtin_amdgcn_permlane16_swap(__float_as_uint(v), __float_as_uint(v), false, false);
  return __uint_as_float(b[0]) + __uint_as_float(b[1]);
}
DI float red_max(float v) {
  auto a = __builtin_amdgcn_permlane32_swap(__float_as_uint(v), __float_as_uint(v), false, false);
  v = fmaxf(__uint_as_float(a[0]), __uint_as_float(a[1]));
  auto b = __builtin_amdgcn_permlane16_swap(__float_as_uint(v), __float_as_uint(v), false, false);
  return fmaxf(__uint_as_float(b[0]), __uint_as_float(b[1]));
}
DI void swap32f(float& a, float& b) {
  auto r = __builtin_amdgcn_permlane32_swap(__float_as_uint(a), __float_as_uint(b), false, false);
  a = __uint_as_float(r[0]); b = __uint_as_float(r[1]);
}
DI void swap32u(unsigned& a, unsigned& b) { auto r = __builtin_amdgcn_permlane32_swap(a, b, false, false); a = r[0]; b = r[1]; }
DI void swap16u(unsigned& a, unsigned& b) { auto r = __builtin_amdgcn_permlane16_swap(a, b, false, false); a = r[0]; b = r[1]; }
constexpr unsigned ONES2 = 0x3F803F80u;
#define STMODE 3
DI void st16(void* p, u32x4 v) {
  asm volatile("global_store_dwordx4 %0, %1, off sc1\n\ts_nop 1" :: "v"(p), "v"(v) : "memory");
}
DI void st8(void* p, u32x2 v) {
  asm volatile("global_store_dwordx2 %0, %1, off sc1" :: "v"(p), "v"(v) : "memory");
}
#define STAMP_AT(kern, i)
#define STAMP_RT(kern, i)
#define STAMP_L0()
#define STAMP_L1(kern)
#define STAMP_END(kern)
#define STAMP_BEG(kern)

extern "C" __global__ void k1_qkv_attn(const u16*, const u16*, const u16*, char*);
extern "C" __global__ void k2_wo(const u16*, const u16*, char*);
extern "C" __global__ void k3_gateup(const u16*, const u16*, const u16*, char*);
extern "C" __global__ void k4_down(const u16*, char*, u16*);

extern "C" __global__ __launch_bounds__(256) void k0_ln(const u16* __restrict__ x, const u16* __restrict__ wqkv, const u16* __restrict__ wo,
                                                        char* __restrict__ ws) {
  __shared__ __attribute__((aligned(256))) char smem[512];
  const unsigned L0 = (unsigned)(size_t)(LAS char*)smem;
  const int tid = threadIdx.x, lane = tid & 63;
  const int wid = __builtin_amdgcn_readfirstlane(tid >> 6);
  const int t = lane & 7, ci = lane >> 3, tok = blockIdx.x * 8 + t;
  u32x4 v[4], wr[2];
  {
    const u16* p = x + (size_t)tok * D_ + wid * 256 + ci * 8;
#pragma unroll
    for (int i = 0; i < 4; ++i) asm volatile("global_load_dwordx4 %0, %1, off offset:%2" : "=v"(v[i]) : "v"(p), "n"(i * 128));
    const u16* q = wo + (size_t)(blockIdx.x * 4 + wid) * D_ + lane * 8;
    asm volatile("global_load_dwordx4 %0, %1, off" : "=v"(wr[0]) : "v"(q));
    asm volatile("global_load_dwordx4 %0, %1, off offset:1024" : "=v"(wr[1]) : "v"(q));
    CODE_ADDR(nextk, k1_qkv_attn);
    touch_g(L0 + 256, lane * 128, wid ? (uint64_t)wqkv + blockIdx.x * 24576 + (wid - 1) * 8192 : nextk);
  }
  WAIT_V(3);
  float f[32];
#pragma unroll
  for (int i = 0; i < 4; ++i) {
    asm volatile("" : "+v"(v[i]));
#pragma unroll
    for (int e = 0; e < 4; ++e) { f[8 * i + 2 * e] = bflo(v[i][e]); f[8 * i + 2 * e + 1] = bfhi(v[i][e]); }
  }
  auto block_sum = [&](float s, int pass) {
    s += __shfl_xor(s, 8);
    s = red_add(s);
    if (lane < 8) *(LAS float*)(L0 + pass * 128 + t * 16 + wid * 4) = s;
    WAIT_L0(); __builtin_amdgcn_s_barrier(); MEMFENCE();
    const f32x4 p = lds128f(L0 + pass * 128 + t * 16);
    return (p[0] + p[1]) + (p[2] + p[3]);
  };
  float s = 0.f;
#pragma unroll
  for (int i = 0; i < 32; i += 4) s += (f[i] + f[i + 1]) + (f[i + 2] + f[i + 3]);
  const float mean = block_sum(s, 0) * (1.0f / D_);
  s = 0.f;
#pragma unroll
  for (int i = 0; i < 32; ++i) { f[i] -= mean; s += f[i] * f[i]; }
  const float rstd = __builtin_amdgcn_rsqf(block_sum(s, 1) * (1.0f / D_) + LN_EPS);
  char* o = ws + WS_XN + (size_t)(8 * wid + (ci >> 2)) * 131072 + (size_t)(tok >> 4) * 1024 + (ci & 3) * 256 + (tok & 15) * 16;
#pragma unroll
  for (int i = 0; i < 4; ++i)
    st16(o + (size_t)i * 262144, (u32x4){pk(f[8 * i] * rstd, f[8 * i + 1] * rstd), pk(f[8 * i + 2] * rstd, f[8 * i + 3] * rstd),
                                         pk(f[8 * i + 4] * rstd, f[8 * i + 5] * rstd), pk(f[8 * i + 6] * rstd, f[8 * i + 7] * rstd)});
  if (tid < 8) ((float*)(ws + WS_MUX))[tok] = mean;
  WAIT_V(5);
  asm volatile("" : "+v"(wr[0]), "+v"(wr[1]));
  s = 0.f;
#pragma unroll
  for (int e = 0; e < 4; ++e) { s = dot2(wr[0][e], ONES2, s); s = dot2(wr[1][e], ONES2, s); }
#pragma unroll
  for (int q = 1; q < 16; q <<= 1) s += __shfl_xor(s, q);
  s = red_add(s) * (1.0f / D_);
  const unsigned hi = pk(s, 0.f) & 0xffffu;
  const unsigned lo = pk(s - bflo(hi), 0.f) & 0xffffu;
  const int k = blockIdx.x * 4 + wid, dd = k & 31;
  u16* tb = (u16*)(ws + WS_WTAB) + (k >> 5) * 96 + ((dd >> 2) & 3) * 24 + 4 * (dd >> 4) + (dd & 3);
  if (lane < 3) tb[lane * 8] = (u16)(lane == 0 ? hi : lane == 1 ? lo : 0u);
}

constexpr int K1_STAGE = 43008, K1_B = 18432, K1_SCR = 3 * K1_STAGE, K1_LDS = K1_SCR + 256;
struct K1Frag { u32x4 a[2]; u32x4 h; u32x2 b[7][2]; };

extern "C" __global__ __launch_bounds__(768, 3) void k1_qkv_attn(const u16* __restrict__ wqkv, const u16* __restrict__ wg, const u16* __restrict__ wu,
                                                                 char* __restrict__ ws) {
  __shared__ __attribute__((aligned(1024))) char smem[K1_LDS];
  const unsigned L0 = (unsigned)(size_t)(LAS char*)smem;
  const int tid = threadIdx.x, lane = tid & 63;
  const int wid = __builtin_amdgcn_readfirstlane(tid >> 6);
  BW_DECL();
  const int xcd = blockIdx.x & 7, idx = blockIdx.x >> 3;
  const int pm = (xcd >> 1) * 4 + (idx >> 3), pn = (xcd & 1) * 8 + (idx & 7);

  if (wid >= 8) {
    const int j = wid - 8, l3 = lane >> 3, l7 = lane & 7;
    const u32x4 srdX = make_srd(ws + WS_XN, 32 * 131072);
    const u32x4 srdW = make_srd(wqkv, D_ * 3 * D_ * 2);
    const unsigned voffA = lane * 16;
    unsigned voffB[2];
#pragma unroll
    for (int i = 0; i < 2; ++i) voffB[i] = (unsigned)(16 * j + 8 * i + l3) * 6144 + pn * 128 + (unsigned)(l7 ^ ((((lane >> 4) & 1) | (i << 1)) << 1)) * 16;
    const unsigned la = L0 + (j & 1) * 9216 + 1024 + (j >> 1) * 4096, ga = (j & 1) * 131072 + pm * 8192 + (j >> 1) * 4096;
    const unsigned gh = j * 131072 + (pm ? pm * 8192 - 1024 : 0);
    unsigned so = 0;
#pragma nounroll
    for (int kt = 0; kt < 16; ++kt) {
      if (j < 2) dma(L0 + so + j * 9216, voffA, srdX, kt * 262144 + gh);
#pragma unroll
      for (int i = 0; i < 4; ++i) dma(la + so + i * 1024, voffA, srdX, kt * 262144 + ga + i * 1024);
#pragma unroll
      for (int i = 0; i < 6; ++i) dma(L0 + so + K1_B + (i >> 1) * 8192 + j * 2048 + (i & 1) * 1024, voffB[i & 1], srdW, kt * (64 * 6144) + (i >> 1) * 2048);
      so = so == 2 * K1_STAGE ? 0 : so + K1_STAGE;
      if (kt > 0) { if (j < 2) WAIT_V(11); else WAIT_V(10); BARRIER(); }
    }
    WAIT_V(0); BARRIER();
    {
      const u32x4 srdGU = make_srd((j & 1) ? wu : wg, D_ * F_ * 2);
      touch_b(L0 + K1_SCR, lane * 128, srdGU, ((j >> 1) * 256 + blockIdx.x) * 8192);
      CODE_ADDR(nextk, k2_wo);
      if (j == 0) touch_g(L0 + K1_SCR, lane * 128, nextk);
    }
    BARRIER();
    BARRIER();
    BW_OUT(0);
    return;
  }

  const int g = wid & 3, hh = wid >> 2, fr = lane & 15, fq = lane >> 4;
  STAMP_BEG(0);
  unsigned rh = L0 + lane * 16, ra = rh + 1024 + g * 2048, rb[3];
  {
    const int q = fr >> 2, p = lane & 3, f = ((q >> 1) & 1) | ((fq & 1) << 1);
#pragma unroll
    for (int u = 0; u < 2; ++u) rb[u] = L0 + K1_B + (unsigned)(8 * fq + q) * 128 + (unsigned)((2 * hh + u) ^ f) * 32 + 8 * p;
    rb[2] = ((g & 1) ? rb[1] : rb[0]) + 8192 + (g >> 1) * 8192;
  }
  f32x4 acc[2][6], accH = {0, 0, 0, 0};
#pragma unroll
  for (int m = 0; m < 2; ++m)
#pragma unroll
    for (int j = 0; j < 6; ++j) acc[m][j] = (f32x4){0, 0, 0, 0};
  K1Frag F0, F1;
  auto rd = [&](K1Frag& F, int i, int ks) {
    if (i < 2) F.a[i] = lds128(ra + ks * 9216 + i * 1024);
    else if (i < 14) { const int e = i - 2, j = e >> 1, t = e & 1; F.b[j][t] = ldstr(rb[j & 1] + (j >> 1) * 8192 + ks * 4096 + t * 512); }
    else if (i == 14) F.h = lds128(rh + ks * 9216);
    else F.b[6][i - 15] = ldstr(rb[2] + ks * 4096 + (i - 15) * 512);
  };
  auto mm = [&](K1Frag& F, int i) {
    if (i < 12) {
      const int j = i >> 1, m = i & 1;
      const u32x4 b = cat(F.b[j][0], F.b[j][1]);
      if (j < 4) acc[m][j] = mfma(b, F.a[m], acc[m][j]); else acc[m][j] = mfma(F.a[m], b, acc[m][j]);
    } else accH = mfma(cat(F.b[6][0], F.b[6][1]), F.h, accH);
  };

  BARRIER();
#pragma unroll
  for (int i = 0; i < 17; ++i) rd(F0, i, 0);
  WAIT_L0();
  STAMP_L0();
  unsigned sidx = 0;
  for (int kt = 0; kt < 16; ++kt) {
    const unsigned dl = sidx == 2 ? (unsigned)(-2 * K1_STAGE) : (unsigned)K1_STAGE;
    sidx = sidx == 2 ? 0 : sidx + 1;
#pragma unroll
    for (int i = 0; i < 13; ++i) {
      mm(F0, i);
      if (i < 8) { rd(F1, 2 * i, 1); rd(F1, 2 * i + 1, 1); }
      if (i == 8) { rd(F1, 16, 1); VADD(ra, dl); }
      if (i >= 9 && i < 12) VADD(rb[i - 9], dl);
      if (i == 12) VADD(rh, dl);
      SB();
    }
    WAIT_L0(); BARRIER();
#pragma unroll
    for (int i = 0; i < 13; ++i) {
      mm(F1, i);
      if (i < 8) { rd(F0, 2 * i, 0); rd(F0, 2 * i + 1, 0); }
      if (i == 8) rd(F0, 16, 0);
      SB();
    }
    WAIT_L0();
  }
  STAMP_L1(0);

  u32x4 qf[2], kf[3], vf[3];
#pragma unroll
  for (int m = 0; m < 2; ++m) {
    qf[m] = (u32x4){pk(acc[m][0][0], acc[m][0][1]), pk(acc[m][0][2], acc[m][0][3]), pk(acc[m][1][0], acc[m][1][1]), pk(acc[m][1][2], acc[m][1][3])};
    kf[m + 1] = (u32x4){pk(acc[m][2][0], acc[m][2][1]), pk(acc[m][2][2], acc[m][2][3]), pk(acc[m][3][0], acc[m][3][1]), pk(acc[m][3][2], acc[m][3][3])};
    vf[m + 1] = (u32x4){pk(acc[m][4][0], acc[m][4][1]), pk(acc[m][4][2], acc[m][4][3]), pk(acc[m][5][0], acc[m][5][1]), pk(acc[m][5][2], acc[m][5][3])};
  }
  const unsigned xb = L0 + 2 * K1_STAGE + hh * 10240;
  *(LAS u32x4*)(xb + (g + 1) * 2048 + lane * 16) = kf[2];
  *(LAS u32x4*)(xb + (g + 1) * 2048 + 1024 + lane * 16) = vf[2];
  {
    const unsigned h0 = pk(accH[0], accH[1]), h1 = pk(accH[2], accH[3]);
    if (g < 2) *(LAS u32x2*)(xb + lane * 16 + g * 8) = (u32x2){h0, h1};
    else {
      const unsigned o = xb + 1024 + (unsigned)((fr >> 2) * 16 + 4 * fq) * 16 + (g - 2) * 8 + (fr & 3) * 2;
      *(LAS u16*)(o) = (u16)h0; *(LAS u16*)(o + 16) = (u16)(h0 >> 16); *(LAS u16*)(o + 32) = (u16)h1; *(LAS u16*)(o + 48) = (u16)(h1 >> 16);
    }
  }
  WAIT_L0(); BARRIER();
  kf[0] = lds128(xb + g * 2048 + lane * 16);
  vf[0] = lds128(xb + g * 2048 + 1024 + lane * 16);
  {
    const float c = 0.17677669529663687f * 1.4426950408889634f;
    char* of = ws + WS_O + (size_t)(2 * pn + hh) * 131072 + (size_t)(pm * 8 + 2 * g) * 1024 + lane * 16;
#pragma unroll
    for (int mi = 0; mi < 2; ++mi) {
      const int m = 1 - mi;
      const f32x4 z = {0, 0, 0, 0};
      f32x4 sp = mfma(kf[m], qf[m], z), sc = mfma(kf[m + 1], qf[m], z);
      const bool pok = (m == 1) || (pm | g) != 0;
      float s[8];
#pragma unroll
      for (int r = 0; r < 4; ++r) {
        const int d = fr - 4 * fq - r;
        s[r] = (pok && ((d & 1) == 0) && d <= -2) ? sp[r] : -INFINITY;
        s[4 + r] = (((d & 1) == 0) && d >= 0 && d <= 14) ? sc[r] : -INFINITY;
      }
      float mx = fmaxf(fmaxf(fmaxf(s[0], s[1]), fmaxf(s[2], s[3])), fmaxf(fmaxf(s[4], s[5]), fmaxf(s[6], s[7])));
      mx = red_max(mx);
      const float mc = mx * c;
      float sum = 0.f;
#pragma unroll
      for (int e = 0; e < 8; ++e) { s[e] = __builtin_amdgcn_exp2f(s[e] * c - mc); sum += s[e]; }
      sum = red_add(sum);
      const float inv = __builtin_amdgcn_rcpf(sum);
      u32x4 p;
#pragma unroll
      for (int e = 0; e < 4; ++e) p[e] = pk(s[2 * e] * inv, s[2 * e + 1] * inv);
      u32x4 va, vb;
      va.x = vf[m].x; va.y = vf[m].y; va.z = vf[m + 1].x; va.w = vf[m + 1].y;
      vb.x = vf[m].z; vb.y = vf[m].w; vb.z = vf[m + 1].z; vb.w = vf[m + 1].w;
      const f32x4 oa = mfma(va, p, z), ob = mfma(vb, p, z);
      u32x4 w; w.x = pk(oa[0], oa[1]); w.y = pk(oa[2], oa[3]); w.z = pk(ob[0], ob[1]); w.w = pk(ob[2], ob[3]);
      st16(of + m * 1024, w);
    }
  }
  BW_OUT(0);
  STAMP_END(0);
}

constexpr int K2_SLOT = 24576, K2_NS = 6, K2_TAB = K2_SLOT * K2_NS, K2_SCR = K2_TAB + 8192, K2_LDS = K2_SCR + 256;
struct K2Frag { u32x4 a[2]; u32x2 b[4][2]; };

template <int MODE>
DI void proj_body(const u16* __restrict__ x, const u16* __restrict__ wmat, char* __restrict__ ws, u16* __restrict__ out, const unsigned L0,
                  const uint64_t nextk) {
  constexpr int NKS = MODE == 0 ? 32 : 112, NIT = NKS / 2;
  const int tid = threadIdx.x, lane = tid & 63;
  const int wid = __builtin_amdgcn_readfirstlane(tid >> 6);
  BW_DECL();
  const int g = wid & 3, fr = lane & 15, fq = lane >> 4;
  const int xcd = blockIdx.x & 7, idx = blockIdx.x >> 3;
#define MAP24 2
  const int pm = (xcd >> 1) * 4 + (idx >> 3), pn = (xcd & 1) + 2 * (idx & 7);

  if (wid >= 4) {
    const int ga = wid & 1;
    int kP = 0, kk = wid == 8 ? 0 : ga;
    auto rowbase = [&](int ks, int j) -> unsigned {
      if (MODE == 0) return ks * 32 + 8 * j;
      const int a = 56 * kk + 8 * j, b = 112 * kk + 56 * (j & 1) + 8 * (j >> 1) - 416, c = 56 * j + 48;
      int r = kk < 4 ? a : b;
      r = kk == 6 ? c : r;
      return kP + r;
    };
    auto adv = [&](int n) { const int p2 = kP + 224, k1 = kk + n, k2 = k1 - 7; kP = k1 >= 7 ? p2 : kP; kk = k1 >= 7 ? k2 : k1; };
    const u32x4 srdA = make_srd(ws + (MODE == 0 ? WS_O : WS_ACT), NKS * 131072);
    const u32x4 srdB = make_srd(wmat, NKS * 32 * 2048);
    const unsigned voffA = lane * 16 + pm * 8192;
    const unsigned voffB = (unsigned)(lane >> 3) * 2048 + pn * 128 + (unsigned)((lane & 7) ^ (((lane >> 4) & 3) << 1)) * 16;
    const int ml = idx & 7, jl = idx >> 3;
    unsigned so = 0;
    if (wid == 8) {
      if (MODE == 0) {
        const u32x4 srdT = make_srd(ws + WS_WTAB, 6144);
#pragma unroll
        for (int q = 0; q < 6; ++q) dma(L0 + K2_TAB + q * 1024, lane * 16, srdT, q * 1024);
      }
#pragma nounroll
      for (int it = 0; it < NIT + 2; ++it) {
        if (it < NIT) {
#pragma unroll
          for (int u = 0; u < 2; ++u) {
            dma(L0 + so + u * 8192 + ml * 1024, voffA, srdA, (2 * it + u) * 131072 + ml * 1024);
            dma(L0 + so + 16384 + u * 4096 + jl * 1024, voffB, srdB, rowbase(2 * it + u, jl) * 2048);
            adv(1);
          }
          so = so == (K2_NS - 1) * K2_SLOT ? 0 : so + K2_SLOT;
          if (it < 4) continue;
          WAIT_V(16);
        } else if (it == NIT) WAIT_V(12);
        else WAIT_V(8);
        BARRIER();
      }
      WAIT_V(4); BARRIER();
      WAIT_V(0); BARRIER();
      if (MODE == 0) { touch_g(L0 + K2_SCR, lane * 128, nextk); touch_g(L0 + K2_SCR, lane * 128, nextk + 8192); }
      BW_OUT(1 + 2 * MODE);
      return;
    }
    unsigned mo[5];
#pragma unroll
    for (int q = 0; q < 5; ++q) mo[q] = ((ml + 1 + q + (wid >= 6 ? 5 : 0)) & 7) * 1024;
#pragma nounroll
    for (int it = 0; it < NIT; ++it) {
      const unsigned la = L0 + so + ga * 8192, sa = (2 * it + ga) * 131072;
      dma(la + mo[0], voffA, srdA, sa + mo[0]);
      dma(la + mo[1], voffA, srdA, sa + mo[1]);
      if (wid < 6) {
#pragma unroll
        for (int q = 2; q < 5; ++q) dma(la + mo[q], voffA, srdA, sa + mo[q]);
      } else {
#pragma unroll
        for (int q = 1; q < 4; ++q) { const int j = (jl + q) & 3; dma(L0 + so + 16384 + ga * 4096 + j * 1024, voffB, srdB, rowbase(2 * it + ga, j) * 2048); }
        adv(2);
      }
      so = so == (K2_NS - 1) * K2_SLOT ? 0 : so + K2_SLOT;
      if (it > 0) { WAIT_V(5); BARRIER(); }
    }
    WAIT_V(0); BARRIER();
    BW_OUT(1 + 2 * MODE);
    return;
  }

  STAMP_BEG(1 + 2 * MODE);
  const int tok = pm * 128 + 32 * g + fr;
  u32x2 rx[2][4]; u32x4 rh[2][2]; float rmu[2];
#pragma unroll
  for (int m = 0; m < 2; ++m) {
    if (MODE == 0) {
      const u16* p = x + (size_t)(tok + 16 * m) * D_ + pn * 64 + 4 * fq;
      asm volatile("global_load_dwordx2 %0, %1, off" : "=v"(rx[m][0]) : "v"(p));
      asm volatile("global_load_dwordx2 %0, %1, off offset:32" : "=v"(rx[m][1]) : "v"(p));
      asm volatile("global_load_dwordx2 %0, %1, off offset:64" : "=v"(rx[m][2]) : "v"(p));
      asm volatile("global_load_dwordx2 %0, %1, off offset:96" : "=v"(rx[m][3]) : "v"(p));
    } else {
      const char* p = ws + WS_HC + (size_t)(2 * pn) * 131072 + (size_t)(pm * 8 + 2 * g + m) * 1024 + lane * 16;
      const char* p1 = p + 131072;
      asm volatile("global_load_dwordx4 %0, %1, off" : "=v"(rh[m][0]) : "v"(p));
      asm volatile("global_load_dwordx4 %0, %1, off" : "=v"(rh[m][1]) : "v"(p1));
    }
    const float* q = (const float*)(ws + (MODE == 0 ? WS_MUX : WS_MU)) + tok + 16 * m;
    asm volatile("global_load_dword %0, %1, off" : "=v"(rmu[m]) : "v"(q));
  }

  unsigned ra = L0 + g * 2048 + lane * 16;
  unsigned rb[4];
  {
    const int q = fr >> 2, p = lane & 3, sw = (2 * fq + (q >> 1)) & 3;
#pragma unroll
    for (int n = 0; n < 4; ++n) rb[n] = L0 + 16384 + (unsigned)(4 * fq + q) * 128 + (unsigned)(n ^ sw) * 32 + 8 * p;
  }
  unsigned rw = L0 + K2_TAB + fq * 48 + (fr < 2 ? fr : 2) * 16;

  f32x4 acc[2][4], accW[2];
#pragma unroll
  for (int m = 0; m < 2; ++m) {
    accW[m] = (f32x4){0, 0, 0, 0};
#pragma unroll
    for (int n = 0; n < 4; ++n) acc[m][n] = (f32x4){0, 0, 0, 0};
  }
  K2Frag F0[2], F1[2];
  constexpr int NRD = 10;
  auto rd = [&](K2Frag (&F)[2], int i) {
    const int ga = i / NRD, k = i % NRD;
    if (k < 2) F[ga].a[k] = lds128(ra + ga * 8192 + k * 1024);
    else { const int e = k - 2; F[ga].b[e >> 1][e & 1] = ldstr(rb[e >> 1] + ga * 4096 + (e & 1) * 2048); }
  };
  auto bump = [&](int i, unsigned dl) {
    if (i == 0) VADD(ra, dl);
    else if (i < 5) VADD(rb[i - 1], dl);
    else if (MODE == 0) { const unsigned c = 384; VADD(rw, c); }
  };
  auto step = [&](K2Frag (&F)[2], K2Frag (&N)[2], unsigned dl) {
    u32x4 wf[2];
    if (MODE == 0) { wf[0] = lds128(rw); wf[1] = lds128(rw + 192); }
#pragma unroll
    for (int i = 0; i < 16; ++i) {
      const int ga = i >> 3, n = (i >> 1) & 3, m = i & 1;
      acc[m][n] = mfma(cat(F[ga].b[n][0], F[ga].b[n][1]), F[ga].a[m], acc[m][n]);
      if (i < NRD) { rd(N, 2 * i); rd(N, 2 * i + 1); }
      if (i >= 13) { bump(2 * (i - 13), dl); bump(2 * (i - 13) + 1, dl); }
      SB();
    }
    if (MODE == 0) {
#pragma unroll
      for (int i = 0; i < 4; ++i) accW[i & 1] = mfma(wf[i >> 1], F[i >> 1].a[i & 1], accW[i & 1]);
      SB();
    }
  };

  BARRIER();
#pragma unroll
  for (int i = 0; i < 2 * NRD; ++i) rd(F0, i);
#pragma unroll
  for (int i = 0; i < 5; ++i) bump(i, K2_SLOT);
  WAIT_L0(); BARRIER();
  STAMP_L0();
  int sidx = 1;
  for (int it = 0; it < NIT - 2; it += 2) {
    step(F0, F1, sidx == 5 ? (unsigned)(-5 * K2_SLOT) : (unsigned)K2_SLOT);
    WAIT_L0(); BARRIER();
    step(F1, F0, K2_SLOT);
    WAIT_L0(); BARRIER();
    sidx = sidx == 5 ? 1 : sidx + 2;
  }
  step(F0, F1, 0);
  step(F1, F0, 0);
  WAIT_VL(0);
  STAMP_L1(1 + 2 * MODE);

#pragma unroll
  for (int m = 0; m < 2; ++m) {
    if (MODE == 0) {
      asm volatile("" : "+v"(rx[m][0]), "+v"(rx[m][1]), "+v"(rx[m][2]), "+v"(rx[m][3]), "+v"(rmu[m]));
      const float mu = rmu[m] + red_add(accW[m][0] + accW[m][1]);
      unsigned hp[4][2]; float ss = 0.f;
#pragma unroll
      for (int n = 0; n < 4; ++n) {
        hp[n][0] = pk(acc[m][n][0] + bflo(rx[m][n].x) - mu, acc[m][n][1] + bfhi(rx[m][n].x) - mu);
        hp[n][1] = pk(acc[m][n][2] + bflo(rx[m][n].y) - mu, acc[m][n][3] + bfhi(rx[m][n].y) - mu);
        ss = dot2(hp[n][0], hp[n][0], ss); ss = dot2(hp[n][1], hp[n][1], ss);
      }
      char* hb = ws + WS_HC + (size_t)(2 * pn) * 131072 + (size_t)(pm * 8 + 2 * g + m) * 1024 + lane * 16;
      st16(hb, (u32x4){hp[0][0], hp[0][1], hp[1][0], hp[1][1]});
      st16(hb + 131072, (u32x4){hp[2][0], hp[2][1], hp[3][0], hp[3][1]});
      ss = red_add(ss);
      if (fq == 0) {
        ((float*)(ws + WS_HC + (size_t)32 * 131072))[(tok + 16 * m) * 16 + pn] = ss;
        if (pn == 0) ((float*)(ws + WS_MU))[tok + 16 * m] = mu;
      }
    } else {
      asm volatile("" : "+v"(rh[m][0]), "+v"(rh[m][1]), "+v"(rmu[m]));
      u16* op = out + (size_t)(tok + 16 * m) * D_ + pn * 64 + (fq & 1) * 16 + (fq >> 1) * 8;
      const float mu = rmu[m];
#pragma unroll
      for (int c = 0; c < 2; ++c) {
        const u32x4 h = rh[m][c];
        const f32x4 y0 = acc[m][2 * c], y1 = acc[m][2 * c + 1];
        unsigned a0 = pk(y0[0] + (bflo(h.x) + mu), y0[1] + (bfhi(h.x) + mu));
        unsigned a1 = pk(y0[2] + (bflo(h.y) + mu), y0[3] + (bfhi(h.y) + mu));
        unsigned b0 = pk(y1[0] + (bflo(h.z) + mu), y1[1] + (bfhi(h.z) + mu));
        unsigned b1 = pk(y1[2] + (bflo(h.w) + mu), y1[3] + (bfhi(h.w) + mu));
        swap16u(a0, b0); swap16u(a1, b1);
        *(u32x4*)(op + c * 32) = (u32x4){a0, a1, b0, b1};
      }
    }
  }
  BW_OUT(1 + 2 * MODE);
  STAMP_END(1 + 2 * MODE);
}

extern "C" __global__ __launch_bounds__(576, 3) void k2_wo(const u16* __restrict__ x, const u16* __restrict__ wo, char* __restrict__ ws) {
  __shared__ __attribute__((aligned(1024))) char smem[K2_LDS];
  CODE_ADDR(nextk, k3_gateup);
  proj_body<0>(x, wo, ws, nullptr, (unsigned)(size_t)(LAS char*)smem, nextk);
}
extern "C" __global__ __launch_bounds__(576, 3) void k4_down(const u16* __restrict__ wd, char* __restrict__ ws, u16* __restrict__ out) {
  __shared__ __attribute__((aligned(1024))) char smem[K2_LDS];
  proj_body<1>(nullptr, wd, ws, out, (unsigned)(size_t)(LAS char*)smem, 0);
}

constexpr int K3_G = 16384, K3_U = K3_G + 7168 + 16, K3_SLOT = 30752, K3_NS = 5, K3_SCR = K3_SLOT * K3_NS, K3_LDS = K3_SCR + 256;
struct K3Frag { u32x4 a[4]; u32x2 b[7][2]; };

DI unsigned geglu2(f32x2 g, f32x2 u, float k1, float k2, float ir2) {
  const f32x2 z = g * (g * g * k2 + k1);
  f32x2 e; e.x = __builtin_amdgcn_exp2f(z.x); e.y = __builtin_amdgcn_exp2f(z.y);
  const f32x2 d = e * ir2 + ir2;
  f32x2 r; r.x = __builtin_amdgcn_rcpf(d.x); r.y = __builtin_amdgcn_rcpf(d.y);
  const f32x2 o = (g * u) * r;
  return pk(o.x, o.y);
}

extern "C" __global__ __launch_bounds__(512, 2) void k3_gateup(const u16* __restrict__ wg, const u16* __restrict__ wu, const u16* __restrict__ wd,
                                                               char* __restrict__ ws) {
  __shared__ __attribute__((aligned(1024))) char smem[K3_LDS];
  const unsigned L0 = (unsigned)(size_t)(LAS char*)smem;
  const int tid = threadIdx.x, lane = tid & 63;
  const int wid = __builtin_amdgcn_readfirstlane(tid >> 6);
  BW_DECL();
  const int wm = wid & 3, wn = wid >> 2, fr = lane & 15, fq = lane >> 4;
  const int xcd = blockIdx.x & 7, idx = blockIdx.x >> 3;
  const int pm = (xcd >> 2) * 4 + (idx >> 3), pn = (xcd & 3) * 8 + (idx & 7);
  STAMP_BEG(2);

  const u32x4 srdA = make_srd(ws + WS_HC, 32 * 131072);
  const u32x4 srdG = make_srd(wg, D_ * F_ * 2), srdU = make_srd(wu, D_ * F_ * 2);
  const unsigned voffA = lane * 16 + wid * 2048 + pm * 16384;
  unsigned voffB;
  const int wb = wid < 7 ? wid : 6;
  { const unsigned L = wb * 64 + lane, r = (L * 4682u) >> 16; voffB = r * 7168 + pn * 224 + (L - r * 14) * 16; }
  const unsigned ldA = L0 + wid * 2048, ldG = L0 + K3_G + wb * 1024, ldU = L0 + K3_U + wb * 1024;
  auto dA = [&](int j, unsigned so, int i) { dma(ldA + so + i * 1024, voffA, srdA, j * 131072 + i * 1024); };
  auto dG = [&](int j, unsigned so) { dma(ldG + so, voffB, srdG, j * (32 * 7168)); };
  auto dU = [&](int j, unsigned so) { dma(ldU + so, voffB, srdU, j * (32 * 7168)); };
#define K3_WAIT(slots, extra) do { WAIT_VL(4 * (slots) + (extra)); BARRIER(); } while (0)
#pragma unroll
  for (int j = 0; j < 3; ++j) { dA(j, j * K3_SLOT, 0); dA(j, j * K3_SLOT, 1); dG(j, j * K3_SLOT); dU(j, j * K3_SLOT); }
  unsigned ra = L0 + wm * 4096 + lane * 16;
  unsigned rb = L0 + K3_G + (unsigned)(4 * fq + (fr >> 2)) * 224 + 112 * wn + (lane & 3) * 8;
  unsigned rm = L0 + K3_G + (unsigned)(4 * fq + (fr >> 2)) * 224 + 112 * wn + 96 + (lane & 1) * 8 + ((lane >> 1) & 1) * (K3_U - K3_G);

  f32x4 acc[4][7];
#pragma unroll
  for (int m = 0; m < 4; ++m)
#pragma unroll
    for (int n = 0; n < 7; ++n) acc[m][n] = (f32x4){0, 0, 0, 0};
  K3Frag F0, F1;
  auto rd = [&](K3Frag& F, int i) {
    if (i < 4) F.a[i] = lds128(ra + i * 1024);
    else {
      const int e = i - 4, n = e >> 1, t = e & 1;
      F.b[n][t] = n < 6 ? ldstr(rb + (n >> 1) * 32 + (n & 1) * (K3_U - K3_G) + t * 3584) : ldstr(rm + t * 3584);
    }
  };
  auto step = [&](K3Frag& F, K3Frag& N, unsigned so1, int ND, int jd, unsigned sd, unsigned sd2, bool AONLY, bool RD) {
    const unsigned dl = so1 == (K3_NS - 1) * K3_SLOT ? (unsigned)(-(K3_NS - 1) * K3_SLOT) : (unsigned)K3_SLOT;
#pragma unroll
    for (int i = 0; i < 28; ++i) {
      const int n = i >> 2, m = i & 3;
      acc[m][n] = mfma(cat(F.b[n][0], F.b[n][1]), F.a[m], acc[m][n]);
      if (RD && i < 18) rd(N, i);
      if (RD && i == 19) VADD(ra, dl);
      if (RD && i == 21) VADD(rb, dl);
      if (RD && i == 23) VADD(rm, dl);
      const int s = ND == 2 ? 10 : 18;
      if (ND > 0) {
        if (i == s) dA(jd, sd, 0);
        if (i == s + 2) dA(jd, sd, 1);
        if (i == s + 4 && !AONLY) dG(jd, sd);
        if (i == s + 6 && !AONLY) dU(jd, sd);
      }
      if (ND > 1) {
        if (i == 18) dA(jd + 1, sd2, 0);
        if (i == 20) dA(jd + 1, sd2, 1);
        if (i == 22) dG(jd + 1, sd2);
        if (i == 24) dU(jd + 1, sd2);
      }
      SB();
    }
  };

  K3_WAIT(2, 0);
#pragma unroll
  for (int i = 0; i < 18; ++i) rd(F0, i);
  { const unsigned dl = K3_SLOT; VADD(ra, dl); VADD(rb, dl); VADD(rm, dl); }
  K3_WAIT(1, 0);
  STAMP_L0();
  step(F0, F1, K3_SLOT, 2, 3, 3 * K3_SLOT, 4 * K3_SLOT, false, true);
  K3_WAIT(2, 0);
  step(F1, F0, 2 * K3_SLOT, 2, 5, 0, K3_SLOT, false, true);
  touch_b(L0 + K3_SCR, (lane < 24 ? lane : 23) * 128 + wm * 3072, wn ? srdU : srdG, 4194304 + blockIdx.x * 12288);
  K3_WAIT(3, 0);
  CODE_ADDR(nextk, k4_down);
  uint64_t here; asm volatile("s_getpc_b64 %0" : "=s"(here));
  unsigned so = 2 * K3_SLOT;
  for (int j = 2; j < 26; j += 2) {
    const unsigned so1 = so == (K3_NS - 1) * K3_SLOT ? 0 : so + K3_SLOT;
    const unsigned so2 = so1 == (K3_NS - 1) * K3_SLOT ? 0 : so1 + K3_SLOT;
    step(F0, F1, so1, 1, j + 5, so, 0, false, true);
    K3_WAIT(3, 0);
    step(F1, F0, so2, 1, j + 6, so1, 0, false, true);
    if (j == 18 && wid < 3) touch_g(L0 + K3_SCR, lane * 128, wid == 2 ? nextk : here + wid * 8192);
    K3_WAIT(3, 0);
    so = so2;
  }
  f32x4 ssq[4];
  {
    const char* q = ws + WS_HC + (size_t)32 * 131072 + (size_t)(pm * 256 + 64 * wm + fr) * 64 + fq * 16;
#pragma unroll
    for (int m = 0; m < 4; ++m) asm volatile("global_load_dwordx4 %0, %1, off offset:%2" : "=v"(ssq[m]) : "v"(q), "n"(m * 1024));
  }
  step(F0, F1, 2 * K3_SLOT, 1, 31, 1 * K3_SLOT, 0, false, true);
  K3_WAIT(3, 4);
  {
    const u32x4 srdD = make_srd(wd, D_ * F_ * 2);
    touch_b(L0 + K3_SCR, (lane < 28 ? lane : 27) * 128 + wid * 3584, srdD, blockIdx.x * 28672);
  }
  step(F1, F0, 3 * K3_SLOT, 0, 0, 0, 0, false, true);
  WAIT_VL(1); BARRIER();
  step(F0, F1, 4 * K3_SLOT, 0, 0, 0, 0, false, true);
  step(F1, F0, 0, 0, 0, 0, 0, false, true);
  step(F0, F1, 1 * K3_SLOT, 0, 0, 0, 0, false, true);
  step(F1, F0, 0, 0, 0, 0, 0, false, false);
  STAMP_L1(2);
  float k1[4], k2[4], ir2[4];
#pragma unroll
  for (int m = 0; m < 4; ++m) {
    asm volatile("" : "+v"(ssq[m]));
    const f32x4 p = ssq[m];
    ir2[m] = red_add((p[0] + p[1]) + (p[2] + p[3])) * (1.0f / D_) + LN_EPS;
    const float r = __builtin_amdgcn_rsqf(ir2[m]);
    const float c1 = -2.0f * 0.7978845608028654f * 1.4426950408889634f, c2 = c1 * 0.044715f;
    k1[m] = c1 * r; k2[m] = c2 * r * r * r;
  }
  const int P = pn >> 1, w = 2 * (pn & 1) + wn, mt0 = pm * 16 + 4 * wm;
  char* ab = ws + WS_ACT + (size_t)(7 * P) * 131072 + (size_t)mt0 * 1024;
  unsigned xb[4][2];
#pragma unroll
  for (int m = 0; m < 4; ++m) {
    unsigned o[3][2];
#pragma unroll
    for (int c = 0; c < 3; ++c) {
      const f32x4 a = acc[m][2 * c], b = acc[m][2 * c + 1];
      o[c][0] = geglu2(a.xy, b.xy, k1[m], k2[m], ir2[m]);
      o[c][1] = geglu2(a.zw, b.zw, k1[m], k2[m], ir2[m]);
    }
    st16(ab + (size_t)w * 131072 + m * 1024 + lane * 16, (u32x4){o[0][0], o[0][1], o[1][0], o[1][1]});
    xb[m][0] = o[2][0]; xb[m][1] = o[2][1];
  }
  const int hf = lane >> 5, fq1 = fq & 1;
#pragma unroll
  for (int mp = 0; mp < 2; ++mp) {
    swap32u(xb[2 * mp][0], xb[2 * mp + 1][0]); swap32u(xb[2 * mp][1], xb[2 * mp + 1][1]);
    const unsigned slot = (unsigned)((2 * (w & 1) + fq1) * 16 + fr) * 16 + (2 * mp + hf) * 1024;
    st16(ab + (size_t)(4 + (w >> 1)) * 131072 + slot, (u32x4){xb[2 * mp][0], xb[2 * mp][1], xb[2 * mp + 1][0], xb[2 * mp + 1][1]});
    f32x4 a = acc[2 * mp][6], b = acc[2 * mp + 1][6];
#pragma unroll
    for (int r = 0; r < 4; ++r) { float u = a[r], v = b[r]; swap32f(u, v); a[r] = u; b[r] = v; }
    const float q1 = hf ? k1[2 * mp + 1] : k1[2 * mp], q2 = hf ? k2[2 * mp + 1] : k2[2 * mp], qi = hf ? ir2[2 * mp + 1] : ir2[2 * mp];
    st8(ab + (size_t)6 * 131072 + slot + (w >> 1) * 8, (u32x2){geglu2(a.xy, b.xy, q1, q2, qi), geglu2(a.zw, b.zw, q1, q2, qi)});
  }
  BW_OUT(2);
  STAMP_END(2);
}

extern "C" __global__ void kz_pad() { asm volatile(".fill 4096, 4, 0xBF800000"); }

extern "C" void kernel_launch(void* const* d_in, const int* in_sizes, int n_in, void* d_out, int out_size, void* d_ws, size_t ws_size,
                              hipStream_t stream) {
  const u16* x = (const u16*)d_in[0];
  const u16* wqkv = (const u16*)d_in[1];
  const u16* wo = (const u16*)d_in[2];
  const u16* wg = (const u16*)d_in[4];
  const u16* wu = (const u16*)d_in[5];
  const u16* wd = (const u16*)d_in[6];
  char* ws = (char*)d_ws;
  u16* out = (u16*)d_out;
  k0_ln<<<256, 256, 0, stream>>>(x, wqkv, wo, ws);
  k1_qkv_attn<<<256, 768, 0, stream>>>(wqkv, wg, wu, ws);
  k2_wo<<<256, 576, 0, stream>>>(x, wo, ws);
  k3_gateup<<<256, 512, 0, stream>>>(wg, wu, wd, ws);
  k4_down<<<256, 576, 0, stream>>>(wd, ws, out);
}
```

```cpp
#include <hip/hip_runtime.h>
#include <hip/hip_bf16.h>
#include <stdint.h>

#pragma clang diagnostic ignored "-Winline-asm"
#pragma clang diagnostic ignored "-Wint-to-pointer-cast"

typedef unsigned short u16;
typedef __bf16 bf16x8 __attribute__((ext_vector_type(8)));
typedef __bf16 bf16x2 __attribute__((ext_vector_type(2)));
typedef short s16x4 __attribute__((ext_vector_type(4)));
typedef float f32x4 __attribute__((ext_vector_type(4)));
typedef float f32x2 __attribute__((ext_vector_type(2)));
typedef unsigned u32x4 __attribute__((ext_vector_type(4)));
typedef unsigned u32x2 __attribute__((ext_vector_type(2)));
#define LAS __attribute__((address_space(3)))
#define DI __device__ __forceinline__

constexpr int S_ = 2048, D_ = 1024, F_ = 3584;
constexpr float LN_EPS = 1e-5f;

constexpr size_t WS_O = 0;
constexpr size_t WS_HC = WS_O + (size_t)32 * 131072;
constexpr size_t WS_ACT = WS_HC + (size_t)33 * 131072;
constexpr size_t WS_MUX = WS_ACT + (size_t)112 * 131072;
constexpr size_t WS_MU = WS_MUX + 8192;
constexpr size_t WS_WTAB = WS_MU + 8192;

DI u32x4 make_srd(const void* p, unsigned bytes) {
  uint64_t a = (uint64_t)p;
  u32x4 r; r.x = (unsigned)a; r.y = (unsigned)(a >> 32) & 0xffffu; r.z = bytes; r.w = 0x00020000u;
  return r;
}
DI void dma(unsigned lds, unsigned voff, u32x4 srd, unsigned soff) {
  asm volatile("s_mov_b32 m0, %0\n\ts_nop 0\n\tbuffer_load_dwordx4 %1, %2, %3 offen lds" ::"s"(lds), "v"(voff), "s"(srd), "s"(soff) : "m0");
}
DI void dma_nt(unsigned lds, unsigned voff, u32x4 srd, unsigned soff) {
  asm volatile("s_mov_b32 m0, %0\n\ts_nop 0\n\tbuffer_load_dwordx4 %1, %2, %3 offen nt lds" ::"s"(lds), "v"(voff), "s"(srd), "s"(soff) : "m0");
}
#define dma1 dma
DI void touch_g(unsigned lds, unsigned voff, uint64_t base) {
  asm volatile("s_mov_b32 m0, %0\n\ts_nop 0\n\tglobal_load_lds_dword %1, %2" ::"s"(lds), "v"(voff), "s"(base) : "m0");
}
DI void touch_b(unsigned lds, unsigned voff, u32x4 srd, unsigned soff) {
  asm volatile("s_mov_b32 m0, %0\n\ts_nop 0\n\tbuffer_load_dword %1, %2, %3 offen lds" ::"s"(lds), "v"(voff), "s"(srd), "s"(soff) : "m0");
}
DI void touch_v(unsigned lds, uint64_t addr) {
  asm volatile("s_mov_b32 m0, %0\n\ts_nop 0\n\tglobal_load_lds_dword %1, off" ::"s"(lds), "v"(addr) : "m0");
}
#define CODE_ADDR(var, sym) uint64_t var; { int d_; asm volatile("s_getpc_b64 %0\n\ts_mov_b32 %1, " #sym "@rel32@lo+4" : "=s"(var), "=s"(d_)); var += (int64_t)d_; }
#define VADD(v, s) asm volatile("v_add_u32 %0, %1, %0" : "+v"(v) : "s"(s))
#define SB() __builtin_amdgcn_sched_barrier(0)
#define MEMFENCE() asm volatile("" ::: "memory")
#define WAIT_VL(V) do { SB(); MEMFENCE(); __builtin_amdgcn_s_waitcnt(((V) & 15) | (((V) >> 4) << 14) | 0x70); MEMFENCE(); SB(); } while (0)
#define WAIT_V(V) do { SB(); MEMFENCE(); __builtin_amdgcn_s_waitcnt(((V) & 15) | (((V) >> 4) << 14) | 0xF70); MEMFENCE(); SB(); } while (0)
#define WAIT_L0() do { SB(); MEMFENCE(); __builtin_amdgcn_s_waitcnt(0xC07F); MEMFENCE(); SB(); } while (0)
#define BARRIER() do { SB(); MEMFENCE(); __builtin_amdgcn_s_barrier(); MEMFENCE(); SB(); } while (0)
#define BW_DECL()
#define BW_OUT(kern)

DI u32x4 lds128(unsigned a) { return *(LAS u32x4*)(a); }
DI f32x4 lds128f(unsigned a) { return *(LAS f32x4*)(a); }
DI u32x2 ldstr(unsigned a) { return __builtin_bit_cast(u32x2, __builtin_amdgcn_ds_read_tr16_b64_v4i16((LAS s16x4*)(a))); }
DI f32x4 mfma(u32x4 a, u32x4 b, f32x4 c) {
  return __builtin_amdgcn_mfma_f32_16x16x32_bf16(__builtin_bit_cast(bf16x8, a), __builtin_bit_cast(bf16x8, b), c, 0, 0, 0);
}
DI u32x4 cat(u32x2 lo, u32x2 hi) { u32x4 r; r.x = lo.x; r.y = lo.y; r.z = hi.x; r.w = hi.y; return r; }
DI unsigned pk(float a, float b) { f32x2 v = {a, b}; return __builtin_bit_cast(unsigned, __builtin_convertvector(v, bf16x2)); }
DI float dot2(unsigned a, unsigned b, float c) {
  return __builtin_amdgcn_fdot2_f32_bf16(__builtin_bit_cast(bf16x2, a), __builtin_bit_cast(bf16x2, b), c, false);
}
DI float bflo(unsigned u) { return __uint_as_float(u << 16); }
DI float bfhi(unsigned u) { return __uint_as_float(u & 0xffff0000u); }
DI float red_add(float v) {
  auto a = __builtin_amdgcn_permlane32_swap(__float_as_uint(v), __float_as_uint(v), false, false);
  v = __uint_as_float(a[0]) + __uint_as_float(a[1]);
  auto b = __builtin_amdgcn_permlane16_swap(__float_as_uint(v), __float_as_uint(v), false, false);
  return __uint_as_float(b[0]) + __uint_as_float(b[1]);
}
DI float red_max(float v) {
  auto a = __builtin_amdgcn_permlane32_swap(__float_as_uint(v), __float_as_uint(v), false, false);
  v = fmaxf(__uint_as_float(a[0]), __uint_as_float(a[1]));
  auto b = __builtin_amdgcn_permlane16_swap(__float_as_uint(v), __float_as_uint(v), false, false);
  return fmaxf(__uint_as_float(b[0]), __uint_as_float(b[1]));
}
DI void swap32f(float& a, float& b) {
  auto r = __builtin_amdgcn_permlane32_swap(__float_as_uint(a), __float_as_uint(b), false, false);
  a = __uint_as_float(r[0]); b = __uint_as_float(r[1]);
}
DI void swap32u(unsigned& a, unsigned& b) { auto r = __builtin_amdgcn_permlane32_swap(a, b, false, false); a = r[0]; b = r[1]; }
DI void swap16u(unsigned& a, unsigned& b) { auto r = __builtin_amdgcn_permlane16_swap(a, b, false, false); a = r[0]; b = r[1]; }
constexpr unsigned ONES2 = 0x3F803F80u;
#define STMODE 3
DI void st16(void* p, u32x4 v) {
  asm volatile("global_store_dwordx4 %0, %1, off sc1\n\ts_nop 1" :: "v"(p), "v"(v) : "memory");
}
DI void st8(void* p, u32x2 v) {
  asm volatile("global_store_dwordx2 %0, %1, off sc1" :: "v"(p), "v"(v) : "memory");
}
#define STAMP_AT(kern, i)
#define STAMP_RT(kern, i)
#define STAMP_L0()
#define STAMP_L1(kern)
#define STAMP_END(kern)
#define STAMP_BEG(kern)

extern "C" __global__ void k1_qkv_attn(const u16*, const u16*, const u16*, const u16*, const u16*, char*);
extern "C" __global__ void k2_wo(const u16*, const u16*, char*);
extern "C" __global__ void k3_gateup(const u16*, const u16*, const u16*, char*);
extern "C" __global__ void k4_down(const u16*, char*, u16*);

constexpr int K1_STAGE = 43008, K1_B = 18432, K1_ST = 3 * K1_STAGE;
constexpr int K1_SCR = K1_ST + 144 * 8, K1_LDS = K1_SCR + 256;
#define PFD 3
struct K1Frag { u32x4 a[2]; u32x4 h; u32x2 b[6][2]; };

extern "C" __global__ __launch_bounds__(768, 3) void k1_qkv_attn(const u16* __restrict__ x, const u16* __restrict__ wqkv, const u16* __restrict__ wo,
                                                                 const u16* __restrict__ wg, const u16* __restrict__ wu, char* __restrict__ ws) {
  __shared__ __attribute__((aligned(1024))) char smem[K1_LDS];
  const unsigned L0 = (unsigned)(size_t)(LAS char*)smem;
  const int tid = threadIdx.x, lane = tid & 63;
  const int wid = __builtin_amdgcn_readfirstlane(tid >> 6);
  BW_DECL();
  const int xcd = blockIdx.x & 7, idx = blockIdx.x >> 3;
  const int pm = (xcd >> 1) * 4 + (idx >> 3), pn = (xcd & 1) * 8 + (idx & 7);
  const int s0 = pm * 128;
  const int l3 = lane >> 3, l7 = lane & 7;

  if (wid >= 8) {
    const int j = wid - 8;
    __builtin_amdgcn_s_setprio(3);
    const unsigned vline = lane * 128;
    CODE_ADDR(self, k1_qkv_attn);
    if (j >= 2) touch_g(L0 + K1_SCR, vline, self + (j - 2) * 8192);
    const u32x4 srdX = make_srd(x, S_ * D_ * 2);
    const u32x4 srdW = make_srd(wqkv, D_ * 3 * D_ * 2);
    unsigned voffA[2], voffB[2];
#pragma unroll
    for (int i = 0; i < 2; ++i) {
      voffA[i] = (unsigned)(s0 + 32 * j + 8 * i + l3) * 2048 + (unsigned)(l7 ^ (4 * i + (lane >> 4))) * 16;
      voffB[i] = (unsigned)(16 * j + 8 * i + l3) * 6144 + pn * 128 + (unsigned)(l7 ^ ((((lane >> 4) & 1) | (i << 1)) << 1)) * 16;
    }
    unsigned voffE;
    if (j < 2) {
      const int r = s0 - 16 + 8 * j + l3;
      voffE = (unsigned)(r < 0 ? 0 : r) * 2048 + (unsigned)(l7 ^ (4 * j + (lane >> 4))) * 16;
      if (j == 0 && lane < 24) *(LAS u32x4*)(L0 + l3 * K1_STAGE + 16384 + l7 * 16) = (u32x4){ONES2, ONES2, ONES2, ONES2};
    } else voffE = j == 2 ? (blockIdx.x * 8 + l7) * 2048 + PFD * 128 : PFD * (64 * 6144) + (blockIdx.x * 12 + (lane < 12 ? lane : 11)) * 128;
    const unsigned long long emask = j == 0 ? ~0xFFull : ~0ull;
    CODE_ADDR(nextk, k2_wo);
    unsigned so = 0;
#pragma nounroll
    for (int kt = 0; kt < 16; ++kt) {
      if (j < 2) asm volatile("s_mov_b32 m0, %0\n\ts_mov_b64 exec, %4\n\tbuffer_load_dwordx4 %1, %2, %3 offen lds\n\ts_mov_b64 exec, -1"
                              ::"s"(L0 + so + 16384 + j * 1024), "v"(voffE), "s"(srdX), "s"(kt * 128), "s"(emask) : "m0");
      else if (kt >= 16 - PFD) touch_g(L0 + K1_SCR, vline, j == 2 ? self + 2048 : nextk);
      else touch_b(L0 + K1_SCR, voffE, j == 2 ? srdX : srdW, j == 2 ? kt * 128 : kt * (64 * 6144));
#pragma unroll
      for (int i = 0; i < 4; ++i) dma(L0 + so + j * 4096 + i * 1024, voffA[i & 1], srdX, kt * 128 + (i >> 1) * 32768);
#pragma unroll
      for (int i = 0; i < 6; ++i) dma(L0 + so + K1_B + (i >> 1) * 8192 + j * 2048 + (i & 1) * 1024, voffB[i & 1], srdW, kt * (64 * 6144) + (i >> 1) * 2048);
      so = so == 2 * K1_STAGE ? 0 : so + K1_STAGE;
      if (kt > 0) { WAIT_VL(11); BARRIER(); }
    }
    u32x4 hv[8];
    {
      const int r = s0 - 16 + 4 * j + (lane >> 4);
      const u16* p = x + (size_t)(r < 0 ? 0 : r) * D_ + (lane & 15) * 8;
#pragma unroll
      for (int i = 0; i < 8; ++i) asm volatile("global_load_dwordx4 %0, %1, off offset:%2" : "=v"(hv[i]) : "v"(p), "n"(i * 256));
    }
    WAIT_V(8); BARRIER();
    u32x4 wrow0, wrow1;
    {
      const u16* p = wo + (size_t)(blockIdx.x * 4 + j) * D_ + lane * 8;
      asm volatile("global_load_dwordx4 %0, %1, off" : "=v"(wrow0) : "v"(p));
      asm volatile("global_load_dwordx4 %0, %1, off offset:1024" : "=v"(wrow1) : "v"(p));
      const u32x4 srdGU = make_srd((j & 1) ? wu : wg, D_ * F_ * 2);
      touch_b(L0 + K1_SCR, vline, srdGU, ((j >> 1) * 256 + blockIdx.x) * 8192);
    }
    BARRIER();
    WAIT_V(3);
    {
      float a[2] = {0.f, 0.f}, q[2] = {0.f, 0.f};
#pragma unroll
      for (int i = 0; i < 8; ++i) {
        asm volatile("" : "+v"(hv[i]));
#pragma unroll
        for (int e = 0; e < 4; ++e) { a[e & 1] = dot2(hv[i][e], ONES2, a[e & 1]); q[e & 1] = dot2(hv[i][e], hv[i][e], q[e & 1]); }
      }
      float sa = a[0] + a[1], sq = q[0] + q[1];
#pragma unroll
      for (int o = 1; o < 16; o <<= 1) { sa += __shfl_xor(sa, o); sq += __shfl_xor(sq, o); }
      const float mean = sa * (1.0f / D_), var = fmaxf(sq * (1.0f / D_) - mean * mean, 0.f);
      if ((lane & 15) == 0) *(LAS f32x2*)(L0 + K1_ST + (4 * j + (lane >> 4)) * 8) = (f32x2){mean, __builtin_amdgcn_rsqf(var + LN_EPS)};
    }
    WAIT_L0(); BARRIER();
    BARRIER();
    WAIT_V(1);
    asm volatile("" : "+v"(wrow0), "+v"(wrow1));
    float s = 0.f;
#pragma unroll
    for (int e = 0; e < 4; ++e) { s = dot2(wrow0[e], ONES2, s); s = dot2(wrow1[e], ONES2, s); }
#pragma unroll
    for (int o = 1; o < 16; o <<= 1) s += __shfl_xor(s, o);
    s = red_add(s) * (1.0f / D_);
    const unsigned hi = pk(s, 0.f) & 0xffffu;
    const unsigned lo = pk(s - bflo(hi), 0.f) & 0xffffu;
    const int k = blockIdx.x * 4 + j, dd = k & 31;
    u16* t = (u16*)(ws + WS_WTAB) + (k >> 5) * 96 + ((dd >> 2) & 3) * 24 + 4 * (dd >> 4) + (dd & 3);
    if (lane < 3) t[lane * 8] = (u16)(lane == 0 ? hi : lane == 1 ? lo : 0u);
    BW_OUT(0);
    return;
  }

  const int g = wid & 3, hh = wid >> 2, fr = lane & 15, fq = lane >> 4;
  STAMP_BEG(0);
  unsigned ra[2][2], rh[2], rb[6];
#pragma unroll
  for (int ks = 0; ks < 2; ++ks) {
    const unsigned c = (unsigned)((4 * ks + fq) ^ (fr >> 1)) * 16;
#pragma unroll
    for (int m = 0; m < 2; ++m) ra[m][ks] = L0 + (unsigned)(16 * (2 * g + (m ^ hh)) + fr) * 128 + c;
    rh[ks] = L0 + 16384 + fr * 128 + c;
  }
  {
    const int q = fr >> 2, p = lane & 3, f = ((q >> 1) & 1) | ((fq & 1) << 1);
#pragma unroll
    for (int j = 0; j < 6; ++j) {
      const int a = j < 4 ? ((j + g) & 3) : 4 + ((j + g) & 1);
      rb[j] = L0 + K1_B + (a >> 1) * 8192 + (unsigned)(8 * fq + q) * 128 + (unsigned)((2 * hh + (a & 1)) ^ f) * 32 + 8 * p;
    }
  }
  f32x4 acc[2][6], accHT = {0, 0, 0, 0}, accHV = {0, 0, 0, 0}, accS = {0, 0, 0, 0}, accQ = {0, 0, 0, 0};
  u32x4 onesf = {ONES2, ONES2, ONES2, ONES2};
  asm volatile("" : "+v"(onesf));
#pragma unroll
  for (int m = 0; m < 2; ++m)
#pragma unroll
    for (int j = 0; j < 6; ++j) acc[m][j] = (f32x4){0, 0, 0, 0};
  K1Frag F0, F1;
  auto rd = [&](K1Frag& F, int i, int ks) {
    if (i < 2) F.a[i] = lds128(ra[i][ks]);
    else if (i == 2) F.h = lds128(rh[ks]);
    else { const int e = i - 3, j = e >> 1, t = e & 1; F.b[j][t] = ldstr(rb[j] + ks * 4096 + t * 512); }
  };
  auto mm = [&](K1Frag& F, int i) {
    if (i < 12) {
      const int j = i >> 1, m = i & 1;
      const u32x4 b = cat(F.b[j][0], F.b[j][1]);
      if (j < 4) acc[m][j] = mfma(b, F.a[m], acc[m][j]); else acc[m][j] = mfma(F.a[m], b, acc[m][j]);
    } else if (i == 12) accHT = mfma(cat(F.b[0][0], F.b[0][1]), F.h, accHT);
    else if (i == 13) accS = mfma(onesf, F.a[0], accS);
    else if (i == 14) accQ = mfma(F.a[0], F.a[0], accQ);
    else accHV = mfma(F.h, cat(F.b[4][0], F.b[4][1]), accHV);
  };

  BARRIER();
#pragma unroll
  for (int i = 0; i < 15; ++i) rd(F0, i, 0);
  WAIT_L0();
  STAMP_L0();
  unsigned sidx = 0;
  for (int kt = 0; kt < 16; ++kt) {
    const unsigned dl = sidx == 2 ? (unsigned)(-2 * K1_STAGE) : (unsigned)K1_STAGE;
    sidx = sidx == 2 ? 0 : sidx + 1;
#pragma unroll
    for (int i = 0; i < 16; ++i) {
      mm(F0, i);
      if (i < 7) { rd(F1, 2 * i, 1); rd(F1, 2 * i + 1, 1); }
      if (i == 5) VADD(ra[0][0], dl);
      if (i == 6) VADD(ra[1][0], dl);
      if (i == 7) rd(F1, 14, 1);
      if (i >= 8 && i < 14) VADD(rb[i - 8], dl);
      if (i == 14) VADD(rh[0], dl);
      SB();
    }
    WAIT_L0(); BARRIER();
#pragma unroll
    for (int i = 0; i < 16; ++i) {
      mm(F1, i);
      if (i < 7) { rd(F0, 2 * i, 0); rd(F0, 2 * i + 1, 0); }
      if (i == 7) rd(F0, 14, 0);
      if (i == 9) VADD(ra[0][1], dl);
      if (i == 11) VADD(ra[1][1], dl);
      if (i == 13) VADD(rh[1], dl);
      SB();
    }
    WAIT_L0();
  }
  STAMP_L1(0);

  constexpr unsigned X_OFF = 0;
  constexpr unsigned CT = 2 * K1_STAGE;
  constexpr unsigned CV = CT + 512;
  {
    if (fr == 0) *(LAS f32x4*)(L0 + CT + (hh * 4 + g) * 64 + fq * 16) = accHT;
    if (fq == 0) *(LAS float*)(L0 + CV + (hh * 2 + (g & 1)) * 64 + fr * 4) = accHV[0];
    const int r = fr & 3;
    const float dq = r == 0 ? accQ[0] : r == 1 ? accQ[1] : r == 2 ? accQ[2] : accQ[3];
    const float mean = accS[0] * (1.0f / D_), var = fmaxf(dq * (1.0f / D_) - mean * mean, 0.f);
    const int tl = 16 * (2 * g + hh) + fr;
    if (fq == (fr >> 2)) {
      *(LAS f32x2*)(L0 + K1_ST + (16 + tl) * 8) = (f32x2){mean, __builtin_amdgcn_rsqf(var + LN_EPS)};
      if (pn == 0) ((float*)(ws + WS_MUX))[s0 + tl] = mean;
    }
  }
  WAIT_L0(); BARRIER();
  {
    const unsigned xb = L0 + X_OFF + hh * (9 * 3072) + lane * 16;
    f32x4 ct[4]; float cv[2];
#pragma unroll
    for (int j = 0; j < 4; ++j) ct[j] = lds128f(L0 + CT + (hh * 4 + ((j + g) & 3)) * 64 + fq * 16);
#pragma unroll
    for (int j = 0; j < 2; ++j) cv[j] = *(LAS float*)(L0 + CV + (hh * 2 + ((j + g) & 1)) * 64 + fr * 4);
#pragma unroll
    for (int m = 0; m < 3; ++m) {
      const int ti = m == 2 ? 0 : 2 * g + (m ^ hh) + 1;
      f32x2 st = *(LAS f32x2*)(L0 + K1_ST + (16 * ti + fr) * 8);
      f32x4 s01 = lds128f(L0 + K1_ST + (16 * ti + 4 * fq) * 8), s23 = lds128f(L0 + K1_ST + (16 * ti + 4 * fq + 2) * 8);
#pragma unroll
      for (int j = 0; j < (m == 2 ? 1 : 4); ++j) {
        const f32x4 a = m == 2 ? accHT : acc[m & 1][j];
        const f32x4 v = (a - ct[j] * st.x) * st.y;
        const int at = (j + g) & 3;
        u32x2 w; w.x = pk(v[0], v[1]); w.y = pk(v[2], v[3]);
        *(LAS u32x2*)(xb + ti * 3072 + (at >> 1) * 1024 + (at & 1) * 8) = w;
      }
#pragma unroll
      for (int j = 0; j < (m == 2 ? 1 : 2); ++j) {
        const f32x4 a = m == 2 ? accHV : acc[m & 1][4 + j];
        u32x2 w;
        w.x = pk((a[0] - cv[j] * s01[0]) * s01[1], (a[1] - cv[j] * s01[2]) * s01[3]);
        w.y = pk((a[2] - cv[j] * s23[0]) * s23[1], (a[3] - cv[j] * s23[2]) * s23[3]);
        *(LAS u32x2*)(xb + ti * 3072 + 2048 + ((j + g) & 1) * 8) = w;
      }
    }
  }
  WAIT_L0(); BARRIER();
  {
    const unsigned xb = L0 + X_OFF + hh * (9 * 3072) + 2 * g * 3072 + lane * 16;
    u32x4 kf[3], vf[3], qf[2];
#pragma unroll
    for (int t = 0; t < 3; ++t) { kf[t] = lds128(xb + t * 3072 + 1024); vf[t] = lds128(xb + t * 3072 + 2048); }
#pragma unroll
    for (int t = 0; t < 2; ++t) qf[t] = lds128(xb + (t + 1) * 3072);
    const float c = 0.17677669529663687f * 1.4426950408889634f;
    char* of = ws + WS_O + (size_t)(2 * pn + hh) * 131072 + (size_t)(pm * 8 + 2 * g) * 1024 + lane * 16;
#pragma unroll
    for (int m = 0; m < 2; ++m) {
      const f32x4 z = {0, 0, 0, 0};
      f32x4 sp = mfma(kf[m], qf[m], z), sc = mfma(kf[m + 1], qf[m], z);
      const bool pok = (m == 1) || (pm | g) != 0;
      float s[8];
#pragma unroll
      for (int r = 0; r < 4; ++r) {
        const int d = fr - 4 * fq - r;
        s[r] = (pok && ((d & 1) == 0) && d <= -2) ? sp[r] : -INFINITY;
        s[4 + r] = (((d & 1) == 0) && d >= 0 && d <= 14) ? sc[r] : -INFINITY;
      }
      float mx = fmaxf(fmaxf(fmaxf(s[0], s[1]), fmaxf(s[2], s[3])), fmaxf(fmaxf(s[4], s[5]), fmaxf(s[6], s[7])));
      mx = red_max(mx);
      const float mc = mx * c;
      float sum = 0.f;
#pragma unroll
      for (int e = 0; e < 8; ++e) { s[e] = __builtin_amdgcn_exp2f(s[e] * c - mc); sum += s[e]; }
      sum = red_add(sum);
      const float inv = __builtin_amdgcn_rcpf(sum);
      u32x4 p;
#pragma unroll
      for (int e = 0; e < 4; ++e) p[e] = pk(s[2 * e] * inv, s[2 * e + 1] * inv);
      u32x4 va, vb;
      va.x = vf[m].x; va.y = vf[m].y; va.z = vf[m + 1].x; va.w = vf[m + 1].y;
      vb.x = vf[m].z; vb.y = vf[m].w; vb.z = vf[m + 1].z; vb.w = vf[m + 1].w;
      const f32x4 oa = mfma(va, p, z), ob = mfma(vb, p, z);
      u32x4 w; w.x = pk(oa[0], oa[1]); w.y = pk(oa[2], oa[3]); w.z = pk(ob[0], ob[1]); w.w = pk(ob[2], ob[3]);
      st16(of + m * 1024, w);
    }
  }
  BW_OUT(0);
  STAMP_END(0);
}

constexpr int K2_SLOT = 24576, K2_NS = 6, K2_TAB = K2_SLOT * K2_NS, K2_SCR = K2_TAB + 8192, K2_LDS = K2_SCR + 256;
struct K2Frag { u32x4 a[2]; u32x2 b[4][2]; };

template <int MODE>
DI void proj_body(const u16* __restrict__ x, const u16* __restrict__ wmat, char* __restrict__ ws, u16* __restrict__ out, const unsigned L0,
                  const uint64_t nextk) {
  constexpr int NKS = MODE == 0 ? 32 : 112, NIT = NKS / 2;
  const int tid = threadIdx.x, lane = tid & 63;
  const int wid = __builtin_amdgcn_readfirstlane(tid >> 6);
  BW_DECL();
  const int g = wid & 3, fr = lane & 15, fq = lane >> 4;
  const int xcd = blockIdx.x & 7, idx = blockIdx.x >> 3;
#define MAP24 2
  const int pm = (xcd >> 1) * 4 + (idx >> 3), pn = (xcd & 1) + 2 * (idx & 7);

  if (wid >= 4) {
    const int ga = wid & 1;
    int kP = 0, kk = wid == 8 ? 0 : ga;
    auto rowbase = [&](int ks, int j) -> unsigned {
      if (MODE == 0) return ks * 32 + 8 * j;
      const int a = 56 * kk + 8 * j, b = 112 * kk + 56 * (j & 1) + 8 * (j >> 1) - 416, c = 56 * j + 48;
      int r = kk < 4 ? a : b;
      r = kk == 6 ? c : r;
      return kP + r;
    };
    auto adv = [&](int n) { const int p2 = kP + 224, k1 = kk + n, k2 = k1 - 7; kP = k1 >= 7 ? p2 : kP; kk = k1 >= 7 ? k2 : k1; };
    const u32x4 srdA = make_srd(ws + (MODE == 0 ? WS_O : WS_ACT), NKS * 131072);
    const u32x4 srdB = make_srd(wmat, NKS * 32 * 2048);
    const unsigned voffA = lane * 16 + pm * 8192;
    const unsigned voffB = (unsigned)(lane >> 3) * 2048 + pn * 128 + (unsigned)((lane & 7) ^ (((lane >> 4) & 3) << 1)) * 16;
    const int ml = idx & 7, jl = idx >> 3;
    unsigned so = 0;
    if (wid == 8) {
      if (MODE == 0) {
        const u32x4 srdT = make_srd(ws + WS_WTAB, 6144);
#pragma unroll
        for (int q = 0; q < 6; ++q) dma(L0 + K2_TAB + q * 1024, lane * 16, srdT, q * 1024);
      }
#pragma nounroll
      for (int it = 0; it < NIT + 2; ++it) {
        if (it < NIT) {
#pragma unroll
          for (int u = 0; u < 2; ++u) {
            dma(L0 + so + u * 8192 + ml * 1024, voffA, srdA, (2 * it + u) * 131072 + ml * 1024);
            dma(L0 + so + 16384 + u * 4096 + jl * 1024, voffB, srdB, rowbase(2 * it + u, jl) * 2048);
            adv(1);
          }
          so = so == (K2_NS - 1) * K2_SLOT ? 0 : so + K2_SLOT;
          if (it < 4) continue;
          WAIT_V(16);
        } else if (it == NIT) WAIT_V(12);
        else WAIT_V(8);
        BARRIER();
      }
      WAIT_V(4); BARRIER();
      WAIT_V(0); BARRIER();
      if (MODE == 0) { touch_g(L0 + K2_SCR, lane * 128, nextk); touch_g(L0 + K2_SCR, lane * 128, nextk + 8192); }
      BW_OUT(1 + 2 * MODE);
      return;
    }
    unsigned mo[5];
#pragma unroll
    for (int q = 0; q < 5; ++q) mo[q] = ((ml + 1 + q + (wid >= 6 ? 5 : 0)) & 7) * 1024;
#pragma nounroll
    for (int it = 0; it < NIT; ++it) {
      const unsigned la = L0 + so + ga * 8192, sa = (2 * it + ga) * 131072;
      dma(la + mo[0], voffA, srdA, sa + mo[0]);
      dma(la + mo[1], voffA, srdA, sa + mo[1]);
      if (wid < 6) {
#pragma unroll
        for (int q = 2; q < 5; ++q) dma(la + mo[q], voffA, srdA, sa + mo[q]);
      } else {
#pragma unroll
        for (int q = 1; q < 4; ++q) { const int j = (jl + q) & 3; dma(L0 + so + 16384 + ga * 4096 + j * 1024, voffB, srdB, rowbase(2 * it + ga, j) * 2048); }
        adv(2);
      }
      so = so == (K2_NS - 1) * K2_SLOT ? 0 : so + K2_SLOT;
      if (it > 0) { WAIT_V(5); BARRIER(); }
    }
    WAIT_V(0); BARRIER();
    BW_OUT(1 + 2 * MODE);
    return;
  }

  STAMP_BEG(1 + 2 * MODE);
  const int tok = pm * 128 + 32 * g + fr;
  u32x2 rx[2][4]; u32x4 rh[2][2]; float rmu[2];
#pragma unroll
  for (int m = 0; m < 2; ++m) {
    if (MODE == 0) {
      const u16* p = x + (size_t)(tok + 16 * m) * D_ + pn * 64 + 4 * fq;
      asm volatile("global_load_dwordx2 %0, %1, off" : "=v"(rx[m][0]) : "v"(p));
      asm volatile("global_load_dwordx2 %0, %1, off offset:32" : "=v"(rx[m][1]) : "v"(p));
      asm volatile("global_load_dwordx2 %0, %1, off offset:64" : "=v"(rx[m][2]) : "v"(p));
      asm volatile("global_load_dwordx2 %0, %1, off offset:96" : "=v"(rx[m][3]) : "v"(p));
    } else {
      const char* p = ws + WS_HC + (size_t)(2 * pn) * 131072 + (size_t)(pm * 8 + 2 * g + m) * 1024 + lane * 16;
      const char* p1 = p + 131072;
      asm volatile("global_load_dwordx4 %0, %1, off" : "=v"(rh[m][0]) : "v"(p));
      asm volatile("global_load_dwordx4 %0, %1, off" : "=v"(rh[m][1]) : "v"(p1));
    }
    const float* q = (const float*)(ws + (MODE == 0 ? WS_MUX : WS_MU)) + tok + 16 * m;
    asm volatile("global_load_dword %0, %1, off" : "=v"(rmu[m]) : "v"(q));
  }

  unsigned ra = L0 + g * 2048 + lane * 16;
  unsigned rb[4];
  {
    const int q = fr >> 2, p = lane & 3, sw = (2 * fq + (q >> 1)) & 3;
#pragma unroll
    for (int n = 0; n < 4; ++n) rb[n] = L0 + 16384 + (unsigned)(4 * fq + q) * 128 + (unsigned)(n ^ sw) * 32 + 8 * p;
  }
  unsigned rw = L0 + K2_TAB + fq * 48 + (fr < 2 ? fr : 2) * 16;

  f32x4 acc[2][4], accW[2];
#pragma unroll
  for (int m = 0; m < 2; ++m) {
    accW[m] = (f32x4){0, 0, 0, 0};
#pragma unroll
    for (int n = 0; n < 4; ++n) acc[m][n] = (f32x4){0, 0, 0, 0};
  }
  K2Frag F0[2], F1[2];
  constexpr int NRD = 10;
  auto rd = [&](K2Frag (&F)[2], int i) {
    const int ga = i / NRD, k = i % NRD;
    if (k < 2) F[ga].a[k] = lds128(ra + ga * 8192 + k * 1024);
    else { const int e = k - 2; F[ga].b[e >> 1][e & 1] = ldstr(rb[e >> 1] + ga * 4096 + (e & 1) * 2048); }
  };
  auto bump = [&](int i, unsigned dl) {
    if (i == 0) VADD(ra, dl);
    else if (i < 5) VADD(rb[i - 1], dl);
    else if (MODE == 0) { const unsigned c = 384; VADD(rw, c); }
  };
  auto step = [&](K2Frag (&F)[2], K2Frag (&N)[2], unsigned dl) {
    u32x4 wf[2];
    if (MODE == 0) { wf[0] = lds128(rw); wf[1] = lds128(rw + 192); }
#pragma unroll
    for (int i = 0; i < 16; ++i) {
      const int ga = i >> 3, n = (i >> 1) & 3, m = i & 1;
      acc[m][n] = mfma(cat(F[ga].b[n][0], F[ga].b[n][1]), F[ga].a[m], acc[m][n]);
      if (i < NRD) { rd(N, 2 * i); rd(N, 2 * i + 1); }
      if (i >= 13) { bump(2 * (i - 13), dl); bump(2 * (i - 13) + 1, dl); }
      SB();
    }
    if (MODE == 0) {
#pragma unroll
      for (int i = 0; i < 4; ++i) accW[i & 1] = mfma(wf[i >> 1], F[i >> 1].a[i & 1], accW[i & 1]);
      SB();
    }
  };

  BARRIER();
#pragma unroll
  for (int i = 0; i < 2 * NRD; ++i) rd(F0, i);
#pragma unroll
  for (int i = 0; i < 5; ++i) bump(i, K2_SLOT);
  WAIT_L0(); BARRIER();
  STAMP_L0();
  int sidx = 1;
  for (int it = 0; it < NIT - 2; it += 2) {
    step(F0, F1, sidx == 5 ? (unsigned)(-5 * K2_SLOT) : (unsigned)K2_SLOT);
    WAIT_L0(); BARRIER();
    step(F1, F0, K2_SLOT);
    WAIT_L0(); BARRIER();
    sidx = sidx == 5 ? 1 : sidx + 2;
  }
  step(F0, F1, 0);
  step(F1, F0, 0);
  WAIT_VL(0);
  STAMP_L1(1 + 2 * MODE);

#pragma unroll
  for (int m = 0; m < 2; ++m) {
    if (MODE == 0) {
      asm volatile("" : "+v"(rx[m][0]), "+v"(rx[m][1]), "+v"(rx[m][2]), "+v"(rx[m][3]), "+v"(rmu[m]));
      const float mu = rmu[m] + red_add(accW[m][0] + accW[m][1]);
      unsigned hp[4][2]; float ss = 0.f;
#pragma unroll
      for (int n = 0; n < 4; ++n) {
        hp[n][0] = pk(acc[m][n][0] + bflo(rx[m][n].x) - mu, acc[m][n][1] + bfhi(rx[m][n].x) - mu);
        hp[n][1] = pk(acc[m][n][2] + bflo(rx[m][n].y) - mu, acc[m][n][3] + bfhi(rx[m][n].y) - mu);
        ss = dot2(hp[n][0], hp[n][0], ss); ss = dot2(hp[n][1], hp[n][1], ss);
      }
      char* hb = ws + WS_HC + (size_t)(2 * pn) * 131072 + (size_t)(pm * 8 + 2 * g + m) * 1024 + lane * 16;
      st16(hb, (u32x4){hp[0][0], hp[0][1], hp[1][0], hp[1][1]});
      st16(hb + 131072, (u32x4){hp[2][0], hp[2][1], hp[3][0], hp[3][1]});
      ss = red_add(ss);
      if (fq == 0) {
        ((float*)(ws + WS_HC + (size_t)32 * 131072))[(tok + 16 * m) * 16 + pn] = ss;
        if (pn == 0) ((float*)(ws + WS_MU))[tok + 16 * m] = mu;
      }
    } else {
      asm volatile("" : "+v"(rh[m][0]), "+v"(rh[m][1]), "+v"(rmu[m]));
      u16* op = out + (size_t)(tok + 16 * m) * D_ + pn * 64 + (fq & 1) * 16 + (fq >> 1) * 8;
      const float mu = rmu[m];
#pragma unroll
      for (int c = 0; c < 2; ++c) {
        const u32x4 h = rh[m][c];
        const f32x4 y0 = acc[m][2 * c], y1 = acc[m][2 * c + 1];
        unsigned a0 = pk(y0[0] + (bflo(h.x) + mu), y0[1] + (bfhi(h.x) + mu));
        unsigned a1 = pk(y0[2] + (bflo(h.y) + mu), y0[3] + (bfhi(h.y) + mu));
        unsigned b0 = pk(y1[0] + (bflo(h.z) + mu), y1[1] + (bfhi(h.z) + mu));
        unsigned b1 = pk(y1[2] + (bflo(h.w) + mu), y1[3] + (bfhi(h.w) + mu));
        swap16u(a0, b0); swap16u(a1, b1);
        *(u32x4*)(op + c * 32) = (u32x4){a0, a1, b0, b1};
      }
    }
  }
  BW_OUT(1 + 2 * MODE);
  STAMP_END(1 + 2 * MODE);
}

extern "C" __global__ __launch_bounds__(576, 3) void k2_wo(const u16* __restrict__ x, const u16* __restrict__ wo, char* __restrict__ ws) {
  __shared__ __attribute__((aligned(1024))) char smem[K2_LDS];
  CODE_ADDR(nextk, k3_gateup);
  proj_body<0>(x, wo, ws, nullptr, (unsigned)(size_t)(LAS char*)smem, nextk);
}
extern "C" __global__ __launch_bounds__(576, 3) void k4_down(const u16* __restrict__ wd, char* __restrict__ ws, u16* __restrict__ out) {
  __shared__ __attribute__((aligned(1024))) char smem[K2_LDS];
  proj_body<1>(nullptr, wd, ws, out, (unsigned)(size_t)(LAS char*)smem, 0);
}

constexpr int K3_G = 16384, K3_U = K3_G + 7168 + 16, K3_SLOT = 30752, K3_NS = 5, K3_SCR = K3_SLOT * K3_NS, K3_LDS = K3_SCR + 256;
struct K3Frag { u32x4 a[4]; u32x2 b[7][2]; };

DI unsigned geglu2(f32x2 g, f32x2 u, float k1, float k2, float ir2) {
  const f32x2 z = g * (g * g * k2 + k1);
  f32x2 e; e.x = __builtin_amdgcn_exp2f(z.x); e.y = __builtin_amdgcn_exp2f(z.y);
  const f32x2 d = e * ir2 + ir2;
  f32x2 r; r.x = __builtin_amdgcn_rcpf(d.x); r.y = __builtin_amdgcn_rcpf(d.y);
  const f32x2 o = (g * u) * r;
  return pk(o.x, o.y);
}

extern "C" __global__ __launch_bounds__(512, 2) void k3_gateup(const u16* __restrict__ wg, const u16* __restrict__ wu, const u16* __restrict__ wd,
                                                               char* __restrict__ ws) {
  __shared__ __attribute__((aligned(1024))) char smem[K3_LDS];
  const unsigned L0 = (unsigned)(size_t)(LAS char*)smem;
  const int tid = threadIdx.x, lane = tid & 63;
  const int wid = __builtin_amdgcn_readfirstlane(tid >> 6);
  BW_DECL();
  const int wm = wid & 3, wn = wid >> 2, fr = lane & 15, fq = lane >> 4;
  const int xcd = blockIdx.x & 7, idx = blockIdx.x >> 3;
  const int pm = (xcd >> 2) * 4 + (idx >> 3), pn = (xcd & 3) * 8 + (idx & 7);
  STAMP_BEG(2);

  const u32x4 srdA = make_srd(ws + WS_HC, 32 * 131072);
  const u32x4 srdG = make_srd(wg, D_ * F_ * 2), srdU = make_srd(wu, D_ * F_ * 2);
  const unsigned voffA = lane * 16 + wid * 2048 + pm * 16384;
  unsigned voffB;
  const int wb = wid < 7 ? wid : 6;
  { const unsigned L = wb * 64 + lane, r = (L * 4682u) >> 16; voffB = r * 7168 + pn * 224 + (L - r * 14) * 16; }
  const unsigned ldA = L0 + wid * 2048, ldG = L0 + K3_G + wb * 1024, ldU = L0 + K3_U + wb * 1024;
  auto dA = [&](int j, unsigned so, int i) { dma(ldA + so + i * 1024, voffA, srdA, j * 131072 + i * 1024); };
  auto dG = [&](int j, unsigned so) { dma(ldG + so, voffB, srdG, j * (32 * 7168)); };
  auto dU = [&](int j, unsigned so) { dma(ldU + so, voffB, srdU, j * (32 * 7168)); };
#define K3_WAIT(slots, extra) do { WAIT_VL(4 * (slots) + (extra)); BARRIER(); } while (0)
#pragma unroll
  for (int j = 0; j < 3; ++j) { dA(j, j * K3_SLOT, 0); dA(j, j * K3_SLOT, 1); dG(j, j * K3_SLOT); dU(j, j * K3_SLOT); }
  unsigned ra = L0 + wm * 4096 + lane * 16;
  unsigned rb = L0 + K3_G + (unsigned)(4 * fq + (fr >> 2)) * 224 + 112 * wn + (lane & 3) * 8;
  unsigned rm = L0 + K3_G + (unsigned)(4 * fq + (fr >> 2)) * 224 + 112 * wn + 96 + (lane & 1) * 8 + ((lane >> 1) & 1) * (K3_U - K3_G);

  f32x4 acc[4][7];
#pragma unroll
  for (int m = 0; m < 4; ++m)
#pragma unroll
    for (int n = 0; n < 7; ++n) acc[m][n] = (f32x4){0, 0, 0, 0};
  K3Frag F0, F1;
  auto rd = [&](K3Frag& F, int i) {
    if (i < 4) F.a[i] = lds128(ra + i * 1024);
    else {
      const int e = i - 4, n = e >> 1, t = e & 1;
      F.b[n][t] = n < 6 ? ldstr(rb + (n >> 1) * 32 + (n & 1) * (K3_U - K3_G) + t * 3584) : ldstr(rm + t * 3584);
    }
  };
  auto step = [&](K3Frag& F, K3Frag& N, unsigned so1, int ND, int jd, unsigned sd, unsigned sd2, bool AONLY, bool RD) {
    const unsigned dl = so1 == (K3_NS - 1) * K3_SLOT ? (unsigned)(-(K3_NS - 1) * K3_SLOT) : (unsigned)K3_SLOT;
#pragma unroll
    for (int i = 0; i < 28; ++i) {
      const int n = i >> 2, m = i & 3;
      acc[m][n] = mfma(cat(F.b[n][0], F.b[n][1]), F.a[m], acc[m][n]);
      if (RD && i < 18) rd(N, i);
      if (RD && i == 19) VADD(ra, dl);
      if (RD && i == 21) VADD(rb, dl);
      if (RD && i == 23) VADD(rm, dl);
      const int s = ND == 2 ? 10 : 18;
      if (ND > 0) {
        if (i == s) dA(jd, sd, 0);
        if (i == s + 2) dA(jd, sd, 1);
        if (i == s + 4 && !AONLY) dG(jd, sd);
        if (i == s + 6 && !AONLY) dU(jd, sd);
      }
      if (ND > 1) {
        if (i == 18) dA(jd + 1, sd2, 0);
        if (i == 20) dA(jd + 1, sd2, 1);
        if (i == 22) dG(jd + 1, sd2);
        if (i == 24) dU(jd + 1, sd2);
      }
      SB();
    }
  };

  K3_WAIT(2, 0);
#pragma unroll
  for (int i = 0; i < 18; ++i) rd(F0, i);
  { const unsigned dl = K3_SLOT; VADD(ra, dl); VADD(rb, dl); VADD(rm, dl); }
  K3_WAIT(1, 0);
  STAMP_L0();
  step(F0, F1, K3_SLOT, 2, 3, 3 * K3_SLOT, 4 * K3_SLOT, false, true);
  K3_WAIT(2, 0);
  step(F1, F0, 2 * K3_SLOT, 2, 5, 0, K3_SLOT, false, true);
  touch_b(L0 + K3_SCR, (lane < 24 ? lane : 23) * 128 + wm * 3072, wn ? srdU : srdG, 4194304 + blockIdx.x * 12288);
  K3_WAIT(3, 0);
  CODE_ADDR(nextk, k4_down);
  uint64_t here; asm volatile("s_getpc_b64 %0" : "=s"(here));
  unsigned so = 2 * K3_SLOT;
  for (int j = 2; j < 26; j += 2) {
    const unsigned so1 = so == (K3_NS - 1) * K3_SLOT ? 0 : so + K3_SLOT;
    const unsigned so2 = so1 == (K3_NS - 1) * K3_SLOT ? 0 : so1 + K3_SLOT;
    step(F0, F1, so1, 1, j + 5, so, 0, false, true);
    K3_WAIT(3, 0);
    step(F1, F0, so2, 1, j + 6, so1, 0, false, true);
    if (j == 18 && wid < 3) touch_g(L0 + K3_SCR, lane * 128, wid == 2 ? nextk : here + wid * 8192);
    K3_WAIT(3, 0);
    so = so2;
  }
  f32x4 ssq[4];
  {
    const char* q = ws + WS_HC + (size_t)32 * 131072 + (size_t)(pm * 256 + 64 * wm + fr) * 64 + fq * 16;
#pragma unroll
    for (int m = 0; m < 4; ++m) asm volatile("global_load_dwordx4 %0, %1, off offset:%2" : "=v"(ssq[m]) : "v"(q), "n"(m * 1024));
  }
  step(F0, F1, 2 * K3_SLOT, 1, 31, 1 * K3_SLOT, 0, false, true);
  K3_WAIT(3, 4);
  {
    const u32x4 srdD = make_srd(wd, D_ * F_ * 2);
    touch_b(L0 + K3_SCR, (lane < 28 ? lane : 27) * 128 + wid * 3584, srdD, blockIdx.x * 28672);
  }
  step(F1, F0, 3 * K3_SLOT, 0, 0, 0, 0, false, true);
  WAIT_VL(1); BARRIER();
  step(F0, F1, 4 * K3_SLOT, 0, 0, 0, 0, false, true);
  step(F1, F0, 0, 0, 0, 0, 0, false, true);
  step(F0, F1, 1 * K3_SLOT, 0, 0, 0, 0, false, true);
  step(F1, F0, 0, 0, 0, 0, 0, false, false);
  STAMP_L1(2);
  float k1[4], k2[4], ir2[4];
#pragma unroll
  for (int m = 0; m < 4; ++m) {
    asm volatile("" : "+v"(ssq[m]));
    const f32x4 p = ssq[m];
    ir2[m] = red_add((p[0] + p[1]) + (p[2] + p[3])) * (1.0f / D_) + LN_EPS;
    const float r = __builtin_amdgcn_rsqf(ir2[m]);
    const float c1 = -2.0f * 0.7978845608028654f * 1.4426950408889634f, c2 = c1 * 0.044715f;
    k1[m] = c1 * r; k2[m] = c2 * r * r * r;
  }
  const int P = pn >> 1, w = 2 * (pn & 1) + wn, mt0 = pm * 16 + 4 * wm;
  char* ab = ws + WS_ACT + (size_t)(7 * P) * 131072 + (size_t)mt0 * 1024;
  unsigned xb[4][2];
#pragma unroll
  for (int m = 0; m < 4; ++m) {
    unsigned o[3][2];
#pragma unroll
    for (int c = 0; c < 3; ++c) {
      const f32x4 a = acc[m][2 * c], b = acc[m][2 * c + 1];
      o[c][0] = geglu2(a.xy, b.xy, k1[m], k2[m], ir2[m]);
      o[c][1] = geglu2(a.zw, b.zw, k1[m], k2[m], ir2[m]);
    }
    st16(ab + (size_t)w * 131072 + m * 1024 + lane * 16, (u32x4){o[0][0], o[0][1], o[1][0], o[1][1]});
    xb[m][0] = o[2][0]; xb[m][1] = o[2][1];
  }
  const int hf = lane >> 5, fq1 = fq & 1;
#pragma unroll
  for (int mp = 0; mp < 2; ++mp) {
    swap32u(xb[2 * mp][0], xb[2 * mp + 1][0]); swap32u(xb[2 * mp][1], xb[2 * mp + 1][1]);
    const unsigned slot = (unsigned)((2 * (w & 1) + fq1) * 16 + fr) * 16 + (2 * mp + hf) * 1024;
    st16(ab + (size_t)(4 + (w >> 1)) * 131072 + slot, (u32x4){xb[2 * mp][0], xb[2 * mp][1], xb[2 * mp + 1][0], xb[2 * mp + 1][1]});
    f32x4 a = acc[2 * mp][6], b = acc[2 * mp + 1][6];
#pragma unroll
    for (int r = 0; r < 4; ++r) { float u = a[r], v = b[r]; swap32f(u, v); a[r] = u; b[r] = v; }
    const float q1 = hf ? k1[2 * mp + 1] : k1[2 * mp], q2 = hf ? k2[2 * mp + 1] : k2[2 * mp], qi = hf ? ir2[2 * mp + 1] : ir2[2 * mp];
    st8(ab + (size_t)6 * 131072 + slot + (w >> 1) * 8, (u32x2){geglu2(a.xy, b.xy, q1, q2, qi), geglu2(a.zw, b.zw, q1, q2, qi)});
  }
  BW_OUT(2);
  STAMP_END(2);
}

extern "C" __global__ void kz_pad() { asm volatile(".fill 4096, 4, 0xBF800000"); }

extern "C" void kernel_launch(void* const* d_in, const int* in_sizes, int n_in, void* d_out, int out_size, void* d_ws, size_t ws_size,
                              hipStream_t stream) {
  const u16* x = (const u16*)d_in[0];
  const u16* wqkv = (const u16*)d_in[1];
  const u16* wo = (const u16*)d_in[2];
  const u16* wg = (const u16*)d_in[4];
  const u16* wu = (const u16*)d_in[5];
  const u16* wd = (const u16*)d_in[6];
  char* ws = (char*)d_ws;
  u16* out = (u16*)d_out;
  k1_qkv_attn<<<256, 768, 0, stream>>>(x, wqkv, wo, wg, wu, ws);
  k2_wo<<<256, 576, 0, stream>>>(x, wo, ws);
  k3_gateup<<<256, 512, 0, stream>>>(wg, wu, wd, ws);
  k4_down<<<256, 576, 0, stream>>>(wd, ws, out);
}
```

```cpp
#include <hip/hip_runtime.h>
#include <hip/hip_bf16.h>
#include <stdint.h>

#pragma clang diagnostic ignored "-Winline-asm"
#pragma clang diagnostic ignored "-Wint-to-pointer-cast"

typedef unsigned short u16;
typedef __bf16 bf16x8 __attribute__((ext_vector_type(8)));
typedef __bf16 bf16x2 __attribute__((ext_vector_type(2)));
typedef short s16x4 __attribute__((ext_vector_type(4)));
typedef float f32x4 __attribute__((ext_vector_type(4)));
typedef float f32x2 __attribute__((ext_vector_type(2)));
typedef unsigned u32x4 __attribute__((ext_vector_type(4)));
typedef unsigned u32x2 __attribute__((ext_vector_type(2)));
#define LAS __attribute__((address_space(3)))
#define DI __device__ __forceinline__

constexpr int S_ = 2048, D_ = 1024, F_ = 3584;
constexpr float LN_EPS = 1e-5f;

constexpr size_t WS_O = 0;
constexpr size_t WS_HC = WS_O + (size_t)32 * 131072;
constexpr size_t WS_ACT = WS_HC + (size_t)33 * 131072;
constexpr size_t WS_MUX = WS_ACT + (size_t)112 * 131072;
constexpr size_t WS_MU = WS_MUX + 8192;
constexpr size_t WS_WTAB = WS_MU + 8192;
#define K4_XN 2
constexpr size_t WS_XCH = WS_WTAB + 8192;

DI u32x4 make_srd(const void* p, unsigned bytes) {
  uint64_t a = (uint64_t)p;
  u32x4 r; r.x = (unsigned)a; r.y = (unsigned)(a >> 32) & 0xffffu; r.z = bytes; r.w = 0x00020000u;
  return r;
}
DI void dma(unsigned lds, unsigned voff, u32x4 srd, unsigned soff) {
  asm volatile("s_mov_b32 m0, %0\n\ts_nop 0\n\tbuffer_load_dwordx4 %1, %2, %3 offen lds" ::"s"(lds), "v"(voff), "s"(srd), "s"(soff) : "m0");
}
DI void dma2(unsigned lds, unsigned so, unsigned voff, u32x4 srd, unsigned soff) {
  asm volatile("s_add_u32 m0, %0, %1\n\ts_nop 0\n\tbuffer_load_dwordx4 %2, %3, %4 offen lds" ::"s"(lds), "s"(so), "v"(voff), "s"(srd), "s"(soff) : "m0", "scc");
}
DI void dma_nt(unsigned lds, unsigned voff, u32x4 srd, unsigned soff) {
  asm volatile("s_mov_b32 m0, %0\n\ts_nop 0\n\tbuffer_load_dwordx4 %1, %2, %3 offen nt lds" ::"s"(lds), "v"(voff), "s"(srd), "s"(soff) : "m0");
}
#define dma1 dma
DI void touch_g(unsigned lds, unsigned voff, uint64_t base) {
  asm volatile("s_mov_b32 m0, %0\n\ts_nop 0\n\tglobal_load_lds_dword %1, %2" ::"s"(lds), "v"(voff), "s"(base) : "m0");
}
DI void touch_b(unsigned lds, unsigned voff, u32x4 srd, unsigned soff) {
  asm volatile("s_mov_b32 m0, %0\n\ts_nop 0\n\tbuffer_load_dword %1, %2, %3 offen lds" ::"s"(lds), "v"(voff), "s"(srd), "s"(soff) : "m0");
}
DI void touch_v(unsigned lds, uint64_t addr) {
  asm volatile("s_mov_b32 m0, %0\n\ts_nop 0\n\tglobal_load_lds_dword %1, off" ::"s"(lds), "v"(addr) : "m0");
}
#define CODE_ADDR(var, sym) uint64_t var; { int d_; asm volatile("s_getpc_b64 %0\n\ts_mov_b32 %1, " #sym "@rel32@lo+4" : "=s"(var), "=s"(d_)); var += (int64_t)d_; }
#define VADD(v, s) asm volatile("v_add_u32 %0, %1, %0" : "+v"(v) : "s"(s))
#define SB() __builtin_amdgcn_sched_barrier(0)
#define MEMFENCE() asm volatile("" ::: "memory")
#define WAIT_VL(V) do { SB(); MEMFENCE(); __builtin_amdgcn_s_waitcnt(((V) & 15) | (((V) >> 4) << 14) | 0x70); MEMFENCE(); SB(); } while (0)
#define WAIT_V(V) do { SB(); MEMFENCE(); __builtin_amdgcn_s_waitcnt(((V) & 15) | (((V) >> 4) << 14) | 0xF70); MEMFENCE(); SB(); } while (0)
#define WAIT_L0() do { SB(); MEMFENCE(); __builtin_amdgcn_s_waitcnt(0xC07F); MEMFENCE(); SB(); } while (0)
#define BARRIER() do { SB(); MEMFENCE(); __builtin_amdgcn_s_barrier(); MEMFENCE(); SB(); } while (0)
#define BW_DECL()
#define BW_OUT(kern)

DI u32x4 lds128(unsigned a) { return *(LAS u32x4*)(a); }
DI f32x4 lds128f(unsigned a) { return *(LAS f32x4*)(a); }
DI u32x2 ldstr(unsigned a) { return __builtin_bit_cast(u32x2, __builtin_amdgcn_ds_read_tr16_b64_v4i16((LAS s16x4*)(a))); }
DI f32x4 mfma(u32x4 a, u32x4 b, f32x4 c) {
  return __builtin_amdgcn_mfma_f32_16x16x32_bf16(__builtin_bit_cast(bf16x8, a), __builtin_bit_cast(bf16x8, b), c, 0, 0, 0);
}
DI u32x4 cat(u32x2 lo, u32x2 hi) { u32x4 r; r.x = lo.x; r.y = lo.y; r.z = hi.x; r.w = hi.y; return r; }
DI unsigned pk(float a, float b) { f32x2 v = {a, b}; return __builtin_bit_cast(unsigned, __builtin_convertvector(v, bf16x2)); }
DI float dot2(unsigned a, unsigned b, float c) {
  return __builtin_amdgcn_fdot2_f32_bf16(__builtin_bit_cast(bf16x2, a), __builtin_bit_cast(bf16x2, b), c, false);
}
DI float bflo(unsigned u) { return __uint_as_float(u << 16); }
DI float bfhi(unsigned u) { return __uint_as_float(u & 0xffff0000u); }
DI float red_add(float v) {
  auto a = __builtin_amdgcn_permlane32_swap(__float_as_uint(v), __float_as_uint(v), false, false);
  v = __uint_as_float(a[0]) + __uint_as_float(a[1]);
  auto b = __builtin_amdgcn_permlane16_swap(__float_as_uint(v), __float_as_uint(v), false, false);
  return __uint_as_float(b[0]) + __uint_as_float(b[1]);
}
DI float red_max(float v) {
  auto a = __builtin_amdgcn_permlane32_swap(__float_as_uint(v), __float_as_uint(v), false, false);
  v = fmaxf(__uint_as_float(a[0]), __uint_as_float(a[1]));
  auto b = __builtin_amdgcn_permlane16_swap(__float_as_uint(v), __float_as_uint(v), false, false);
  return fmaxf(__uint_as_float(b[0]), __uint_as_float(b[1]));
}
DI void swap32f(float& a, float& b) {
  auto r = __builtin_amdgcn_permlane32_swap(__float_as_uint(a), __float_as_uint(b), false, false);
  a = __uint_as_float(r[0]); b = __uint_as_float(r[1]);
}
DI void swap32u(unsigned& a, unsigned& b) { auto r = __builtin_amdgcn_permlane32_swap(a, b, false, false); a = r[0]; b = r[1]; }
DI void swap16u(unsigned& a, unsigned& b) { auto r = __builtin_amdgcn_permlane16_swap(a, b, false, false); a = r[0]; b = r[1]; }
constexpr unsigned ONES2 = 0x3F803F80u;
#define STMODE 3
DI void st16(void* p, u32x4 v) {
  asm volatile("global_store_dwordx4 %0, %1, off sc1\n\ts_nop 1" :: "v"(p), "v"(v) : "memory");
}
DI void st8(void* p, u32x2 v) {
  asm volatile("global_store_dwordx2 %0, %1, off sc1" :: "v"(p), "v"(v) : "memory");
}
#define STAMP_AT(kern, i)
#define STAMP_RT(kern, i)
#define STAMP_L0()
#define STAMP_L1(kern)
#define STAMP_END(kern)
#define STAMP_BEG(kern)

extern "C" __global__ void k1_qkv_attn(const u16*, const u16*, const u16*, const u16*, const u16*, char*);
extern "C" __global__ void k2_wo(const u16*, const u16*, char*);
extern "C" __global__ void k3_gateup(const u16*, const u16*, const u16*, char*);
extern "C" __global__ void k4_down(const u16*, char*, u16*);

constexpr int K1_STAGE = 43008, K1_B = 18432, K1_ST = 3 * K1_STAGE;
constexpr int K1_SCR = K1_ST + 144 * 8, K1_LDS = K1_SCR + 256;
#define PFD 3
struct K1Frag { u32x4 a[2]; u32x4 h; u32x2 b[6][2]; };

extern "C" __global__ __launch_bounds__(768, 3) void k1_qkv_attn(const u16* __restrict__ x, const u16* __restrict__ wqkv, const u16* __restrict__ wo,
                                                                 const u16* __restrict__ wg, const u16* __restrict__ wu, char* __restrict__ ws) {
  __shared__ __attribute__((aligned(1024))) char smem[K1_LDS];
  const unsigned L0 = (unsigned)(size_t)(LAS char*)smem;
  const int tid = threadIdx.x, lane = tid & 63;
  const int wid = __builtin_amdgcn_readfirstlane(tid >> 6);
  BW_DECL();
  const int xcd = blockIdx.x & 7, idx = blockIdx.x >> 3;
  const int pm = (xcd >> 1) * 4 + (idx >> 3), pn = (xcd & 1) * 8 + (idx & 7);
  const int s0 = pm * 128;
  const int l3 = lane >> 3, l7 = lane & 7;

  if (wid >= 8) {
    const int j = wid - 8;
    __builtin_amdgcn_s_setprio(3);
    const unsigned vline = lane * 128;
    CODE_ADDR(self, k1_qkv_attn);
    if (j >= 2) touch_g(L0 + K1_SCR, vline, self + (j - 2) * 8192);
    const u32x4 srdX = make_srd(x, S_ * D_ * 2);
    const u32x4 srdW = make_srd(wqkv, D_ * 3 * D_ * 2);
    unsigned voffA[2], voffB[2];
#pragma unroll
    for (int i = 0; i < 2; ++i) {
      voffA[i] = (unsigned)(s0 + 32 * j + 8 * i + l3) * 2048 + (unsigned)(l7 ^ (4 * i + (lane >> 4))) * 16;
      voffB[i] = (unsigned)(16 * j + 8 * i + l3) * 6144 + pn * 128 + (unsigned)(l7 ^ ((((lane >> 4) & 1) | (i << 1)) << 1)) * 16;
    }
    unsigned voffE;
    if (j < 2) {
      const int r = s0 - 16 + 8 * j + l3;
      voffE = (unsigned)(r < 0 ? 0 : r) * 2048 + (unsigned)(l7 ^ (4 * j + (lane >> 4))) * 16;
      if (j == 0 && lane < 24) *(LAS u32x4*)(L0 + l3 * K1_STAGE + 16384 + l7 * 16) = (u32x4){ONES2, ONES2, ONES2, ONES2};
    } else {
      voffE = j == 2 ? (blockIdx.x * 8 + l7) * 2048 + PFD * 128 : PFD * (64 * 6144) + (blockIdx.x * 12 + (lane < 12 ? lane : 11)) * 128;
      const int q = idx * 6 + (lane & 7), ps = (xcd & 1) * 8 + ((3 - (int)((uint64_t)wqkv >> 7)) & 7);
      if (j == 3 && lane >= 16 && lane < 22) voffE = PFD * (64 * 6144) + (q / 3) * 6144 + (q % 3) * 2048 + ps * 128;
    }
    const unsigned long long emask = j == 0 ? ~0xFFull : ~0ull;
    CODE_ADDR(nextk, k2_wo);
    unsigned so = 0;
#pragma nounroll
    for (int kt = 0; kt < 16; ++kt) {
      if (j < 2) asm volatile("s_mov_b32 m0, %0\n\ts_mov_b64 exec, %4\n\tbuffer_load_dwordx4 %1, %2, %3 offen lds\n\ts_mov_b64 exec, -1"
                              ::"s"(L0 + so + 16384 + j * 1024), "v"(voffE), "s"(srdX), "s"(kt * 128), "s"(emask) : "m0");
      else if (kt >= 16 - PFD) touch_g(L0 + K1_SCR, vline, j == 2 ? self + 2048 : nextk);
      else touch_b(L0 + K1_SCR, voffE, j == 2 ? srdX : srdW, j == 2 ? kt * 128 : kt * (64 * 6144));
#pragma unroll
      for (int i = 0; i < 4; ++i) dma(L0 + so + j * 4096 + i * 1024, voffA[i & 1], srdX, kt * 128 + (i >> 1) * 32768);
#pragma unroll
      for (int i = 0; i < 6; ++i) dma(L0 + so + K1_B + (i >> 1) * 8192 + j * 2048 + (i & 1) * 1024, voffB[i & 1], srdW, kt * (64 * 6144) + (i >> 1) * 2048);
      so = so == 2 * K1_STAGE ? 0 : so + K1_STAGE;
      if (kt > 0) { WAIT_VL(11); BARRIER(); }
    }
    u32x4 hv[8];
    {
      const int r = s0 - 16 + 4 * j + (lane >> 4);
      const u16* p = x + (size_t)(r < 0 ? 0 : r) * D_ + (lane & 15) * 8;
#pragma unroll
      for (int i = 0; i < 8; ++i) asm volatile("global_load_dwordx4 %0, %1, off offset:%2" : "=v"(hv[i]) : "v"(p), "n"(i * 256));
    }
    WAIT_V(8); BARRIER();
    u32x4 wrow0, wrow1;
    {
      const u16* p = wo + (size_t)(blockIdx.x * 4 + j) * D_ + lane * 8;
      asm volatile("global_load_dwordx4 %0, %1, off" : "=v"(wrow0) : "v"(p));
      asm volatile("global_load_dwordx4 %0, %1, off offset:1024" : "=v"(wrow1) : "v"(p));
      const u32x4 srdGU = make_srd((j & 1) ? wu : wg, D_ * F_ * 2);
      touch_b(L0 + K1_SCR, vline, srdGU, ((j >> 1) * 256 + blockIdx.x) * 8192);
    }
    BARRIER();
    WAIT_V(3);
    {
      float a[2] = {0.f, 0.f}, q[2] = {0.f, 0.f};
#pragma unroll
      for (int i = 0; i < 8; ++i) {
        asm volatile("" : "+v"(hv[i]));
#pragma unroll
        for (int e = 0; e < 4; ++e) { a[e & 1] = dot2(hv[i][e], ONES2, a[e & 1]); q[e & 1] = dot2(hv[i][e], hv[i][e], q[e & 1]); }
      }
      float sa = a[0] + a[1], sq = q[0] + q[1];
#pragma unroll
      for (int o = 1; o < 16; o <<= 1) { sa += __shfl_xor(sa, o); sq += __shfl_xor(sq, o); }
      const float mean = sa * (1.0f / D_), var = fmaxf(sq * (1.0f / D_) - mean * mean, 0.f);
      if ((lane & 15) == 0) *(LAS f32x2*)(L0 + K1_ST + (4 * j + (lane >> 4)) * 8) = (f32x2){mean, __builtin_amdgcn_rsqf(var + LN_EPS)};
    }
    WAIT_L0(); BARRIER();
    BARRIER();
    WAIT_V(1);
    asm volatile("" : "+v"(wrow0), "+v"(wrow1));
    float s = 0.f;
#pragma unroll
    for (int e = 0; e < 4; ++e) { s = dot2(wrow0[e], ONES2, s); s = dot2(wrow1[e], ONES2, s); }
#pragma unroll
    for (int o = 1; o < 16; o <<= 1) s += __shfl_xor(s, o);
    s = red_add(s) * (1.0f / D_);
    const unsigned hi = pk(s, 0.f) & 0xffffu;
    const unsigned lo = pk(s - bflo(hi), 0.f) & 0xffffu;
    const int k = blockIdx.x * 4 + j, dd = k & 31;
    u16* t = (u16*)(ws + WS_WTAB) + (k >> 5) * 96 + ((dd >> 2) & 3) * 24 + 4 * (dd >> 4) + (dd & 3);
    if (lane < 3) t[lane * 8] = (u16)(lane == 0 ? hi : lane == 1 ? lo : 0u);
    BW_OUT(0);
    return;
  }

  const int g = wid & 3, hh = wid >> 2, fr = lane & 15, fq = lane >> 4;
  STAMP_BEG(0);
  unsigned ra[2][2], rh[2], rb[6];
#pragma unroll
  for (int ks = 0; ks < 2; ++ks) {
    const unsigned c = (unsigned)((4 * ks + fq) ^ (fr >> 1)) * 16;
#pragma unroll
    for (int m = 0; m < 2; ++m) ra[m][ks] = L0 + (unsigned)(16 * (2 * g + (m ^ hh)) + fr) * 128 + c;
    rh[ks] = L0 + 16384 + fr * 128 + c;
  }
  {
    const int q = fr >> 2, p = lane & 3, f = ((q >> 1) & 1) | ((fq & 1) << 1);
#pragma unroll
    for (int j = 0; j < 6; ++j) {
      const int a = j < 4 ? ((j + g) & 3) : 4 + ((j + g) & 1);
      rb[j] = L0 + K1_B + (a >> 1) * 8192 + (unsigned)(8 * fq + q) * 128 + (unsigned)((2 * hh + (a & 1)) ^ f) * 32 + 8 * p;
    }
  }
  f32x4 acc[2][6], accHT = {0, 0, 0, 0}, accHV = {0, 0, 0, 0}, accS = {0, 0, 0, 0}, accQ = {0, 0, 0, 0};
  u32x4 onesf = {ONES2, ONES2, ONES2, ONES2};
  asm volatile("" : "+v"(onesf));
#pragma unroll
  for (int m = 0; m < 2; ++m)
#pragma unroll
    for (int j = 0; j < 6; ++j) acc[m][j] = (f32x4){0, 0, 0, 0};
  K1Frag F0, F1;
  auto rd = [&](K1Frag& F, int i, int ks) {
    if (i < 2) F.a[i] = lds128(ra[i][ks]);
    else if (i == 2) F.h = lds128(rh[ks]);
    else { const int e = i - 3, j = e >> 1, t = e & 1; F.b[j][t] = ldstr(rb[j] + ks * 4096 + t * 512); }
  };
  auto mm = [&](K1Frag& F, int i) {
    if (i < 12) {
      const int j = i >> 1, m = i & 1;
      const u32x4 b = cat(F.b[j][0], F.b[j][1]);
      if (j < 4) acc[m][j] = mfma(b, F.a[m], acc[m][j]); else acc[m][j] = mfma(F.a[m], b, acc[m][j]);
    } else if (i == 12) accHT = mfma(cat(F.b[0][0], F.b[0][1]), F.h, accHT);
    else if (i == 13) accS = mfma(onesf, F.a[0], accS);
    else if (i == 14) accQ = mfma(F.a[0], F.a[0], accQ);
    else accHV = mfma(F.h, cat(F.b[4][0], F.b[4][1]), accHV);
  };

  BARRIER();
#pragma unroll
  for (int i = 0; i < 15; ++i) rd(F0, i, 0);
  WAIT_L0();
  STAMP_L0();
  unsigned sidx = 0;
  for (int kt = 0; kt < 16; ++kt) {
    const unsigned dl = sidx == 2 ? (unsigned)(-2 * K1_STAGE) : (unsigned)K1_STAGE;
    sidx = sidx == 2 ? 0 : sidx + 1;
#pragma unroll
    for (int i = 0; i < 16; ++i) {
      mm(F0, i);
      if (i < 7) { rd(F1, 2 * i, 1); rd(F1, 2 * i + 1, 1); }
      if (i == 5) VADD(ra[0][0], dl);
      if (i == 6) VADD(ra[1][0], dl);
      if (i == 7) rd(F1, 14, 1);
      if (i >= 8 && i < 14) VADD(rb[i - 8], dl);
      if (i == 14) VADD(rh[0], dl);
      SB();
    }
    WAIT_L0(); BARRIER();
#pragma unroll
    for (int i = 0; i < 16; ++i) {
      mm(F1, i);
      if (i < 7) { rd(F0, 2 * i, 0); rd(F0, 2 * i + 1, 0); }
      if (i == 7) rd(F0, 14, 0);
      if (i == 9) VADD(ra[0][1], dl);
      if (i == 11) VADD(ra[1][1], dl);
      if (i == 13) VADD(rh[1], dl);
      SB();
    }
    WAIT_L0();
  }
  STAMP_L1(0);

  constexpr unsigned X_OFF = 0;
  constexpr unsigned CT = 2 * K1_STAGE;
  constexpr unsigned CV = CT + 512;
  {
    if (fr == 0) *(LAS f32x4*)(L0 + CT + (hh * 4 + g) * 64 + fq * 16) = accHT;
    if (fq == 0) *(LAS float*)(L0 + CV + (hh * 2 + (g & 1)) * 64 + fr * 4) = accHV[0];
    const int r = fr & 3;
    const float dq = r == 0 ? accQ[0] : r == 1 ? accQ[1] : r == 2 ? accQ[2] : accQ[3];
    const float mean = accS[0] * (1.0f / D_), var = fmaxf(dq * (1.0f / D_) - mean * mean, 0.f);
    const int tl = 16 * (2 * g + hh) + fr;
    if (fq == (fr >> 2)) {
      *(LAS f32x2*)(L0 + K1_ST + (16 + tl) * 8) = (f32x2){mean, __builtin_amdgcn_rsqf(var + LN_EPS)};
      if (pn == 0) ((float*)(ws + WS_MUX))[s0 + tl] = mean;
    }
  }
  WAIT_L0(); BARRIER();
  {
    const unsigned xb = L0 + X_OFF + hh * (9 * 3072) + lane * 16;
    f32x4 ct[4]; float cv[2];
#pragma unroll
    for (int j = 0; j < 4; ++j) ct[j] = lds128f(L0 + CT + (hh * 4 + ((j + g) & 3)) * 64 + fq * 16);
#pragma unroll
    for (int j = 0; j < 2; ++j) cv[j] = *(LAS float*)(L0 + CV + (hh * 2 + ((j + g) & 1)) * 64 + fr * 4);
#pragma unroll
    for (int m = 0; m < 3; ++m) {
      const int ti = m == 2 ? 0 : 2 * g + (m ^ hh) + 1;
      f32x2 st = *(LAS f32x2*)(L0 + K1_ST + (16 * ti + fr) * 8);
      f32x4 s01 = lds128f(L0 + K1_ST + (16 * ti + 4 * fq) * 8), s23 = lds128f(L0 + K1_ST + (16 * ti + 4 * fq + 2) * 8);
#pragma unroll
      for (int j = 0; j < (m == 2 ? 1 : 4); ++j) {
        const f32x4 a = m == 2 ? accHT : acc[m & 1][j];
        const f32x4 v = (a - ct[j] * st.x) * st.y;
        const int at = (j + g) & 3;
        u32x2 w; w.x = pk(v[0], v[1]); w.y = pk(v[2], v[3]);
        *(LAS u32x2*)(xb + ti * 3072 + (at >> 1) * 1024 + (at & 1) * 8) = w;
      }
#pragma unroll
      for (int j = 0; j < (m == 2 ? 1 : 2); ++j) {
        const f32x4 a = m == 2 ? accHV : acc[m & 1][4 + j];
        u32x2 w;
        w.x = pk((a[0] - cv[j] * s01[0]) * s01[1], (a[1] - cv[j] * s01[2]) * s01[3]);
        w.y = pk((a[2] - cv[j] * s23[0]) * s23[1], (a[3] - cv[j] * s23[2]) * s23[3]);
        *(LAS u32x2*)(xb + ti * 3072 + 2048 + ((j + g) & 1) * 8) = w;
      }
    }
  }
  WAIT_L0(); BARRIER();
  {
    const unsigned xb = L0 + X_OFF + hh * (9 * 3072) + 2 * g * 3072 + lane * 16;
    u32x4 kf[3], vf[3], qf[2];
#pragma unroll
    for (int t = 0; t < 3; ++t) { kf[t] = lds128(xb + t * 3072 + 1024); vf[t] = lds128(xb + t * 3072 + 2048); }
#pragma unroll
    for (int t = 0; t < 2; ++t) qf[t] = lds128(xb + (t + 1) * 3072);
    const float c = 0.17677669529663687f * 1.4426950408889634f;
    char* of = ws + WS_O + (size_t)(2 * pn + hh) * 131072 + (size_t)(pm * 8 + 2 * g) * 1024 + lane * 16;
#pragma unroll
    for (int m = 0; m < 2; ++m) {
      const f32x4 z = {0, 0, 0, 0};
      f32x4 sp = mfma(kf[m], qf[m], z), sc = mfma(kf[m + 1], qf[m], z);
      const bool pok = (m == 1) || (pm | g) != 0;
      float s[8];
#pragma unroll
      for (int r = 0; r < 4; ++r) {
        const int d = fr - 4 * fq - r;
        s[r] = (pok && ((d & 1) == 0) && d <= -2) ? sp[r] : -INFINITY;
        s[4 + r] = (((d & 1) == 0) && d >= 0 && d <= 14) ? sc[r] : -INFINITY;
      }
      float mx = fmaxf(fmaxf(fmaxf(s[0], s[1]), fmaxf(s[2], s[3])), fmaxf(fmaxf(s[4], s[5]), fmaxf(s[6], s[7])));
      mx = red_max(mx);
      const float mc = mx * c;
      float sum = 0.f;
#pragma unroll
      for (int e = 0; e < 8; ++e) { s[e] = __builtin_amdgcn_exp2f(s[e] * c - mc); sum += s[e]; }
      sum = red_add(sum);
      const float inv = __builtin_amdgcn_rcpf(sum);
      u32x4 p;
#pragma unroll
      for (int e = 0; e < 4; ++e) p[e] = pk(s[2 * e] * inv, s[2 * e + 1] * inv);
      u32x4 va, vb;
      va.x = vf[m].x; va.y = vf[m].y; va.z = vf[m + 1].x; va.w = vf[m + 1].y;
      vb.x = vf[m].z; vb.y = vf[m].w; vb.z = vf[m + 1].z; vb.w = vf[m + 1].w;
      const f32x4 oa = mfma(va, p, z), ob = mfma(vb, p, z);
      u32x4 w; w.x = pk(oa[0], oa[1]); w.y = pk(oa[2], oa[3]); w.z = pk(ob[0], ob[1]); w.w = pk(ob[2], ob[3]);
      st16(of + m * 1024, w);
    }
  }
  BW_OUT(0);
  STAMP_END(0);
}

constexpr int K2_SLOT = 24576, K2_NS = 6, K2_TAB = K2_SLOT * K2_NS, K2_SCR = K2_TAB + 8192, K2_LDS = K2_SCR + 256;
struct K2Frag { u32x4 a[2]; u32x2 b[4][2]; };

template <int MODE>
DI void proj_body(const u16* __restrict__ x, const u16* __restrict__ wmat, char* __restrict__ ws, u16* __restrict__ out, const unsigned L0,
                  const uint64_t nextk) {
  constexpr int NKS = MODE == 0 ? 32 : 112, NIT = NKS / 2;
  const int tid = threadIdx.x, lane = tid & 63;
  const int wid = __builtin_amdgcn_readfirstlane(tid >> 6);
  BW_DECL();
  const int g = wid & 3, fr = lane & 15, fq = lane >> 4;
  const int xcd = blockIdx.x & 7, idx = blockIdx.x >> 3;
#define MAP24 0
  const int pm = (xcd >> 1) * 4 + (idx >> 3), pn = (xcd & 1) * 8 + (idx & 7);

  if (wid >= 4) {
    const int ga = wid & 1;
    int kP = 0, kk = wid == 8 ? 0 : ga;
    auto rowbase = [&](int ks, int j) -> unsigned {
      if (MODE == 0) return ks * 32 + 8 * j;
      const int a = 56 * kk + 8 * j, b = 112 * kk + 56 * (j & 1) + 8 * (j >> 1) - 416, c = 56 * j + 48;
      int r = kk < 4 ? a : b;
      r = kk == 6 ? c : r;
      return kP + r;
    };
    auto adv = [&](int n) { const int p2 = kP + 224, k1 = kk + n, k2 = k1 - 7; kP = k1 >= 7 ? p2 : kP; kk = k1 >= 7 ? k2 : k1; };
    const u32x4 srdA = make_srd(ws + (MODE == 0 ? WS_O : WS_ACT), NKS * 131072);
    const u32x4 srdB = make_srd(wmat, NKS * 32 * 2048);
    const unsigned voffA = lane * 16 + pm * 8192;
    const unsigned voffB = (unsigned)(lane >> 3) * 2048 + pn * 128 + (unsigned)((lane & 7) ^ (((lane >> 4) & 3) << 1)) * 16;
    const int ml = idx & 7, jl = idx >> 3;
    unsigned so = 0;
    if (wid == 8) {
      if (MODE == 0) {
        const u32x4 srdT = make_srd(ws + WS_WTAB, 6144);
#pragma unroll
        for (int q = 0; q < 6; ++q) dma(L0 + K2_TAB + q * 1024, lane * 16, srdT, q * 1024);
      }
      constexpr int SR = MODE == 0 ? 32 : 56;
      const unsigned voffS = (unsigned)(SR * idx + (lane < SR ? lane : SR - 1)) * 2048 + ((xcd & 1) * 8 + ((3 - (int)((uint64_t)wmat >> 7)) & 7)) * 128;
#pragma nounroll
      for (int it = 0; it < NIT + 2; ++it) {
        if (it == 2 || (MODE == 1 && it == 26)) touch_b(L0 + K2_SCR, voffS, srdB, it == 2 ? 0 : 1792 * 2048);
        if (it < NIT) {
#pragma unroll
          for (int u = 0; u < 2; ++u) {
            dma(L0 + so + u * 8192 + ml * 1024, voffA, srdA, (2 * it + u) * 131072 + ml * 1024);
            dma(L0 + so + 16384 + u * 4096 + jl * 1024, voffB, srdB, rowbase(2 * it + u, jl) * 2048);
            adv(1);
          }
          so = so == (K2_NS - 1) * K2_SLOT ? 0 : so + K2_SLOT;
          if (it < 4) continue;
          WAIT_V(16);
        } else if (it == NIT) WAIT_V(12);
        else WAIT_V(8);
        BARRIER();
      }
      WAIT_V(4); BARRIER();
      WAIT_V(0); BARRIER();
      if (MODE == 0) { touch_g(L0 + K2_SCR, lane * 128, nextk); touch_g(L0 + K2_SCR, lane * 128, nextk + 8192); }
      BW_OUT(1 + 2 * MODE);
      return;
    }
    unsigned mo[5];
#pragma unroll
    for (int q = 0; q < 5; ++q) mo[q] = ((ml + 1 + q + (wid >= 6 ? 5 : 0)) & 7) * 1024;
#pragma nounroll
    for (int it = 0; it < NIT; ++it) {
      const unsigned la = L0 + so + ga * 8192, sa = (2 * it + ga) * 131072;
      dma(la + mo[0], voffA, srdA, sa + mo[0]);
      dma(la + mo[1], voffA, srdA, sa + mo[1]);
      if (wid < 6) {
#pragma unroll
        for (int q = 2; q < 5; ++q) dma(la + mo[q], voffA, srdA, sa + mo[q]);
      } else {
#pragma unroll
        for (int q = 1; q < 4; ++q) { const int j = (jl + q) & 3; dma(L0 + so + 16384 + ga * 4096 + j * 1024, voffB, srdB, rowbase(2 * it + ga, j) * 2048); }
        adv(2);
      }
      so = so == (K2_NS - 1) * K2_SLOT ? 0 : so + K2_SLOT;
      if (it > 0) { WAIT_V(5); BARRIER(); }
    }
    WAIT_V(0); BARRIER();
    BW_OUT(1 + 2 * MODE);
    return;
  }

  STAMP_BEG(1 + 2 * MODE);
  const int tok = pm * 128 + 32 * g + fr;
  u32x2 rx[2][4]; u32x4 rh[2][2]; float rmu[2];
#pragma unroll
  for (int m = 0; m < 2; ++m) {
    if (MODE == 0) {
      const u16* p = x + (size_t)(tok + 16 * m) * D_ + pn * 64 + 4 * fq;
      asm volatile("global_load_dwordx2 %0, %1, off" : "=v"(rx[m][0]) : "v"(p));
      asm volatile("global_load_dwordx2 %0, %1, off offset:32" : "=v"(rx[m][1]) : "v"(p));
      asm volatile("global_load_dwordx2 %0, %1, off offset:64" : "=v"(rx[m][2]) : "v"(p));
      asm volatile("global_load_dwordx2 %0, %1, off offset:96" : "=v"(rx[m][3]) : "v"(p));
    } else {
      const char* p = ws + WS_HC + (size_t)(2 * pn) * 131072 + (size_t)(pm * 8 + 2 * g + m) * 1024 + lane * 16;
      const char* p1 = p + 131072;
      asm volatile("global_load_dwordx4 %0, %1, off" : "=v"(rh[m][0]) : "v"(p));
      asm volatile("global_load_dwordx4 %0, %1, off" : "=v"(rh[m][1]) : "v"(p1));
    }
    const float* q = (const float*)(ws + (MODE == 0 ? WS_MUX : WS_MU)) + tok + 16 * m;
    asm volatile("global_load_dword %0, %1, off" : "=v"(rmu[m]) : "v"(q));
  }

  unsigned ra = L0 + g * 2048 + lane * 16;
  unsigned rb[4];
  {
    const int q = fr >> 2, p = lane & 3, sw = (2 * fq + (q >> 1)) & 3;
#pragma unroll
    for (int n = 0; n < 4; ++n) rb[n] = L0 + 16384 + (unsigned)(4 * fq + q) * 128 + (unsigned)(n ^ sw) * 32 + 8 * p;
  }
  unsigned rw = L0 + K2_TAB + fq * 48 + (fr < 2 ? fr : 2) * 16;

  f32x4 acc[2][4], accW[2];
#pragma unroll
  for (int m = 0; m < 2; ++m) {
    accW[m] = (f32x4){0, 0, 0, 0};
#pragma unroll
    for (int n = 0; n < 4; ++n) acc[m][n] = (f32x4){0, 0, 0, 0};
  }
  K2Frag F0[2], F1[2];
  constexpr int NRD = 10;
  auto rd = [&](K2Frag (&F)[2], int i) {
    const int ga = i / NRD, k = i % NRD;
    if (k < 2) F[ga].a[k] = lds128(ra + ga * 8192 + k * 1024);
    else { const int e = k - 2; F[ga].b[e >> 1][e & 1] = ldstr(rb[e >> 1] + ga * 4096 + (e & 1) * 2048); }
  };
  auto bump = [&](int i, unsigned dl) {
    if (i == 0) VADD(ra, dl);
    else if (i < 5) VADD(rb[i - 1], dl);
    else if (MODE == 0) { const unsigned c = 384; VADD(rw, c); }
  };
  auto step = [&](K2Frag (&F)[2], K2Frag (&N)[2], unsigned dl) {
    u32x4 wf[2];
    if (MODE == 0) { wf[0] = lds128(rw); wf[1] = lds128(rw + 192); }
#pragma unroll
    for (int i = 0; i < 16; ++i) {
      const int ga = i >> 3, n = (i >> 1) & 3, m = i & 1;
      acc[m][n] = mfma(cat(F[ga].b[n][0], F[ga].b[n][1]), F[ga].a[m], acc[m][n]);
      if (i < NRD) { rd(N, 2 * i); rd(N, 2 * i + 1); }
      if (i >= 13) { bump(2 * (i - 13), dl); bump(2 * (i - 13) + 1, dl); }
      SB();
    }
    if (MODE == 0) {
#pragma unroll
      for (int i = 0; i < 4; ++i) accW[i & 1] = mfma(wf[i >> 1], F[i >> 1].a[i & 1], accW[i & 1]);
      SB();
    }
  };

  BARRIER();
#pragma unroll
  for (int i = 0; i < 2 * NRD; ++i) rd(F0, i);
#pragma unroll
  for (int i = 0; i < 5; ++i) bump(i, K2_SLOT);
  WAIT_L0(); BARRIER();
  STAMP_L0();
  int sidx = 1;
  for (int it = 0; it < NIT - 2; it += 2) {
    step(F0, F1, sidx == 5 ? (unsigned)(-5 * K2_SLOT) : (unsigned)K2_SLOT);
    WAIT_L0(); BARRIER();
    step(F1, F0, K2_SLOT);
    WAIT_L0(); BARRIER();
    sidx = sidx == 5 ? 1 : sidx + 2;
  }
  step(F0, F1, 0);
  step(F1, F0, 0);
  WAIT_VL(0);
  STAMP_L1(1 + 2 * MODE);

#pragma unroll
  for (int m = 0; m < 2; ++m) {
    if (MODE == 0) {
      asm volatile("" : "+v"(rx[m][0]), "+v"(rx[m][1]), "+v"(rx[m][2]), "+v"(rx[m][3]), "+v"(rmu[m]));
      const float mu = rmu[m] + red_add(accW[m][0] + accW[m][1]);
      unsigned hp[4][2]; float ss = 0.f;
#pragma unroll
      for (int n = 0; n < 4; ++n) {
        hp[n][0] = pk(acc[m][n][0] + bflo(rx[m][n].x) - mu, acc[m][n][1] + bfhi(rx[m][n].x) - mu);
        hp[n][1] = pk(acc[m][n][2] + bflo(rx[m][n].y) - mu, acc[m][n][3] + bfhi(rx[m][n].y) - mu);
        ss = dot2(hp[n][0], hp[n][0], ss); ss = dot2(hp[n][1], hp[n][1], ss);
      }
      char* hb = ws + WS_HC + (size_t)(2 * pn) * 131072 + (size_t)(pm * 8 + 2 * g + m) * 1024 + lane * 16;
      st16(hb, (u32x4){hp[0][0], hp[0][1], hp[1][0], hp[1][1]});
      st16(hb + 131072, (u32x4){hp[2][0], hp[2][1], hp[3][0], hp[3][1]});
      ss = red_add(ss);
      if (fq == 0) {
        ((float*)(ws + WS_HC + (size_t)32 * 131072))[(tok + 16 * m) * 16 + pn] = ss;
        if (pn == 0) ((float*)(ws + WS_MU))[tok + 16 * m] = mu;
      }
    } else {
      asm volatile("" : "+v"(rh[m][0]), "+v"(rh[m][1]), "+v"(rmu[m]));
      u16* op = out + (size_t)(tok + 16 * m) * D_ + pn * 64 + (fq & 1) * 16 + (fq >> 1) * 8;
      const float mu = rmu[m];
#pragma unroll
      for (int c = 0; c < 2; ++c) {
        const u32x4 h = rh[m][c];
        const f32x4 y0 = acc[m][2 * c], y1 = acc[m][2 * c + 1];
        unsigned a0 = pk(y0[0] + (bflo(h.x) + mu), y0[1] + (bfhi(h.x) + mu));
        unsigned a1 = pk(y0[2] + (bflo(h.y) + mu), y0[3] + (bfhi(h.y) + mu));
        unsigned b0 = pk(y1[0] + (bflo(h.z) + mu), y1[1] + (bfhi(h.z) + mu));
        unsigned b1 = pk(y1[2] + (bflo(h.w) + mu), y1[3] + (bfhi(h.w) + mu));
        swap16u(a0, b0); swap16u(a1, b1);
        *(u32x4*)(op + c * 32) = (u32x4){a0, a1, b0, b1};
      }
    }
  }
  BW_OUT(1 + 2 * MODE);
  STAMP_END(1 + 2 * MODE);
}

extern "C" __global__ __launch_bounds__(576, 3) void k2_wo(const u16* __restrict__ x, const u16* __restrict__ wo, char* __restrict__ ws) {
  __shared__ __attribute__((aligned(1024))) char smem[K2_LDS];
  CODE_ADDR(nextk, k3_gateup);
  proj_body<0>(x, wo, ws, nullptr, (unsigned)(size_t)(LAS char*)smem, nextk);
}
#define K4_LAG 2
#define K4_LEAD 4
#define K4_BLEAD 0
#define K4_SLEEP 2
#define K4_DLY 0
#define K4_S 52
#define K4_POLL 2
template <int V> struct IC { static constexpr int value = V; };
template <int I, int E, class Fn> DI void sfor(Fn&& f) { if constexpr (I < E) { f(IC<I>{}); sfor<I + 1, E>(f); } }
constexpr int K4_SLOT = 16384, K4_NS = 8, K4_SCR = K4_SLOT * K4_NS, K4_LDS = K4_SCR + 256, K4_R = 112 - K4_S;
static_assert(K4_S >= 50 && K4_S <= 54 && K4_S % 2 == 0, "");
struct K4Frag { u32x4 a[4]; u32x2 b[2][2]; };

extern "C" __global__ __launch_bounds__(832, 4) void k4_down(const u16* __restrict__ wd, char* __restrict__ ws, u16* __restrict__ out) {
  __shared__ __attribute__((aligned(1024))) char smem[K4_LDS];
  const unsigned L0 = (unsigned)(size_t)(LAS char*)smem;
  const int tid = threadIdx.x, lane = tid & 63;
  const int wid = __builtin_amdgcn_readfirstlane(tid >> 6);
  BW_DECL();
  const int xcd = blockIdx.x & 7, idx = blockIdx.x >> 3;
  const int kh = xcd & 1, pm = (xcd >> 2) * 8 + (idx >> 2), pq = ((xcd >> 1) & 1) * 4 + (idx & 3);

  if (wid >= 8) {
    const int j = wid - 8, ca = idx & 3, cb = idx >> 2;
    const u32x4 srdA = make_srd(ws + WS_ACT, 112 * 131072), srdB = make_srd(wd, F_ * D_ * 2);
    const unsigned voffA = lane * 16 + pm * 8192;
    const unsigned voffB = (unsigned)(lane >> 3) * 2048 + pq * 256 + (unsigned)((lane & 7) ^ (((lane >> 4) & 3) << 1)) * 16;
    auto run = [&](auto na, auto pa, auto nb, auto lag, int qa, int qb) {
      constexpr int NA = decltype(na)::value, PA = decltype(pa)::value, NB = decltype(nb)::value, LG = decltype(lag)::value;
      constexpr int NF = NA + NB, NO = (NA + 1 - PA) / 2 + NB;
      static_assert(LG <= 4, "");
      unsigned va[NA + 1], la[NA + 1], vb[3][NB + 1], lb[NB + 1];
#pragma unroll
      for (int i = 0; i < NA; ++i) { const int q = (qa + i) & 7; va[i] = voffA + (q ^ kh) * 1024; la[i] = L0 + q * 1024; }
#pragma unroll
      for (int i = 0; i < NB; ++i) {
        const int q = (qb + i) & 7, r = q & 3;
        const unsigned v = voffB + (q >> 2) * 128;
        vb[0][i] = v + 8 * r * 2048; vb[1][i] = v + (56 * (r & 1) + 8 * (r >> 1)) * 2048; vb[2][i] = v + 56 * r * 2048;
        lb[i] = L0 + 8192 + q * 1024;
      }
      unsigned so = 0, sa = kh * (63 * 131072), sb = kh * (9 * 224 * 2048);
      auto slot = [&](auto tt, int kk) {
        constexpr int t = decltype(tt)::value;
        if (t < K4_R) {
#pragma unroll
          for (int i = 0; i < NA; ++i) if (t < K4_S || ((PA + i) & 1) == 0) dma2(la[i], so, va[i], srdA, sa);
          const unsigned sr = sb + (kk < 4 ? 56 * kk : kk < 6 ? 112 * kk - 416 : 48) * 2048;
#pragma unroll
          for (int i = 0; i < NB; ++i) dma2(lb[i], so, vb[kk < 4 ? 0 : kk < 6 ? 1 : 2][i], srdB, sr);
          so = (so + K4_SLOT) & (K4_NS * K4_SLOT - 1); sa += 131072;
          if (kk == 6) sb += 224 * 2048;
        }
        if (t >= LG && (t - LG) % 2 == 0) {
          constexpr int lo = t - LG + 1, hi = t < K4_R ? t : K4_R - 1;
          constexpr int n = hi >= lo ? hi - lo + 1 : 0, nf = lo < K4_S ? (hi < K4_S ? n : K4_S - lo) : 0;
          WAIT_V(nf * NF + (n - nf) * NO);
          BARRIER();
        }
      };
      if (NA == 3 && K4_DLY) __builtin_amdgcn_s_sleep(K4_DLY);
      sfor<0, LG + 1>([&](auto tt) { slot(tt, decltype(tt)::value); });
      if (NA == 2) {
        const unsigned voffS = (unsigned)(kh * 1568 + 63 * idx + (lane < 63 ? lane : 62)) * 2048 + ((xcd >> 1) & 1) * 1024 + ((3 - (int)((uint64_t)wd >> 7)) & 7) * 128;
        touch_b(L0 + K4_SCR, voffS, srdB, 0);
      }
      constexpr int UN = NB ? 14 : 2;
#pragma nounroll
      for (int i = 0; i < 42 / UN; ++i) sfor<LG + 1, LG + 1 + UN>([&](auto tt) { slot(tt, decltype(tt)::value % 7); });
      sfor<LG + 43, 49>([&](auto tt) { slot(tt, decltype(tt)::value % 7); });
      if (kh || NB == 0) {
        sa = (kh ? K4_R : 49) * 131072; sb = 8 * 224 * 2048;
        sfor<49, K4_S>([&](auto tt) { slot(tt, decltype(tt)::value - 49 + K4_R - 56); });
      } else sfor<49, K4_S>([&](auto tt) { slot(tt, decltype(tt)::value - 49); });
      sa = K4_S * 131072; sb = 7 * 224 * 2048;
      sfor<K4_S, K4_R + LG + 1>([&](auto tt) { slot(tt, decltype(tt)::value % 7); });
    };
    if (j == 4) run(IC<2>{}, IC<0>{}, IC<K4_BLEAD>{}, IC<K4_LEAD>{}, 2 * ca, cb);
    else if (j == 0) run(IC<3>{}, IC<0>{}, IC<0>{}, IC<K4_LAG>{}, 2 * ca + 2, 0);
    else if (j == 1) run(IC<3>{}, IC<1>{}, IC<0>{}, IC<K4_LAG>{}, 2 * ca + 5, 0);
    else if (K4_BLEAD && j == 3) run(IC<0>{}, IC<0>{}, IC<3>{}, IC<K4_LAG>{}, 0, cb + 5);
    else run(IC<0>{}, IC<0>{}, IC<4>{}, IC<K4_LAG>{}, 0, K4_BLEAD ? cb + 1 : 4 * j);
    BW_OUT(3);
    return;
  }

  const int gm = wid & 1, gn = wid >> 1, fr = lane & 15, fq = lane >> 4;
  STAMP_BEG(3);
  const int mt = pm * 8 + 4 * gm + kh, tok = mt * 16 + fr;
  u32x4 rh[2]; float rmu[2];
  {
    const char* p = ws + WS_HC + (size_t)(4 * pq + gn) * 131072 + (size_t)mt * 1024 + lane * 16;
    asm volatile("global_load_dwordx4 %0, %1, off" : "=v"(rh[0]) : "v"(p));
    asm volatile("global_load_dwordx4 %0, %1, off offset:2048" : "=v"(rh[1]) : "v"(p));
    const float* q = (const float*)(ws + WS_MU) + tok;
    asm volatile("global_load_dword %0, %1, off" : "=v"(rmu[0]) : "v"(q));
    asm volatile("global_load_dword %0, %1, off offset:128" : "=v"(rmu[1]) : "v"(q));
  }
  f32x4 acc[4][2];
#pragma unroll
  for (int m = 0; m < 4; ++m) acc[m][0] = acc[m][1] = (f32x4){0, 0, 0, 0};
  unsigned ra = L0 + gm * 4096 + lane * 16, rb[2];
  {
    const int q = fr >> 2, p = lane & 3, sw = (2 * fq + (q >> 1)) & 3;
#pragma unroll
    for (int n = 0; n < 2; ++n) rb[n] = L0 + 8192 + (gn >> 1) * 4096 + (unsigned)(4 * fq + q) * 128 + (unsigned)((2 * (gn & 1) + n) ^ sw) * 32 + 8 * p;
  }
  K4Frag F0, F1;
  auto rd = [&](K4Frag& F, int i, int sl) {
    if (i == 0 || i == 3 || i >= 6) { const int m = i == 0 ? 0 : i == 3 ? 2 : 2 * i - 11; F.a[m] = lds128(ra + sl * K4_SLOT + m * 1024); }
    else { const int n = i > 3, e = i - 1 - 3 * n; F.b[n][e] = ldstr(rb[n] + sl * K4_SLOT + e * 2048); }
  };
  auto bump = [&](int i, unsigned dl) { if (i == 0) VADD(ra, dl); else VADD(rb[i - 1], dl); };
  auto mm = [&](K4Frag& F, int i) {
    const int m = 2 * (i & 1) + (i >> 2), n = (i >> 1) & 1;
    acc[m][n] = mfma(cat(F.b[n][0], F.b[n][1]), F.a[m], acc[m][n]);
  };
  auto step = [&](K4Frag& F, K4Frag& N, int sl, unsigned dl) {
#pragma unroll
    for (int i = 0; i < 8; ++i) {
      mm(F, i);
      if (i < 4) { rd(N, 2 * i, sl); rd(N, 2 * i + 1, sl); }
      if (sl == 3 && i >= 5) bump(i - 5, dl);
      SB();
    }
  };
  BARRIER();
#pragma unroll
  for (int i = 0; i < 8; ++i) rd(F0, i, 0);
  WAIT_L0(); BARRIER();
  STAMP_L0();
  unsigned dl = 4 * K4_SLOT;
#pragma nounroll
  for (int it = 0; it < 48; it += 4) {
    step(F0, F1, 1, 0);
    step(F1, F0, 2, 0); WAIT_L0(); BARRIER();
    step(F0, F1, 3, dl);
    step(F1, F0, 0, 0); WAIT_L0(); BARRIER();
    dl = 0u - dl;
  }
  char* xo = ws + WS_XCH + (size_t)(blockIdx.x * 8 + wid) * 2048 + lane * 16;
  const char* xi = ws + WS_XCH + (size_t)((blockIdx.x ^ 1) * 8 + wid) * 2048 + lane * 16;
  u32x4 y[2];
  sfor<48, K4_R>([&](auto tt) {
    constexpr int t = decltype(tt)::value, sl = (t + 1) & 3;
    K4Frag& F = t & 1 ? F1 : F0;
    K4Frag& N = t & 1 ? F0 : F1;
    const unsigned d = (t + 1) & 4 ? 0u - 4 * K4_SLOT : 4 * K4_SLOT;
    if (t + 1 < K4_S) step(F, N, sl, d);
    else {
      if (t + 1 == K4_S) {
#pragma unroll
        for (int i = 0; i < 4; ++i) {
          mm(F, 4 + i);
          if (i < 3) { rd(N, 2 * i, sl); rd(N, 2 * i + 1, sl); }
          SB();
        }
      }
#pragma unroll
      for (int i = 0; i < 4; ++i) {
        mm(F, i);
        if (t + 1 > K4_S && t + 1 < K4_R && i < 2) { rd(N, 3 * i, sl); rd(N, 3 * i + 1, sl); rd(N, 3 * i + 2, sl); }
        SB();
      }
      if (sl == 3 && t + 2 < K4_R) { bump(0, d); bump(1, d); bump(2, d); }
      if (t + 1 == K4_S) {
#pragma unroll
        for (int c = 0; c < 2; ++c) {
          const f32x4 p0 = acc[2 * c + 1][0], p1 = acc[2 * c + 1][1];
          u32x4 v = {pk(p0[0], p0[1]), pk(p0[2], p0[3]), pk(p1[0], p1[1]), pk(p1[2], p1[3])};
#pragma unroll
          for (int e = 0; e < 4; ++e) v[e] = v[e] < 0xFFFFFFFEu ? v[e] : 0xFFFFFFFEu;
          asm volatile("global_store_dwordx4 %0, %1, off offset:%2 sc1\n\ts_nop 1" ::"v"(xo), "v"(v), "n"(c * 1024) : "memory");
        }
      }
      if (t == K4_S + 1) {
        WAIT_V(2);
        asm volatile("" : "+v"(rh[0]), "+v"(rh[1]), "+v"(rmu[0]), "+v"(rmu[1]));
#pragma unroll
        for (int c = 0; c < 2; ++c) {
          const u32x4 h = rh[c];
          acc[2 * c][0] += (f32x4){bflo(h.x), bfhi(h.x), bflo(h.y), bfhi(h.y)} + rmu[c];
          acc[2 * c][1] += (f32x4){bflo(h.z), bfhi(h.z), bflo(h.w), bfhi(h.w)} + rmu[c];
        }
        SB();
      }
    }
    if (t == K4_R - 1 - K4_POLL)
      asm volatile("global_load_dwordx4 %0, %2, off sc1\n\tglobal_load_dwordx4 %1, %2, off offset:1024 sc1" : "=&v"(y[0]), "=&v"(y[1]) : "v"(xi) : "memory");
    if ((t & 1) && t + 1 < K4_R) { WAIT_L0(); BARRIER(); }
  });
  u16* op = out + (size_t)tok * D_ + pq * 128 + gn * 32 + (fq & 1) * 16 + (fq >> 1) * 8;
  asm volatile("" : "+v"(op));
  STAMP_L1(3);
  asm volatile("s_waitcnt vmcnt(0)" : "+v"(y[0]), "+v"(y[1])::"memory");
#pragma nounroll
  for (int spin = 0; spin < (1 << 18); ++spin) {
    const unsigned mx = max(max(max(y[0].x, y[0].y), max(y[0].z, y[0].w)), max(max(y[1].x, y[1].y), max(y[1].z, y[1].w)));
    if (__builtin_amdgcn_ballot_w64(mx == 0xFFFFFFFFu) == 0) break;
    __builtin_amdgcn_s_sleep(K4_SLEEP);
    asm volatile("global_load_dwordx4 %0, %2, off sc1\n\tglobal_load_dwordx4 %1, %2, off offset:1024 sc1\n\ts_waitcnt vmcnt(0)"
                 : "=&v"(y[0]), "=&v"(y[1]) : "v"(xi) : "memory");
  }
#pragma unroll
  for (int c = 0; c < 2; ++c) {
    const f32x4 y0 = (f32x4){bflo(y[c].x), bfhi(y[c].x), bflo(y[c].y), bfhi(y[c].y)} + acc[2 * c][0];
    const f32x4 y1 = (f32x4){bflo(y[c].z), bfhi(y[c].z), bflo(y[c].w), bfhi(y[c].w)} + acc[2 * c][1];
    unsigned a0 = pk(y0[0], y0[1]), a1 = pk(y0[2], y0[3]), b0 = pk(y1[0], y1[1]), b1 = pk(y1[2], y1[3]);
    swap16u(a0, b0); swap16u(a1, b1);
    *(u32x4*)(op + c * 32 * D_) = (u32x4){a0, a1, b0, b1};
  }
  BW_OUT(3);
  STAMP_END(3);
}

constexpr int K3_G = 16384, K3_U = K3_G + 7168 + 16, K3_SLOT = 30752, K3_NS = 5, K3_SCR = K3_SLOT * K3_NS, K3_LDS = K3_SCR + 256;
struct K3Frag { u32x4 a[4]; u32x2 b[7][2]; };

DI unsigned geglu2(f32x2 g, f32x2 u, float k1, float k2, float ir2) {
  const f32x2 z = g * (g * g * k2 + k1);
  f32x2 e; e.x = __builtin_amdgcn_exp2f(z.x); e.y = __builtin_amdgcn_exp2f(z.y);
  const f32x2 d = e * ir2 + ir2;
  f32x2 r; r.x = __builtin_amdgcn_rcpf(d.x); r.y = __builtin_amdgcn_rcpf(d.y);
  const f32x2 o = (g * u) * r;
  return pk(o.x, o.y);
}

extern "C" __global__ __launch_bounds__(512, 2) void k3_gateup(const u16* __restrict__ wg, const u16* __restrict__ wu, const u16* __restrict__ wd,
                                                               char* __restrict__ ws) {
  __shared__ __attribute__((aligned(1024))) char smem[K3_LDS];
  const unsigned L0 = (unsigned)(size_t)(LAS char*)smem;
  const int tid = threadIdx.x, lane = tid & 63;
  const int wid = __builtin_amdgcn_readfirstlane(tid >> 6);
  BW_DECL();
  const int wm = wid & 3, wn = wid >> 2, fr = lane & 15, fq = lane >> 4;
  const int xcd = blockIdx.x & 7, idx = blockIdx.x >> 3;
  const int pm = (xcd >> 2) * 4 + (idx >> 3), pn = (xcd & 3) * 8 + (idx & 7);
  STAMP_BEG(2);

  const u32x4 srdA = make_srd(ws + WS_HC, 32 * 131072);
  const u32x4 srdG = make_srd(wg, D_ * F_ * 2), srdU = make_srd(wu, D_ * F_ * 2);
  const unsigned voffA = lane * 16 + wid * 2048 + pm * 16384;
  unsigned voffB;
  const int wb = wid < 7 ? wid : 6;
  { const unsigned L = wb * 64 + lane, r = (L * 4682u) >> 16; voffB = r * 7168 + pn * 224 + (L - r * 14) * 16; }
  const unsigned ldA = L0 + wid * 2048, ldG = L0 + K3_G + wb * 1024, ldU = L0 + K3_U + wb * 1024;
  auto dA = [&](int j, unsigned so, int i) { dma(ldA + so + i * 1024, voffA, srdA, j * 131072 + i * 1024); };
  auto dG = [&](int j, unsigned so) { dma(ldG + so, voffB, srdG, j * (32 * 7168)); };
  auto dU = [&](int j, unsigned so) { dma(ldU + so, voffB, srdU, j * (32 * 7168)); };
#define K3_WAIT(slots, extra) do { WAIT_VL(4 * (slots) + (extra)); BARRIER(); } while (0)
#pragma unroll
  for (int j = 0; j < 3; ++j) { dA(j, j * K3_SLOT, 0); dA(j, j * K3_SLOT, 1); dG(j, j * K3_SLOT); dU(j, j * K3_SLOT); }
#pragma unroll
  for (int c = 0; c < K4_XN; ++c) st16(ws + WS_XCH + (size_t)(blockIdx.x * 8 + wid) * (K4_XN * 1024) + c * 1024 + lane * 16, (u32x4){~0u, ~0u, ~0u, ~0u});
  unsigned ra = L0 + wm * 4096 + lane * 16;
  unsigned rb = L0 + K3_G + (unsigned)(4 * fq + (fr >> 2)) * 224 + 112 * wn + (lane & 3) * 8;
  unsigned rm = L0 + K3_G + (unsigned)(4 * fq + (fr >> 2)) * 224 + 112 * wn + 96 + (lane & 1) * 8 + ((lane >> 1) & 1) * (K3_U - K3_G);

  f32x4 acc[4][7];
#pragma unroll
  for (int m = 0; m < 4; ++m)
#pragma unroll
    for (int n = 0; n < 7; ++n) acc[m][n] = (f32x4){0, 0, 0, 0};
  K3Frag F0, F1;
  auto rd = [&](K3Frag& F, int i) {
    if (i < 4) F.a[i] = lds128(ra + i * 1024);
    else {
      const int e = i - 4, n = e >> 1, t = e & 1;
      F.b[n][t] = n < 6 ? ldstr(rb + (n >> 1) * 32 + (n & 1) * (K3_U - K3_G) + t * 3584) : ldstr(rm + t * 3584);
    }
  };
  auto step = [&](K3Frag& F, K3Frag& N, unsigned so1, int ND, int jd, unsigned sd, unsigned sd2, bool AONLY, bool RD) {
    const unsigned dl = so1 == (K3_NS - 1) * K3_SLOT ? (unsigned)(-(K3_NS - 1) * K3_SLOT) : (unsigned)K3_SLOT;
#pragma unroll
    for (int i = 0; i < 28; ++i) {
      const int n = i >> 2, m = i & 3;
      acc[m][n] = mfma(cat(F.b[n][0], F.b[n][1]), F.a[m], acc[m][n]);
      if (RD && i < 18) rd(N, i);
      if (RD && i == 19) VADD(ra, dl);
      if (RD && i == 21) VADD(rb, dl);
      if (RD && i == 23) VADD(rm, dl);
      const int s = ND == 2 ? 10 : 18;
      if (ND > 0) {
        if (i == s) dA(jd, sd, 0);
        if (i == s + 2) dA(jd, sd, 1);
        if (i == s + 4 && !AONLY) dG(jd, sd);
        if (i == s + 6 && !AONLY) dU(jd, sd);
      }
      if (ND > 1) {
        if (i == 18) dA(jd + 1, sd2, 0);
        if (i == 20) dA(jd + 1, sd2, 1);
        if (i == 22) dG(jd + 1, sd2);
        if (i == 24) dU(jd + 1, sd2);
      }
      SB();
    }
  };

  K3_WAIT(2, K4_XN);
#pragma unroll
  for (int i = 0; i < 18; ++i) rd(F0, i);
  { const unsigned dl = K3_SLOT; VADD(ra, dl); VADD(rb, dl); VADD(rm, dl); }
  K3_WAIT(1, K4_XN);
  STAMP_L0();
  step(F0, F1, K3_SLOT, 2, 3, 3 * K3_SLOT, 4 * K3_SLOT, false, true);
  K3_WAIT(2, 0);
  step(F1, F0, 2 * K3_SLOT, 2, 5, 0, K3_SLOT, false, true);
  touch_b(L0 + K3_SCR, (lane < 24 ? lane : 23) * 128 + wm * 3072, wn ? srdU : srdG, 4194304 + blockIdx.x * 12288);
  K3_WAIT(3, 0);
  CODE_ADDR(nextk, k4_down);
  uint64_t here; asm volatile("s_getpc_b64 %0" : "=s"(here));
  unsigned so = 2 * K3_SLOT;
  for (int j = 2; j < 26; j += 2) {
    const unsigned so1 = so == (K3_NS - 1) * K3_SLOT ? 0 : so + K3_SLOT;
    const unsigned so2 = so1 == (K3_NS - 1) * K3_SLOT ? 0 : so1 + K3_SLOT;
    step(F0, F1, so1, 1, j + 5, so, 0, false, true);
    K3_WAIT(3, 0);
    step(F1, F0, so2, 1, j + 6, so1, 0, false, true);
    if (j == 18 && wid < 4) touch_g(L0 + K3_SCR, lane * 128, wid >= 2 ? nextk + (wid - 2) * 8192 : here + wid * 8192);
    K3_WAIT(3, 0);
    so = so2;
  }
  f32x4 ssq[4];
  {
    const char* q = ws + WS_HC + (size_t)32 * 131072 + (size_t)(pm * 256 + 64 * wm + fr) * 64 + fq * 16;
#pragma unroll
    for (int m = 0; m < 4; ++m) asm volatile("global_load_dwordx4 %0, %1, off offset:%2" : "=v"(ssq[m]) : "v"(q), "n"(m * 1024));
  }
  step(F0, F1, 2 * K3_SLOT, 1, 31, 1 * K3_SLOT, 0, false, true);
  K3_WAIT(3, 4);
  {
    const u32x4 srdD = make_srd(wd, D_ * F_ * 2);
    const int l = lane < 56 ? lane : 55, P = (xcd & 1) ? (wid < 7 ? 9 + wid : 8) : wid;
    touch_b(L0 + K3_SCR, (l >> 3) * 2048 + (l & 7) * 128, srdD, (224 * P + 7 * idx) * 2048 + ((xcd >> 1) & 1) * 1024);
  }
  step(F1, F0, 3 * K3_SLOT, 0, 0, 0, 0, false, true);
  WAIT_VL(1); BARRIER();
  step(F0, F1, 4 * K3_SLOT, 0, 0, 0, 0, false, true);
  step(F1, F0, 0, 0, 0, 0, 0, false, true);
  step(F0, F1, 1 * K3_SLOT, 0, 0, 0, 0, false, true);
  step(F1, F0, 0, 0, 0, 0, 0, false, false);
  STAMP_L1(2);
  float k1[4], k2[4], ir2[4];
#pragma unroll
  for (int m = 0; m < 4; ++m) {
    asm volatile("" : "+v"(ssq[m]));
    const f32x4 p = ssq[m];
    ir2[m] = red_add((p[0] + p[1]) + (p[2] + p[3])) * (1.0f / D_) + LN_EPS;
    const float r = __builtin_amdgcn_rsqf(ir2[m]);
    const float c1 = -2.0f * 0.7978845608028654f * 1.4426950408889634f, c2 = c1 * 0.044715f;
    k1[m] = c1 * r; k2[m] = c2 * r * r * r;
  }
  const int P = pn >> 1, w = 2 * (pn & 1) + wn, mt0 = pm * 16 + 4 * wm;
  char* ab = ws + WS_ACT + (size_t)(7 * P) * 131072 + (size_t)mt0 * 1024;
  unsigned xb[4][2];
#pragma unroll
  for (int m = 0; m < 4; ++m) {
    unsigned o[3][2];
#pragma unroll
    for (int c = 0; c < 3; ++c) {
      const f32x4 a = acc[m][2 * c], b = acc[m][2 * c + 1];
      o[c][0] = geglu2(a.xy, b.xy, k1[m], k2[m], ir2[m]);
      o[c][1] = geglu2(a.zw, b.zw, k1[m], k2[m], ir2[m]);
    }
    st16(ab + (size_t)w * 131072 + m * 1024 + lane * 16, (u32x4){o[0][0], o[0][1], o[1][0], o[1][1]});
    xb[m][0] = o[2][0]; xb[m][1] = o[2][1];
  }
  const int hf = lane >> 5, fq1 = fq & 1;
#pragma unroll
  for (int mp = 0; mp < 2; ++mp) {
    swap32u(xb[2 * mp][0], xb[2 * mp + 1][0]); swap32u(xb[2 * mp][1], xb[2 * mp + 1][1]);
    const unsigned slot = (unsigned)((2 * (w & 1) + fq1) * 16 + fr) * 16 + (2 * mp + hf) * 1024;
    st16(ab + (size_t)(4 + (w >> 1)) * 131072 + slot, (u32x4){xb[2 * mp][0], xb[2 * mp][1], xb[2 * mp + 1][0], xb[2 * mp + 1][1]});
    f32x4 a = acc[2 * mp][6], b = acc[2 * mp + 1][6];
#pragma unroll
    for (int r = 0; r < 4; ++r) { float u = a[r], v = b[r]; swap32f(u, v); a[r] = u; b[r] = v; }
    const float q1 = hf ? k1[2 * mp + 1] : k1[2 * mp], q2 = hf ? k2[2 * mp + 1] : k2[2 * mp], qi = hf ? ir2[2 * mp + 1] : ir2[2 * mp];
    st8(ab + (size_t)6 * 131072 + slot + (w >> 1) * 8, (u32x2){geglu2(a.xy, b.xy, q1, q2, qi), geglu2(a.zw, b.zw, q1, q2, qi)});
  }
  BW_OUT(2);
  STAMP_END(2);
}

extern "C" __global__ void kz_pad() { asm volatile(".fill 4096, 4, 0xBF800000"); }

extern "C" void kernel_launch(void* const* d_in, const int* in_sizes, int n_in, void* d_out, int out_size, void* d_ws, size_t ws_size,
                              hipStream_t stream) {
  const u16* x = (const u16*)d_in[0];
  const u16* wqkv = (const u16*)d_in[1];
  const u16* wo = (const u16*)d_in[2];
  const u16* wg = (const u16*)d_in[4];
  const u16* wu = (const u16*)d_in[5];
  const u16* wd = (const u16*)d_in[6];
  char* ws = (char*)d_ws;
  u16* out = (u16*)d_out;
  k1_qkv_attn<<<256, 768, 0, stream>>>(x, wqkv, wo, wg, wu, ws);
  k2_wo<<<256, 576, 0, stream>>>(x, wo, ws);
  k3_gateup<<<256, 512, 0, stream>>>(wg, wu, wd, ws);
  k4_down<<<256, 832, 0, stream>>>(wd, ws, out);
}
```

```cpp
#include <hip/hip_runtime.h>
#include <hip/hip_bf16.h>
#include <stdint.h>

#pragma clang diagnostic ignored "-Winline-asm"
#pragma clang diagnostic ignored "-Wint-to-pointer-cast"

typedef unsigned short u16;
typedef __bf16 bf16x8 __attribute__((ext_vector_type(8)));
typedef __bf16 bf16x2 __attribute__((ext_vector_type(2)));
typedef short s16x4 __attribute__((ext_vector_type(4)));
typedef float f32x4 __attribute__((ext_vector_type(4)));
typedef float f32x2 __attribute__((ext_vector_type(2)));
typedef unsigned u32x4 __attribute__((ext_vector_type(4)));
typedef unsigned u32x2 __attribute__((ext_vector_type(2)));
#define LAS __attribute__((address_space(3)))
#define DI __device__ __forceinline__

constexpr int S_ = 2048, D_ = 1024, F_ = 3584;
constexpr float LN_EPS = 1e-5f;

constexpr size_t WS_O = 0;
constexpr size_t WS_ACT = 0;
constexpr size_t WS_HC = WS_ACT + (size_t)112 * 131072;
constexpr size_t WS_MUX = WS_HC + (size_t)33 * 131072;
constexpr size_t WS_MU = WS_MUX + 8192;
constexpr size_t WS_WTAB = WS_MU + 8192;
constexpr size_t WS_XCH = WS_WTAB + 8192;

DI u32x4 make_srd(const void* p, unsigned bytes) {
  uint64_t a = (uint64_t)p;
  u32x4 r; r.x = (unsigned)a; r.y = (unsigned)(a >> 32) & 0xffffu; r.z = bytes; r.w = 0x00020000u;
  return r;
}
DI void dma(unsigned lds, unsigned voff, u32x4 srd, unsigned soff) {
  asm volatile("s_mov_b32 m0, %0\n\ts_nop 0\n\tbuffer_load_dwordx4 %1, %2, %3 offen lds" ::"s"(lds), "v"(voff), "s"(srd), "s"(soff) : "m0");
}
DI void dma2(unsigned lds, unsigned so, unsigned voff, u32x4 srd, unsigned soff) {
  asm volatile("s_add_u32 m0, %0, %1\n\ts_nop 0\n\tbuffer_load_dwordx4 %2, %3, %4 offen lds" ::"s"(lds), "s"(so), "v"(voff), "s"(srd), "s"(soff) : "m0", "scc");
}
DI void dma_nt(unsigned lds, unsigned voff, u32x4 srd, unsigned soff) {
  asm volatile("s_mov_b32 m0, %0\n\ts_nop 0\n\tbuffer_load_dwordx4 %1, %2, %3 offen nt lds" ::"s"(lds), "v"(voff), "s"(srd), "s"(soff) : "m0");
}
#define dma1 dma
DI void touch_g(unsigned lds, unsigned voff, uint64_t base) {
  asm volatile("s_mov_b32 m0, %0\n\ts_nop 0\n\tglobal_load_lds_dword %1, %2" ::"s"(lds), "v"(voff), "s"(base) : "m0");
}
DI void touch_b(unsigned lds, unsigned voff, u32x4 srd, unsigned soff) {
  asm volatile("s_mov_b32 m0, %0\n\ts_nop 0\n\tbuffer_load_dword %1, %2, %3 offen lds" ::"s"(lds), "v"(voff), "s"(srd), "s"(soff) : "m0");
}
DI void touch_v(unsigned lds, uint64_t addr) {
  asm volatile("s_mov_b32 m0, %0\n\ts_nop 0\n\tglobal_load_lds_dword %1, off" ::"s"(lds), "v"(addr) : "m0");
}
#define CODE_ADDR(var, sym) uint64_t var; { int d_; asm volatile("s_getpc_b64 %0\n\ts_mov_b32 %1, " #sym "@rel32@lo+4" : "=s"(var), "=s"(d_)); var += (int64_t)d_; }
#define VADD(v, s) asm volatile("v_add_u32 %0, %1, %0" : "+v"(v) : "s"(s))
#define SB() __builtin_amdgcn_sched_barrier(0)
#define MEMFENCE() asm volatile("" ::: "memory")
#define WAIT_VL(V) do { SB(); MEMFENCE(); __builtin_amdgcn_s_waitcnt(((V) & 15) | (((V) >> 4) << 14) | 0x70); MEMFENCE(); SB(); } while (0)
#define WAIT_V(V) do { SB(); MEMFENCE(); __builtin_amdgcn_s_waitcnt(((V) & 15) | (((V) >> 4) << 14) | 0xF70); MEMFENCE(); SB(); } while (0)
#define WAIT_L0() do { SB(); MEMFENCE(); __builtin_amdgcn_s_waitcnt(0xC07F); MEMFENCE(); SB(); } while (0)
#define BARRIER() do { SB(); MEMFENCE(); __builtin_amdgcn_s_barrier(); MEMFENCE(); SB(); } while (0)
#define BW_DECL()
#define BW_OUT(kern)

DI u32x4 lds128(unsigned a) { return *(LAS u32x4*)(a); }
DI f32x4 lds128f(unsigned a) { return *(LAS f32x4*)(a); }
DI u32x2 ldstr(unsigned a) { return __builtin_bit_cast(u32x2, __builtin_amdgcn_ds_read_tr16_b64_v4i16((LAS s16x4*)(a))); }
DI f32x4 mfma(u32x4 a, u32x4 b, f32x4 c) {
  return __builtin_amdgcn_mfma_f32_16x16x32_bf16(__builtin_bit_cast(bf16x8, a), __builtin_bit_cast(bf16x8, b), c, 0, 0, 0);
}
DI u32x4 cat(u32x2 lo, u32x2 hi) { u32x4 r; r.x = lo.x; r.y = lo.y; r.z = hi.x; r.w = hi.y; return r; }
DI unsigned pk(float a, float b) { f32x2 v = {a, b}; return __builtin_bit_cast(unsigned, __builtin_convertvector(v, bf16x2)); }
DI float dot2(unsigned a, unsigned b, float c) {
  return __builtin_amdgcn_fdot2_f32_bf16(__builtin_bit_cast(bf16x2, a), __builtin_bit_cast(bf16x2, b), c, false);
}
DI float bflo(unsigned u) { return __uint_as_float(u << 16); }
DI float bfhi(unsigned u) { return __uint_as_float(u & 0xffff0000u); }
DI float red_add(float v) {
  auto a = __builtin_amdgcn_permlane32_swap(__float_as_uint(v), __float_as_uint(v), false, false);
  v = __uint_as_float(a[0]) + __uint_as_float(a[1]);
  auto b = __builtin_amdgcn_permlane16_swap(__float_as_uint(v), __float_as_uint(v), false, false);
  return __uint_as_float(b[0]) + __uint_as_float(b[1]);
}
DI float red_max(float v) {
  auto a = __builtin_amdgcn_permlane32_swap(__float_as_uint(v), __float_as_uint(v), false, false);
  v = fmaxf(__uint_as_float(a[0]), __uint_as_float(a[1]));
  auto b = __builtin_amdgcn_permlane16_swap(__float_as_uint(v), __float_as_uint(v), false, false);
  return fmaxf(__uint_as_float(b[0]), __uint_as_float(b[1]));
}
DI void swap32f(float& a, float& b) {
  auto r = __builtin_amdgcn_permlane32_swap(__float_as_uint(a), __float_as_uint(b), false, false);
  a = __uint_as_float(r[0]); b = __uint_as_float(r[1]);
}
DI void swap32u(unsigned& a, unsigned& b) { auto r = __builtin_amdgcn_permlane32_swap(a, b, false, false); a = r[0]; b = r[1]; }
DI void swap16u(unsigned& a, unsigned& b) { auto r = __builtin_amdgcn_permlane16_swap(a, b, false, false); a = r[0]; b = r[1]; }
constexpr unsigned ONES2 = 0x3F803F80u;
#define STMODE 3
DI void st16(void* p, u32x4 v) {
  asm volatile("global_store_dwordx4 %0, %1, off sc1\n\ts_nop 1" :: "v"(p), "v"(v) : "memory");
}
DI void st8(void* p, u32x2 v) {
  asm volatile("global_store_dwordx2 %0, %1, off sc1" :: "v"(p), "v"(v) : "memory");
}
#define STAMP_AT(kern, i)
#define STAMP_RT(kern, i)
#define STAMP_L0()
#define STAMP_L1(kern)
#define STAMP_END(kern)
#define STAMP_BEG(kern)

extern "C" __global__ void k1_qkv_attn(const u16*, const u16*, const u16*, const u16*, const u16*, char*);
extern "C" __global__ void k2_wo(const u16*, const u16*, char*);
extern "C" __global__ void k3_gateup(const u16*, const u16*, const u16*, char*);
extern "C" __global__ void k4_down(const u16*, char*, u16*);

constexpr int K1_STAGE = 43008, K1_B = 18432, K1_ST = 3 * K1_STAGE;
constexpr int K1_SCR = K1_ST + 144 * 8, K1_LDS = K1_SCR + 256;
#define PFD 3
struct K1Frag { u32x4 a[2]; u32x4 h; u32x2 b[6][2]; };

extern "C" __global__ __launch_bounds__(768, 3) void k1_qkv_attn(const u16* __restrict__ x, const u16* __restrict__ wqkv, const u16* __restrict__ wo,
                                                                 const u16* __restrict__ wg, const u16* __restrict__ wu, char* __restrict__ ws) {
  __shared__ __attribute__((aligned(1024))) char smem[K1_LDS];
  const unsigned L0 = (unsigned)(size_t)(LAS char*)smem;
  const int tid = threadIdx.x, lane = tid & 63;
  const int wid = __builtin_amdgcn_readfirstlane(tid >> 6);
  BW_DECL();
  const int xcd = blockIdx.x & 7, idx = blockIdx.x >> 3;
  const int pm = (xcd >> 1) * 4 + (idx >> 3), pn = (xcd & 1) * 8 + (idx & 7);
  const int s0 = pm * 128;
  const int l3 = lane >> 3, l7 = lane & 7;

  if (wid >= 8) {
    const int j = wid - 8;
    __builtin_amdgcn_s_setprio(3);
    const unsigned vline = lane * 128;
    CODE_ADDR(self, k1_qkv_attn);
    if (j >= 2) touch_g(L0 + K1_SCR, vline, self + (j - 2) * 8192);
    const u32x4 srdX = make_srd(x, S_ * D_ * 2);
    const u32x4 srdW = make_srd(wqkv, D_ * 3 * D_ * 2);
    unsigned voffA[2], voffB[2];
#pragma unroll
    for (int i = 0; i < 2; ++i) {
      voffA[i] = (unsigned)(s0 + 32 * j + 8 * i + l3) * 2048 + (unsigned)(l7 ^ (4 * i + (lane >> 4))) * 16;
      voffB[i] = (unsigned)(16 * j + 8 * i + l3) * 6144 + pn * 128 + (unsigned)(l7 ^ ((((lane >> 4) & 1) | (i << 1)) << 1)) * 16;
    }
    unsigned voffE;
    if (j < 2) {
      const int r = s0 - 16 + 8 * j + l3;
      voffE = (unsigned)(r < 0 ? 0 : r) * 2048 + (unsigned)(l7 ^ (4 * j + (lane >> 4))) * 16;
      if (j == 0 && lane < 24) *(LAS u32x4*)(L0 + l3 * K1_STAGE + 16384 + l7 * 16) = (u32x4){ONES2, ONES2, ONES2, ONES2};
    } else {
      voffE = j == 2 ? (blockIdx.x * 8 + l7) * 2048 + PFD * 128 : PFD * (64 * 6144) + (blockIdx.x * 12 + (lane < 12 ? lane : 11)) * 128;
      const int q = idx * 6 + (lane & 7), ps = (xcd & 1) * 8 + ((3 - (int)((uint64_t)wqkv >> 7)) & 7);
      if (j == 3 && lane >= 16 && lane < 22) voffE = PFD * (64 * 6144) + (q / 3) * 6144 + (q % 3) * 2048 + ps * 128;
    }
    const unsigned long long emask = j == 0 ? ~0xFFull : ~0ull;
    CODE_ADDR(nextk, k2_wo);
    unsigned so = 0;
#pragma nounroll
    for (int kt = 0; kt < 16; ++kt) {
      if (j < 2) asm volatile("s_mov_b32 m0, %0\n\ts_mov_b64 exec, %4\n\tbuffer_load_dwordx4 %1, %2, %3 offen lds\n\ts_mov_b64 exec, -1"
                              ::"s"(L0 + so + 16384 + j * 1024), "v"(voffE), "s"(srdX), "s"(kt * 128), "s"(emask) : "m0");
      else if (kt >= 16 - PFD) touch_g(L0 + K1_SCR, vline, j == 2 ? self + 2048 : nextk);
      else touch_b(L0 + K1_SCR, voffE, j == 2 ? srdX : srdW, j == 2 ? kt * 128 : kt * (64 * 6144));
#pragma unroll
      for (int i = 0; i < 4; ++i) dma(L0 + so + j * 4096 + i * 1024, voffA[i & 1], srdX, kt * 128 + (i >> 1) * 32768);
#pragma unroll
      for (int i = 0; i < 6; ++i) dma(L0 + so + K1_B + (i >> 1) * 8192 + j * 2048 + (i & 1) * 1024, voffB[i & 1], srdW, kt * (64 * 6144) + (i >> 1) * 2048);
      so = so == 2 * K1_STAGE ? 0 : so + K1_STAGE;
      if (kt > 0) { WAIT_VL(11); BARRIER(); }
    }
    u32x4 hv[8];
    {
      const int r = s0 - 16 + 4 * j + (lane >> 4);
      const u16* p = x + (size_t)(r < 0 ? 0 : r) * D_ + (lane & 15) * 8;
#pragma unroll
      for (int i = 0; i < 8; ++i) asm volatile("global_load_dwordx4 %0, %1, off offset:%2" : "=v"(hv[i]) : "v"(p), "n"(i * 256));
    }
    WAIT_V(8); BARRIER();
    u32x4 wrow0, wrow1;
    {
      const u16* p = wo + (size_t)(blockIdx.x * 4 + j) * D_ + lane * 8;
      asm volatile("global_load_dwordx4 %0, %1, off" : "=v"(wrow0) : "v"(p));
      asm volatile("global_load_dwordx4 %0, %1, off offset:1024" : "=v"(wrow1) : "v"(p));
      const u32x4 srdGU = make_srd((j & 1) ? wu : wg, D_ * F_ * 2);
      touch_b(L0 + K1_SCR, vline, srdGU, ((j >> 1) * 256 + blockIdx.x) * 8192);
      touch_b(L0 + K1_SCR, l3 * 2048 + l7 * 128, make_srd(wo, D_ * D_ * 2), (idx * 32 + j * 8) * 2048 + (xcd & 1) * 1024);
    }
    BARRIER();
    WAIT_V(4);
    {
      float a[2] = {0.f, 0.f}, q[2] = {0.f, 0.f};
#pragma unroll
      for (int i = 0; i < 8; ++i) {
        asm volatile("" : "+v"(hv[i]));
#pragma unroll
        for (int e = 0; e < 4; ++e) { a[e & 1] = dot2(hv[i][e], ONES2, a[e & 1]); q[e & 1] = dot2(hv[i][e], hv[i][e], q[e & 1]); }
      }
      float sa = a[0] + a[1], sq = q[0] + q[1];
#pragma unroll
      for (int o = 1; o < 16; o <<= 1) { sa += __shfl_xor(sa, o); sq += __shfl_xor(sq, o); }
      const float mean = sa * (1.0f / D_), var = fmaxf(sq * (1.0f / D_) - mean * mean, 0.f);
      if ((lane & 15) == 0) *(LAS f32x2*)(L0 + K1_ST + (4 * j + (lane >> 4)) * 8) = (f32x2){mean, __builtin_amdgcn_rsqf(var + LN_EPS)};
    }
    WAIT_L0(); BARRIER();
    BARRIER();
    WAIT_V(2);
    asm volatile("" : "+v"(wrow0), "+v"(wrow1));
    float s = 0.f;
#pragma unroll
    for (int e = 0; e < 4; ++e) { s = dot2(wrow0[e], ONES2, s); s = dot2(wrow1[e], ONES2, s); }
#pragma unroll
    for (int o = 1; o < 16; o <<= 1) s += __shfl_xor(s, o);
    s = red_add(s) * (1.0f / D_);
    const unsigned hi = pk(s, 0.f) & 0xffffu;
    const unsigned lo = pk(s - bflo(hi), 0.f) & 0xffffu;
    const int k = blockIdx.x * 4 + j, dd = k & 31;
    u16* t = (u16*)(ws + WS_WTAB) + (k >> 5) * 96 + ((dd >> 2) & 3) * 24 + 4 * (dd >> 4) + (dd & 3);
    if (lane < 3) t[lane * 8] = (u16)(lane == 0 ? hi : lane == 1 ? lo : 0u);
    BW_OUT(0);
    return;
  }

  const int g = wid & 3, hh = wid >> 2, fr = lane & 15, fq = lane >> 4;
  STAMP_BEG(0);
  unsigned ra[2][2], rh[2], rb[6];
#pragma unroll
  for (int ks = 0; ks < 2; ++ks) {
    const unsigned c = (unsigned)((4 * ks + fq) ^ (fr >> 1)) * 16;
#pragma unroll
    for (int m = 0; m < 2; ++m) ra[m][ks] = L0 + (unsigned)(16 * (2 * g + (m ^ hh)) + fr) * 128 + c;
    rh[ks] = L0 + 16384 + fr * 128 + c;
  }
  {
    const int q = fr >> 2, p = lane & 3, f = ((q >> 1) & 1) | ((fq & 1) << 1);
#pragma unroll
    for (int j = 0; j < 6; ++j) {
      const int a = j < 4 ? ((j + g) & 3) : 4 + ((j + g) & 1);
      rb[j] = L0 + K1_B + (a >> 1) * 8192 + (unsigned)(8 * fq + q) * 128 + (unsigned)((2 * hh + (a & 1)) ^ f) * 32 + 8 * p;
    }
  }
  f32x4 acc[2][6], accHT = {0, 0, 0, 0}, accHV = {0, 0, 0, 0}, accS = {0, 0, 0, 0}, accQ = {0, 0, 0, 0};
  u32x4 onesf = {ONES2, ONES2, ONES2, ONES2};
  asm volatile("" : "+v"(onesf));
#pragma unroll
  for (int m = 0; m < 2; ++m)
#pragma unroll
    for (int j = 0; j < 6; ++j) acc[m][j] = (f32x4){0, 0, 0, 0};
  K1Frag F0, F1;
  auto rd = [&](K1Frag& F, int i, int ks) {
    if (i < 2) F.a[i] = lds128(ra[i][ks]);
    else if (i == 2) F.h = lds128(rh[ks]);
    else { const int e = i - 3, j = e >> 1, t = e & 1; F.b[j][t] = ldstr(rb[j] + ks * 4096 + t * 512); }
  };
  auto mm = [&](K1Frag& F, int i) {
    if (i < 12) {
      const int j = i >> 1, m = i & 1;
      const u32x4 b = cat(F.b[j][0], F.b[j][1]);
      if (j < 4) acc[m][j] = mfma(b, F.a[m], acc[m][j]); else acc[m][j] = mfma(F.a[m], b, acc[m][j]);
    } else if (i == 12) accHT = mfma(cat(F.b[0][0], F.b[0][1]), F.h, accHT);
    else if (i == 13) accS = mfma(onesf, F.a[0], accS);
    else if (i == 14) accQ = mfma(F.a[0], F.a[0], accQ);
    else accHV = mfma(F.h, cat(F.b[4][0], F.b[4][1]), accHV);
  };

  BARRIER();
#pragma unroll
  for (int i = 0; i < 15; ++i) rd(F0, i, 0);
  WAIT_L0();
  STAMP_L0();
  unsigned sidx = 0;
  for (int kt = 0; kt < 16; ++kt) {
    const unsigned dl = sidx == 2 ? (unsigned)(-2 * K1_STAGE) : (unsigned)K1_STAGE;
    sidx = sidx == 2 ? 0 : sidx + 1;
#pragma unroll
    for (int i = 0; i < 16; ++i) {
      mm(F0, i);
      if (i < 7) { rd(F1, 2 * i, 1); rd(F1, 2 * i + 1, 1); }
      if (i == 5) VADD(ra[0][0], dl);
      if (i == 6) VADD(ra[1][0], dl);
      if (i == 7) rd(F1, 14, 1);
      if (i >= 8 && i < 14) VADD(rb[i - 8], dl);
      if (i == 14) VADD(rh[0], dl);
      SB();
    }
    WAIT_L0(); BARRIER();
#pragma unroll
    for (int i = 0; i < 16; ++i) {
      mm(F1, i);
      if (i < 7) { rd(F0, 2 * i, 0); rd(F0, 2 * i + 1, 0); }
      if (i == 7) rd(F0, 14, 0);
      if (i == 9) VADD(ra[0][1], dl);
      if (i == 11) VADD(ra[1][1], dl);
      if (i == 13) VADD(rh[1], dl);
      SB();
    }
    WAIT_L0();
  }
  STAMP_L1(0);

  constexpr unsigned X_OFF = 0;
  constexpr unsigned CT = 2 * K1_STAGE;
  constexpr unsigned CV = CT + 512;
  {
    if (fr == 0) *(LAS f32x4*)(L0 + CT + (hh * 4 + g) * 64 + fq * 16) = accHT;
    if (fq == 0) *(LAS float*)(L0 + CV + (hh * 2 + (g & 1)) * 64 + fr * 4) = accHV[0];
    const int r = fr & 3;
    const float dq = r == 0 ? accQ[0] : r == 1 ? accQ[1] : r == 2 ? accQ[2] : accQ[3];
    const float mean = accS[0] * (1.0f / D_), var = fmaxf(dq * (1.0f / D_) - mean * mean, 0.f);
    const int tl = 16 * (2 * g + hh) + fr;
    if (fq == (fr >> 2)) {
      *(LAS f32x2*)(L0 + K1_ST + (16 + tl) * 8) = (f32x2){mean, __builtin_amdgcn_rsqf(var + LN_EPS)};
      if (pn == 0) ((float*)(ws + WS_MUX))[s0 + tl] = mean;
    }
  }
  WAIT_L0(); BARRIER();
  {
    const unsigned xb = L0 + X_OFF + hh * (9 * 3072) + lane * 16;
    f32x4 ct[4]; float cv[2];
#pragma unroll
    for (int j = 0; j < 4; ++j) ct[j] = lds128f(L0 + CT + (hh * 4 + ((j + g) & 3)) * 64 + fq * 16);
#pragma unroll
    for (int j = 0; j < 2; ++j) cv[j] = *(LAS float*)(L0 + CV + (hh * 2 + ((j + g) & 1)) * 64 + fr * 4);
#pragma unroll
    for (int m = 0; m < 3; ++m) {
      const int ti = m == 2 ? 0 : 2 * g + (m ^ hh) + 1;
      f32x2 st = *(LAS f32x2*)(L0 + K1_ST + (16 * ti + fr) * 8);
      f32x4 s01 = lds128f(L0 + K1_ST + (16 * ti + 4 * fq) * 8), s23 = lds128f(L0 + K1_ST + (16 * ti + 4 * fq + 2) * 8);
#pragma unroll
      for (int j = 0; j < (m == 2 ? 1 : 4); ++j) {
        const f32x4 a = m == 2 ? accHT : acc[m & 1][j];
        const f32x4 v = (a - ct[j] * st.x) * st.y;
        const int at = (j + g) & 3;
        u32x2 w; w.x = pk(v[0], v[1]); w.y = pk(v[2], v[3]);
        *(LAS u32x2*)(xb + ti * 3072 + (at >> 1) * 1024 + (at & 1) * 8) = w;
      }
#pragma unroll
      for (int j = 0; j < (m == 2 ? 1 : 2); ++j) {
        const f32x4 a = m == 2 ? accHV : acc[m & 1][4 + j];
        u32x2 w;
        w.x = pk((a[0] - cv[j] * s01[0]) * s01[1], (a[1] - cv[j] * s01[2]) * s01[3]);
        w.y = pk((a[2] - cv[j] * s23[0]) * s23[1], (a[3] - cv[j] * s23[2]) * s23[3]);
        *(LAS u32x2*)(xb + ti * 3072 + 2048 + ((j + g) & 1) * 8) = w;
      }
    }
  }
  WAIT_L0(); BARRIER();
  {
    const unsigned xb = L0 + X_OFF + hh * (9 * 3072) + 2 * g * 3072 + lane * 16;
    u32x4 kf[3], vf[3], qf[2];
#pragma unroll
    for (int t = 0; t < 3; ++t) { kf[t] = lds128(xb + t * 3072 + 1024); vf[t] = lds128(xb + t * 3072 + 2048); }
#pragma unroll
    for (int t = 0; t < 2; ++t) qf[t] = lds128(xb + (t + 1) * 3072);
    const float c = 0.17677669529663687f * 1.4426950408889634f;
    char* of = ws + WS_O + (size_t)(2 * pn + hh) * 131072 + (size_t)(pm * 8 + 2 * g) * 1024 + lane * 16;
#pragma unroll
    for (int m = 0; m < 2; ++m) {
      const f32x4 z = {0, 0, 0, 0};
      f32x4 sp = mfma(kf[m], qf[m], z), sc = mfma(kf[m + 1], qf[m], z);
      const bool pok = (m == 1) || (pm | g) != 0;
      float s[8];
#pragma unroll
      for (int r = 0; r < 4; ++r) {
        const int d = fr - 4 * fq - r;
        s[r] = (pok && ((d & 1) == 0) && d <= -2) ? sp[r] : -INFINITY;
        s[4 + r] = (((d & 1) == 0) && d >= 0 && d <= 14) ? sc[r] : -INFINITY;
      }
      float mx = fmaxf(fmaxf(fmaxf(s[0], s[1]), fmaxf(s[2], s[3])), fmaxf(fmaxf(s[4], s[5]), fmaxf(s[6], s[7])));
      mx = red_max(mx);
      const float mc = mx * c;
      float sum = 0.f;
#pragma unroll
      for (int e = 0; e < 8; ++e) { s[e] = __builtin_amdgcn_exp2f(s[e] * c - mc); sum += s[e]; }
      sum = red_add(sum);
      const float inv = __builtin_amdgcn_rcpf(sum);
      u32x4 p;
#pragma unroll
      for (int e = 0; e < 4; ++e) p[e] = pk(s[2 * e] * inv, s[2 * e + 1] * inv);
      u32x4 va, vb;
      va.x = vf[m].x; va.y = vf[m].y; va.z = vf[m + 1].x; va.w = vf[m + 1].y;
      vb.x = vf[m].z; vb.y = vf[m].w; vb.z = vf[m + 1].z; vb.w = vf[m + 1].w;
      const f32x4 oa = mfma(va, p, z), ob = mfma(vb, p, z);
      u32x4 w; w.x = pk(oa[0], oa[1]); w.y = pk(oa[2], oa[3]); w.z = pk(ob[0], ob[1]); w.w = pk(ob[2], ob[3]);
      st16(of + m * 1024, w);
    }
  }
  BW_OUT(0);
  STAMP_END(0);
}

#define K4_LAG 2
#define K4_LEAD 4
#define K4_BLEAD 0
#define K2_BLEAD 1
#define K4_SLEEP 2
#define K4_S 52
#define K4_POLL 2
#define K2_S 12
#define K2_POLL 2
template <int V> struct IC { static constexpr int value = V; };
template <int I, int E, class Fn> DI void sfor(Fn&& f) { if constexpr (I < E) { f(IC<I>{}); sfor<I + 1, E>(f); } }
constexpr int K4_SLOT = 16384, K4_NS = 8, K4_SCR = K4_SLOT * K4_NS, K4_LDS = K4_SCR + 256, K4_R = 112 - K4_S;
constexpr int K2_R = 32 - K2_S;
static_assert(K4_S >= 50 && K4_S <= 54 && K4_S % 2 == 0 && K2_S % 2 == 0, "");

template <int MODE>
DI void proj_loaders(const u16* __restrict__ wmat, char* __restrict__ ws, const unsigned L0, const int wid, const uint64_t nextk) {
  constexpr int S = MODE ? K4_S : K2_S, R = MODE ? K4_R : K2_R, NKS = S + R, BL = MODE ? K4_BLEAD : K2_BLEAD;
  const int lane = threadIdx.x & 63;
  BW_DECL();
  const int xcd = blockIdx.x & 7, idx = blockIdx.x >> 3;
  const int kh = xcd & 1, pm = (xcd >> 2) * 8 + (idx >> 2), pq = ((xcd >> 1) & 1) * 4 + (idx & 3);
  const int j = wid - 8, ca = idx & 3, cb = idx >> 2;
  const u32x4 srdA = make_srd(ws + (MODE ? WS_ACT : WS_O), NKS * 131072), srdB = make_srd(wmat, NKS * 32 * 2048);
  const unsigned voffA = lane * 16 + pm * 8192;
  const unsigned voffB = (unsigned)(lane >> 3) * 2048 + pq * 256 + (unsigned)((lane & 7) ^ (((lane >> 4) & 3) << 1)) * 16;
  auto run = [&](auto na, auto pa, auto nb, auto lag, int qa, int qb) {
    constexpr int NA = decltype(na)::value, PA = decltype(pa)::value, NB = decltype(nb)::value, LG = decltype(lag)::value;
    constexpr int NF = NA + NB, NO = (NA + 1 - PA) / 2 + NB;
    static_assert(LG <= 4, "");
    unsigned va[NA + 1], la[NA + 1], vb[3][NB + 1], lb[NB + 1];
#pragma unroll
    for (int i = 0; i < NA; ++i) { const int q = (qa + i) & 7; va[i] = voffA + (q ^ kh) * 1024; la[i] = L0 + q * 1024; }
#pragma unroll
    for (int i = 0; i < NB; ++i) {
      const int q = (qb + i) & 7, r = q & 3;
      const unsigned v = voffB + (q >> 2) * 128;
      vb[0][i] = v + 8 * r * 2048; vb[1][i] = v + (56 * (r & 1) + 8 * (r >> 1)) * 2048; vb[2][i] = v + (112 * (r & 1) + 4 * (r >> 1) + 52 * (lane >> 5)) * 2048;
      lb[i] = L0 + 8192 + q * 1024;
    }
    unsigned so = 0, sa = kh * ((MODE ? 63 : R) * 131072), sb = kh * (9 * 224 * 2048);
    auto slot = [&](auto tt, int kk) {
      constexpr int t = decltype(tt)::value;
      if (t < R) {
#pragma unroll
        for (int i = 0; i < NA; ++i) if (t < S || ((PA + i) & 1) == 0) dma2(la[i], so, va[i], srdA, sa);
        const unsigned sr = MODE ? sb + (kk < 4 ? 56 * kk : kk < 6 ? 112 * kk - 416 : 48) * 2048 : sa >> 1;
#pragma unroll
        for (int i = 0; i < NB; ++i) dma2(lb[i], so, vb[MODE == 0 || kk < 4 ? 0 : kk < 6 ? 1 : 2][i], srdB, sr);
        so = (so + K4_SLOT) & (K4_NS * K4_SLOT - 1); sa += 131072;
        if (kk == 6) sb += 224 * 2048;
      }
      if (t >= LG && (t - LG) % 2 == 0) {
        constexpr int lo = t - LG + 1, hi = t < R ? t : R - 1;
        constexpr int n = hi >= lo ? hi - lo + 1 : 0, nf = lo < S ? (hi < S ? n : S - lo) : 0;
        WAIT_V(nf * NF + (n - nf) * NO);
        BARRIER();
      }
    };
    sfor<0, LG + 1>([&](auto tt) { slot(tt, decltype(tt)::value); });
    if (NA == 2) {
      constexpr int SR = MODE ? 63 : 32;
      const unsigned voffS = (unsigned)(kh * (MODE ? 1568 : 0) + SR * idx + (lane < SR ? lane : SR - 1)) * 2048 + ((xcd >> 1) & 1) * 1024 + ((3 - (int)((uint64_t)wmat >> 7)) & 7) * 128;
      touch_b(L0 + K4_SCR, voffS, srdB, 0);
    }
    if (MODE) {
      constexpr int UN = NB ? 14 : 2;
#pragma nounroll
      for (int i = 0; i < 42 / UN; ++i) sfor<LG + 1, LG + 1 + UN>([&](auto tt) { slot(tt, decltype(tt)::value % 7); });
      sfor<LG + 43, 49>([&](auto tt) { slot(tt, decltype(tt)::value % 7); });
      if (kh || NB == 0) {
        sa = (kh ? R : 49) * 131072; sb = 8 * 224 * 2048;
        sfor<49, S>([&](auto tt) { slot(tt, decltype(tt)::value - 49 + R - 56); });
      } else sfor<49, S>([&](auto tt) { slot(tt, decltype(tt)::value - 49); });
    } else {
      constexpr int NIT = (S - LG - 1) / 2;
#pragma nounroll
      for (int i = 0; i < NIT; ++i) sfor<LG + 1, LG + 3>([&](auto tt) { slot(tt, 0); });
      sfor<LG + 1 + 2 * NIT, S>([&](auto tt) { slot(tt, 0); });
    }
    sa = S * 131072; sb = 7 * 224 * 2048;
    sfor<S, R + LG + 1>([&](auto tt) { slot(tt, MODE ? decltype(tt)::value % 7 : 0); });
    if (MODE == 0 && NA == 2) { touch_g(L0 + K4_SCR, lane * 128, nextk); touch_g(L0 + K4_SCR, lane * 128, nextk + 8192); }
  };
  if (j == 4) run(IC<2>{}, IC<0>{}, IC<BL>{}, IC<K4_LEAD>{}, 2 * ca, cb);
  else if (j == 0) run(IC<3>{}, IC<0>{}, IC<0>{}, IC<K4_LAG>{}, 2 * ca + 2, 0);
  else if (j == 1) run(IC<3>{}, IC<1>{}, IC<0>{}, IC<K4_LAG>{}, 2 * ca + 5, 0);
  else if (BL && j == 3) run(IC<0>{}, IC<0>{}, IC<3>{}, IC<K4_LAG>{}, 0, cb + 5);
  else run(IC<0>{}, IC<0>{}, IC<4>{}, IC<K4_LAG>{}, 0, BL ? cb + 1 : 4 * j);
  BW_OUT(1 + 2 * MODE);
}
DI unsigned xword(unsigned v) { return v < 0xFFFFFFFEu ? v : 0xFFFFFFFEu; }
DI unsigned max4(u32x4 v) { return max(max(v.x, v.y), max(v.z, v.w)); }

constexpr int K2_TAB = K4_LDS, K2_X = K2_TAB + 6144, K2_LDS = K2_X + 16384;
struct K2Frag { u32x4 a[2]; u32x2 b[4][2]; u32x4 w; };
extern "C" __global__ __launch_bounds__(576, 3) void k2_wo(const u16* __restrict__ x, const u16* __restrict__ wo, char* __restrict__ ws) {
  __shared__ __attribute__((aligned(1024))) char smem[K2_LDS];
  const unsigned L0 = (unsigned)(size_t)(LAS char*)smem;
  const int tid = threadIdx.x, lane = tid & 63;
  const int wid = __builtin_amdgcn_readfirstlane(tid >> 6);
  BW_DECL();
  const int xcd = blockIdx.x & 7, idx = blockIdx.x >> 3;
  const int pm = (xcd >> 1) * 4 + (idx >> 3), pn = (xcd & 1) * 8 + (idx & 7);
  if (wid >= 4) {
    const int j = wid - 4, ca = idx & 7, cb = idx >> 3;
    const u32x4 srdA = make_srd(ws + WS_O, 32 * 131072), srdB = make_srd(wo, D_ * D_ * 2);
    const unsigned voffA = lane * 16 + pm * 8192;
    const unsigned voffB = (unsigned)(lane >> 3) * 2048 + pn * 128 + (unsigned)((lane & 7) ^ (((lane >> 4) & 3) << 1)) * 16;
    auto run = [&](auto na, auto nb, auto lag, int qa, int qb) {
      constexpr int NA = decltype(na)::value, NB = decltype(nb)::value, LG = decltype(lag)::value, N = NA + NB;
      unsigned va[NA + 1], la[NA + 1], vb[NB + 1], lb[NB + 1];
#pragma unroll
      for (int i = 0; i < NA; ++i) { const int q = (qa + i) & 7; va[i] = voffA + q * 1024; la[i] = L0 + q * 1024; }
#pragma unroll
      for (int i = 0; i < NB; ++i) { const int q = (qb + i) & 3; vb[i] = voffB + q * 16384; lb[i] = L0 + 8192 + q * 1024; }
      unsigned so = 0, sa = 0;
      auto issue = [&]() {
#pragma unroll
        for (int i = 0; i < NA; ++i) dma2(la[i], so, va[i], srdA, sa);
        const unsigned sr = sa >> 1;
#pragma unroll
        for (int i = 0; i < NB; ++i) dma2(lb[i], so, vb[i], srdB, sr);
        so = (so + K4_SLOT) & (K4_NS * K4_SLOT - 1); sa += 131072;
      };
#pragma unroll
      for (int u = 0; u <= LG; ++u) issue();
      WAIT_V(N * LG); BARRIER();
#pragma nounroll
      for (int i = 0; i < (30 - LG) / 2; ++i) { issue(); issue(); WAIT_V(N * LG); BARRIER(); }
      issue();
      WAIT_V(N * (LG - 1)); BARRIER();
      if (LG == 4) { WAIT_V(N); BARRIER(); }
      WAIT_V(0); BARRIER();
    };
    if (j == 4) run(IC<1>{}, IC<1>{}, IC<4>{}, ca, cb);
    else if (j == 0) run(IC<4>{}, IC<0>{}, IC<2>{}, ca + 1, 0);
    else if (j == 1) run(IC<3>{}, IC<0>{}, IC<2>{}, ca + 5, 0);
    else if (j == 2) run(IC<0>{}, IC<3>{}, IC<2>{}, 0, cb + 1);
    else {
      const u32x4 srdT = make_srd(ws + WS_WTAB, 6144), srdX = make_srd(x, S_ * D_ * 2);
#pragma unroll
      for (int q = 0; q < 6; ++q) dma(L0 + K2_TAB + q * 1024, lane * 16, srdT, q * 1024);
      WAIT_V(0); BARRIER();
      touch_b(L0 + K4_SCR, (unsigned)(32 * idx + (lane & 31)) * 2048 + ((xcd & 1) * 8 + ((3 - (int)((uint64_t)wo >> 7)) & 7)) * 128, srdB, 0);
      unsigned vx[2];
#pragma unroll
      for (int i = 0; i < 2; ++i) vx[i] = (unsigned)(pm * 128 + 8 * i + (lane >> 3)) * 2048 + pn * 128 + (unsigned)((lane & 7) ^ (4 * i + (lane >> 4))) * 16;
      BARRIER();
#pragma nounroll
      for (int i = 0; i < 8; ++i) { dma(L0 + K2_X + i * 2048, vx[0], srdX, i * 32768); dma(L0 + K2_X + i * 2048 + 1024, vx[1], srdX, i * 32768); BARRIER(); }
      CODE_ADDR(nextk, k3_gateup);
      touch_g(L0 + K4_SCR, lane * 128, nextk); touch_g(L0 + K4_SCR, lane * 128, nextk + 8192);
      BARRIER(); BARRIER();
      WAIT_V(2);
#pragma nounroll
      for (int i = 0; i < 5; ++i) BARRIER();
    }
    BW_OUT(1);
    return;
  }
  const int g = wid, fr = lane & 15, fq = lane >> 4;
  STAMP_BEG(1);
  const int tok = pm * 128 + 32 * g + fr;
  float rmu[2];
  {
    const float* q = (const float*)(ws + WS_MUX) + tok;
    asm volatile("global_load_dword %0, %1, off" : "=v"(rmu[0]) : "v"(q));
    asm volatile("global_load_dword %0, %1, off offset:64" : "=v"(rmu[1]) : "v"(q));
  }
  f32x4 acc[2][4], accW[2];
#pragma unroll
  for (int m = 0; m < 2; ++m) {
    accW[m] = (f32x4){0, 0, 0, 0};
#pragma unroll
    for (int n = 0; n < 4; ++n) acc[m][n] = (f32x4){0, 0, 0, 0};
  }
  unsigned ra = L0 + g * 2048 + lane * 16, rb[4];
  {
    const int q = fr >> 2, p = lane & 3, sw = (2 * fq + (q >> 1)) & 3;
#pragma unroll
    for (int n = 0; n < 4; ++n) rb[n] = L0 + 8192 + (unsigned)(4 * fq + q) * 128 + (unsigned)(n ^ sw) * 32 + 8 * p;
  }
  unsigned rw = L0 + K2_TAB + fq * 48 + (fr < 2 ? fr : 2) * 16;
  K2Frag F0, F1;
  auto rd = [&](K2Frag& F, int i, int sl) {
    if (i < 2) F.a[i] = lds128(ra + sl * K4_SLOT + i * 1024);
    else if (i == 10) F.w = lds128(rw + sl * 192);
    else { const int e = i - 2; F.b[e >> 1][e & 1] = ldstr(rb[e >> 1] + sl * K4_SLOT + (e & 1) * 2048); }
  };
  auto step = [&](K2Frag& F, K2Frag& N, int sl, unsigned dl) {
#pragma unroll
    for (int i = 0; i < 10; ++i) {
      const int n = i >> 1, m = i & 1;
      if (n < 4) acc[m][n] = mfma(cat(F.b[n][0], F.b[n][1]), F.a[m], acc[m][n]);
      else accW[m] = mfma(F.w, F.a[m], accW[m]);
      if (sl >= 0 && i < 6) { rd(N, 2 * i, sl); if (i < 5) rd(N, 2 * i + 1, sl); }
      if (sl == 3 && i == 6) { VADD(ra, dl); VADD(rb[0], dl); }
      if (sl == 3 && i == 7) { VADD(rb[1], dl); VADD(rb[2], dl); }
      if (sl == 3 && i == 8) { const unsigned c = 768; VADD(rb[3], dl); VADD(rw, c); }
      SB();
    }
  };
  BARRIER();
#pragma unroll
  for (int i = 0; i < 11; ++i) rd(F0, i, 0);
  WAIT_L0(); BARRIER();
  STAMP_L0();
  unsigned dl = 4 * K4_SLOT;
#pragma nounroll
  for (int it = 0; it < 28; it += 4) {
    step(F0, F1, 1, 0);
    step(F1, F0, 2, 0); WAIT_L0(); BARRIER();
    step(F0, F1, 3, dl);
    step(F1, F0, 0, 0); WAIT_L0(); BARRIER();
    dl = 0u - dl;
  }
  step(F0, F1, 1, 0);
  {
    const unsigned rx = L0 + K2_X + g * 4096 + fr * 128 + (fq & 1) * 8;
#pragma unroll
    for (int m = 0; m < 2; ++m)
#pragma unroll
      for (int n = 0; n < 4; ++n) {
        const u32x2 v = *(LAS u32x2*)(rx + m * 2048 + (unsigned)((2 * n + (fq >> 1)) ^ (fr >> 1)) * 16);
        acc[m][n] += (f32x4){bflo(v.x), bfhi(v.x), bflo(v.y), bfhi(v.y)};
      }
    SB();
  }
  step(F1, F0, 2, 0); WAIT_L0(); BARRIER();
  step(F0, F1, 3, 0);
  step(F1, F0, -1, 0);
  WAIT_V(0);
  STAMP_L1(1);
  asm volatile("" : "+v"(rmu[0]), "+v"(rmu[1]));
#pragma unroll
  for (int m = 0; m < 2; ++m) {
    const float mu = rmu[m] + red_add(accW[m][0] + accW[m][1]);
    char* hb = ws + WS_HC + (size_t)(2 * pn) * 131072 + (size_t)(pm * 8 + 2 * g + m) * 1024 + lane * 16;
    float ss = 0.f;
#pragma unroll
    for (int c = 0; c < 2; ++c) {
      const f32x4 y0 = acc[m][2 * c] - mu, y1 = acc[m][2 * c + 1] - mu;
      const u32x4 hp = {pk(y0[0], y0[1]), pk(y0[2], y0[3]), pk(y1[0], y1[1]), pk(y1[2], y1[3])};
      st16(hb + c * 131072, hp);
#pragma unroll
      for (int e = 0; e < 4; ++e) ss = dot2(hp[e], hp[e], ss);
    }
    ss = red_add(ss);
    if (fq == 0) {
      ((float*)(ws + WS_HC + (size_t)32 * 131072))[(pm * 8 + 2 * g + m) * 256 + (pn >> 2) * 64 + fr * 4 + (pn & 3)] = ss;
      if (pn == 0) ((float*)(ws + WS_MU))[tok + 16 * m] = mu;
    }
  }
  BW_OUT(1);
  STAMP_END(1);
}

struct K4Frag { u32x4 a[4]; u32x2 b[2][2]; };
extern "C" __global__ __launch_bounds__(832, 4) void k4_down(const u16* __restrict__ wd, char* __restrict__ ws, u16* __restrict__ out) {
  __shared__ __attribute__((aligned(1024))) char smem[K4_LDS];
  const unsigned L0 = (unsigned)(size_t)(LAS char*)smem;
  const int tid = threadIdx.x, lane = tid & 63;
  const int wid = __builtin_amdgcn_readfirstlane(tid >> 6);
  if (wid >= 8) { proj_loaders<1>(wd, ws, L0, wid, 0); return; }
  BW_DECL();
  const int xcd = blockIdx.x & 7, idx = blockIdx.x >> 3;
  const int kh = xcd & 1, pm = (xcd >> 2) * 8 + (idx >> 2), pq = ((xcd >> 1) & 1) * 4 + (idx & 3);
  const int gm = wid & 1, gn = wid >> 1, fr = lane & 15, fq = lane >> 4;
  STAMP_BEG(3);
  const int mt = pm * 8 + 4 * gm + kh, tok = mt * 16 + fr;
  u32x4 rh[2]; float rmu[2];
  {
    const char* p = ws + WS_HC + (size_t)(4 * pq + gn) * 131072 + (size_t)mt * 1024 + lane * 16;
    asm volatile("global_load_dwordx4 %0, %1, off" : "=v"(rh[0]) : "v"(p));
    asm volatile("global_load_dwordx4 %0, %1, off offset:2048" : "=v"(rh[1]) : "v"(p));
    const float* q = (const float*)(ws + WS_MU) + tok;
    asm volatile("global_load_dword %0, %1, off" : "=v"(rmu[0]) : "v"(q));
    asm volatile("global_load_dword %0, %1, off offset:128" : "=v"(rmu[1]) : "v"(q));
  }
  f32x4 acc[4][2];
#pragma unroll
  for (int m = 0; m < 4; ++m) acc[m][0] = acc[m][1] = (f32x4){0, 0, 0, 0};
  unsigned ra = L0 + gm * 4096 + lane * 16, rb[2];
  {
    const int q = fr >> 2, p = lane & 3, sw = (2 * fq + (q >> 1)) & 3;
#pragma unroll
    for (int n = 0; n < 2; ++n) rb[n] = L0 + 8192 + (gn >> 1) * 4096 + (unsigned)(4 * fq + q) * 128 + (unsigned)((2 * (gn & 1) + n) ^ sw) * 32 + 8 * p;
  }
  K4Frag F0, F1;
  auto rd = [&](K4Frag& F, int i, int sl) {
    if (i == 0 || i == 3 || i >= 6) { const int m = i == 0 ? 0 : i == 3 ? 2 : 2 * i - 11; F.a[m] = lds128(ra + sl * K4_SLOT + m * 1024); }
    else { const int n = i > 3, e = i - 1 - 3 * n; F.b[n][e] = ldstr(rb[n] + sl * K4_SLOT + e * 2048); }
  };
  auto bump = [&](int i, unsigned dl) { if (i == 0) VADD(ra, dl); else VADD(rb[i - 1], dl); };
  auto mm = [&](K4Frag& F, int i) {
    const int m = 2 * (i & 1) + (i >> 2), n = (i >> 1) & 1;
    acc[m][n] = mfma(cat(F.b[n][0], F.b[n][1]), F.a[m], acc[m][n]);
  };
  auto step = [&](K4Frag& F, K4Frag& N, int sl, unsigned dl) {
#pragma unroll
    for (int i = 0; i < 8; ++i) {
      mm(F, i);
      if (i < 4) { rd(N, 2 * i, sl); rd(N, 2 * i + 1, sl); }
      if (sl == 3 && i >= 5) bump(i - 5, dl);
      SB();
    }
  };
  BARRIER();
#pragma unroll
  for (int i = 0; i < 8; ++i) rd(F0, i, 0);
  WAIT_L0(); BARRIER();
  STAMP_L0();
  unsigned dl = 4 * K4_SLOT;
#pragma nounroll
  for (int it = 0; it < 48; it += 4) {
    step(F0, F1, 1, 0);
    step(F1, F0, 2, 0); WAIT_L0(); BARRIER();
    step(F0, F1, 3, dl);
    step(F1, F0, 0, 0); WAIT_L0(); BARRIER();
    dl = 0u - dl;
  }
  char* xo = ws + WS_XCH + (size_t)(blockIdx.x * 8 + wid) * 2048 + lane * 16;
  const char* xi = ws + WS_XCH + (size_t)((blockIdx.x ^ 1) * 8 + wid) * 2048 + lane * 16;
  u32x4 y[2];
  sfor<48, K4_R>([&](auto tt) {
    constexpr int t = decltype(tt)::value, sl = (t + 1) & 3;
    K4Frag& F = t & 1 ? F1 : F0;
    K4Frag& N = t & 1 ? F0 : F1;
    const unsigned d = (t + 1) & 4 ? 0u - 4 * K4_SLOT : 4 * K4_SLOT;
    if (t + 1 < K4_S) step(F, N, sl, d);
    else {
      if (t + 1 == K4_S) {
#pragma unroll
        for (int i = 0; i < 4; ++i) {
          mm(F, 4 + i);
          if (i < 3) { rd(N, 2 * i, sl); rd(N, 2 * i + 1, sl); }
          SB();
        }
      }
#pragma unroll
      for (int i = 0; i < 4; ++i) {
        mm(F, i);
        if (t + 1 > K4_S && t + 1 < K4_R && i < 2) { rd(N, 3 * i, sl); rd(N, 3 * i + 1, sl); rd(N, 3 * i + 2, sl); }
        SB();
      }
      if (sl == 3 && t + 2 < K4_R) { bump(0, d); bump(1, d); bump(2, d); }
      if (t + 1 == K4_S) {
#pragma unroll
        for (int c = 0; c < 2; ++c) {
          const f32x4 p0 = acc[2 * c + 1][0], p1 = acc[2 * c + 1][1];
          const u32x4 v = {xword(pk(p0[0], p0[1])), xword(pk(p0[2], p0[3])), xword(pk(p1[0], p1[1])), xword(pk(p1[2], p1[3]))};
          asm volatile("global_store_dwordx4 %0, %1, off offset:%2 sc1\n\ts_nop 1" ::"v"(xo), "v"(v), "n"(c * 1024) : "memory");
        }
      }
      if (t == K4_S + 1) {
        WAIT_V(2);
        asm volatile("" : "+v"(rh[0]), "+v"(rh[1]), "+v"(rmu[0]), "+v"(rmu[1]));
#pragma unroll
        for (int c = 0; c < 2; ++c) {
          const u32x4 h = rh[c];
          acc[2 * c][0] += (f32x4){bflo(h.x), bfhi(h.x), bflo(h.y), bfhi(h.y)} + rmu[c];
          acc[2 * c][1] += (f32x4){bflo(h.z), bfhi(h.z), bflo(h.w), bfhi(h.w)} + rmu[c];
        }
        SB();
      }
    }
    if (t == K4_R - 1 - K4_POLL)
      asm volatile("global_load_dwordx4 %0, %2, off sc1\n\tglobal_load_dwordx4 %1, %2, off offset:1024 sc1" : "=&v"(y[0]), "=&v"(y[1]) : "v"(xi) : "memory");
    if ((t & 1) && t + 1 < K4_R) { WAIT_L0(); BARRIER(); }
  });
  static_assert(((K4_R - 1) & 7) < 4, "");
  unsigned tw = L0 + 4 * K4_SLOT + wid * 2048 + fr * 64 + (unsigned)((fq >> 1) ^ (fr >> 2)) * 16 + (fq & 1) * 8;
  unsigned tr = L0 + 4 * K4_SLOT + wid * 2048 + (lane >> 2) * 64 + (unsigned)((lane & 3) ^ (lane >> 4)) * 16;
  u16* op = out + (size_t)(mt * 16 + (lane >> 2)) * D_ + pq * 128 + gn * 32 + (lane & 3) * 8;
  asm volatile("" : "+v"(op), "+v"(tw), "+v"(tr));
  STAMP_L1(3);
  asm volatile("s_waitcnt vmcnt(0)" : "+v"(y[0]), "+v"(y[1])::"memory");
#pragma nounroll
  for (int spin = 0; spin < (1 << 18); ++spin) {
    if (__builtin_amdgcn_ballot_w64(max(max4(y[0]), max4(y[1])) == 0xFFFFFFFFu) == 0) break;
    __builtin_amdgcn_s_sleep(K4_SLEEP);
    asm volatile("global_load_dwordx4 %0, %2, off sc1\n\tglobal_load_dwordx4 %1, %2, off offset:1024 sc1\n\ts_waitcnt vmcnt(0)"
                 : "=&v"(y[0]), "=&v"(y[1]) : "v"(xi) : "memory");
  }
#pragma unroll
  for (int c = 0; c < 2; ++c) {
    const f32x4 y0 = (f32x4){bflo(y[c].x), bfhi(y[c].x), bflo(y[c].y), bfhi(y[c].y)} + acc[2 * c][0];
    const f32x4 y1 = (f32x4){bflo(y[c].z), bfhi(y[c].z), bflo(y[c].w), bfhi(y[c].w)} + acc[2 * c][1];
    *(LAS u32x2*)(tw + c * 1024) = (u32x2){pk(y0[0], y0[1]), pk(y0[2], y0[3])};
    *(LAS u32x2*)((tw ^ 32) + c * 1024) = (u32x2){pk(y1[0], y1[1]), pk(y1[2], y1[3])};
  }
  MEMFENCE();
#pragma unroll
  for (int c = 0; c < 2; ++c) *(u32x4*)(op + c * 32 * D_) = lds128(tr + c * 1024);
  BW_OUT(3);
  STAMP_END(3);
}

constexpr int K3_G = 16384, K3_U = K3_G + 7168 + 16, K3_SLOT = 30752, K3_NS = 5, K3_SCR = K3_SLOT * K3_NS, K3_LDS = K3_SCR + 256;
struct K3Frag { u32x4 a[4]; u32x2 b[7][2]; };

DI unsigned geglu2(f32x2 g, f32x2 u, float k1, float k2, float ir2) {
  const f32x2 z = g * (g * g * k2 + k1);
  f32x2 e; e.x = __builtin_amdgcn_exp2f(z.x); e.y = __builtin_amdgcn_exp2f(z.y);
  const f32x2 d = e * ir2 + ir2;
  f32x2 r; r.x = __builtin_amdgcn_rcpf(d.x); r.y = __builtin_amdgcn_rcpf(d.y);
  const f32x2 o = (g * u) * r;
  return pk(o.x, o.y);
}

extern "C" __global__ __launch_bounds__(512, 2) void k3_gateup(const u16* __restrict__ wg, const u16* __restrict__ wu, const u16* __restrict__ wd,
                                                               char* __restrict__ ws) {
  __shared__ __attribute__((aligned(1024))) char smem[K3_LDS];
  const unsigned L0 = (unsigned)(size_t)(LAS char*)smem;
  const int tid = threadIdx.x, lane = tid & 63;
  const int wid = __builtin_amdgcn_readfirstlane(tid >> 6);
  BW_DECL();
  const int wm = wid & 3, wn = wid >> 2, fr = lane & 15, fq = lane >> 4;
  const int xcd = blockIdx.x & 7, idx = blockIdx.x >> 3;
  const int pm = (xcd >> 2) * 4 + (idx >> 3), pn = (xcd & 3) * 8 + (idx & 7);
  STAMP_BEG(2);

  const u32x4 srdA = make_srd(ws + WS_HC, 32 * 131072);
  const u32x4 srdG = make_srd(wg, D_ * F_ * 2), srdU = make_srd(wu, D_ * F_ * 2);
  const unsigned voffA = lane * 16 + wid * 2048 + pm * 16384;
  unsigned voffB;
  const int wb = wid < 7 ? wid : 6;
  { const unsigned L = wb * 64 + lane, r = (L * 4682u) >> 16; voffB = r * 7168 + pn * 224 + (L - r * 14) * 16; }
  const unsigned ldA = L0 + wid * 2048, ldG = L0 + K3_G + wb * 1024, ldU = L0 + K3_U + wb * 1024;
  auto dA = [&](int j, unsigned so, int i) { dma(ldA + so + i * 1024, voffA, srdA, j * 131072 + i * 1024); };
  auto dG = [&](int j, unsigned so) { dma(ldG + so, voffB, srdG, j * (32 * 7168)); };
  auto dU = [&](int j, unsigned so) { dma(ldU + so, voffB, srdU, j * (32 * 7168)); };
#define K3_WAIT(slots, extra) do { WAIT_VL(4 * (slots) + (extra)); BARRIER(); } while (0)
#pragma unroll
  for (int j = 0; j < 3; ++j) { dA(j, j * K3_SLOT, 0); dA(j, j * K3_SLOT, 1); dG(j, j * K3_SLOT); dU(j, j * K3_SLOT); }
  unsigned ra = L0 + wm * 4096 + lane * 16;
  unsigned rb = L0 + K3_G + (unsigned)(4 * fq + (fr >> 2)) * 224 + 112 * wn + (lane & 3) * 8;
  unsigned rm = L0 + K3_G + (unsigned)(4 * fq + (fr >> 2)) * 224 + 112 * wn + 96 + (lane & 1) * 8 + ((lane >> 1) & 1) * (K3_U - K3_G);

  f32x4 acc[4][7];
#pragma unroll
  for (int m = 0; m < 4; ++m)
#pragma unroll
    for (int n = 0; n < 7; ++n) acc[m][n] = (f32x4){0, 0, 0, 0};
  K3Frag F0, F1;
  auto rd = [&](K3Frag& F, int i) {
    if (i < 4) F.a[i] = lds128(ra + i * 1024);
    else {
      const int e = i - 4, n = e >> 1, t = e & 1;
      F.b[n][t] = n < 6 ? ldstr(rb + (n >> 1) * 32 + (n & 1) * (K3_U - K3_G) + t * 3584) : ldstr(rm + t * 3584);
    }
  };
  auto step = [&](K3Frag& F, K3Frag& N, unsigned so1, int ND, int jd, unsigned sd, unsigned sd2, bool AONLY, bool RD) {
    const unsigned dl = so1 == (K3_NS - 1) * K3_SLOT ? (unsigned)(-(K3_NS - 1) * K3_SLOT) : (unsigned)K3_SLOT;
#pragma unroll
    for (int i = 0; i < 28; ++i) {
      const int n = i >> 2, m = i & 3;
      acc[m][n] = mfma(cat(F.b[n][0], F.b[n][1]), F.a[m], acc[m][n]);
      if (RD && i < 18) rd(N, i);
      if (RD && i == 19) VADD(ra, dl);
      if (RD && i == 21) VADD(rb, dl);
      if (RD && i == 23) VADD(rm, dl);
      const int s = ND == 2 ? 10 : 18;
      if (ND > 0) {
        if (i == s) dA(jd, sd, 0);
        if (i == s + 2) dA(jd, sd, 1);
        if (i == s + 4 && !AONLY) dG(jd, sd);
        if (i == s + 6 && !AONLY) dU(jd, sd);
      }
      if (ND > 1) {
        if (i == 18) dA(jd + 1, sd2, 0);
        if (i == 20) dA(jd + 1, sd2, 1);
        if (i == 22) dG(jd + 1, sd2);
        if (i == 24) dU(jd + 1, sd2);
      }
      SB();
    }
  };

  K3_WAIT(2, 0);
#pragma unroll
  for (int i = 0; i < 18; ++i) rd(F0, i);
  { const unsigned dl = K3_SLOT; VADD(ra, dl); VADD(rb, dl); VADD(rm, dl); }
  K3_WAIT(1, 0);
  STAMP_L0();
  step(F0, F1, K3_SLOT, 2, 3, 3 * K3_SLOT, 4 * K3_SLOT, false, true);
  K3_WAIT(2, 0);
  step(F1, F0, 2 * K3_SLOT, 2, 5, 0, K3_SLOT, false, true);
  touch_b(L0 + K3_SCR, (lane < 24 ? lane : 23) * 128 + wm * 3072, wn ? srdU : srdG, 4194304 + blockIdx.x * 12288);
  K3_WAIT(3, 0);
  CODE_ADDR(nextk, k4_down);
  uint64_t here; asm volatile("s_getpc_b64 %0" : "=s"(here));
  unsigned so = 2 * K3_SLOT;
  for (int j = 2; j < 26; j += 2) {
    const unsigned so1 = so == (K3_NS - 1) * K3_SLOT ? 0 : so + K3_SLOT;
    const unsigned so2 = so1 == (K3_NS - 1) * K3_SLOT ? 0 : so1 + K3_SLOT;
    step(F0, F1, so1, 1, j + 5, so, 0, false, true);
    K3_WAIT(3, 0);
    step(F1, F0, so2, 1, j + 6, so1, 0, false, true);
    if (j == 24 - 2 * wid) {
      touch_g(L0 + K3_SCR, lane * 128, (wid & 2) ? here + (wid & 1) * 8192 : nextk + (wid & 1) * 8192);
      char* xc = ws + WS_XCH + (size_t)(blockIdx.x * 8 + wid) * 2048 + lane * 16;
      st16(xc, (u32x4){~0u, ~0u, ~0u, ~0u}); st16(xc + 1024, (u32x4){~0u, ~0u, ~0u, ~0u});
    }
    K3_WAIT(3, 0);
    so = so2;
  }
  f32x4 ssq[4];
  {
    const char* q = ws + WS_HC + (size_t)32 * 131072 + (size_t)(pm * 16 + 4 * wm) * 1024 + lane * 16;
#pragma unroll
    for (int m = 0; m < 4; ++m) asm volatile("global_load_dwordx4 %0, %1, off offset:%2" : "=v"(ssq[m]) : "v"(q), "n"(m * 1024));
  }
  step(F0, F1, 2 * K3_SLOT, 1, 31, 1 * K3_SLOT, 0, false, true);
  K3_WAIT(3, 4);
  {
    const u32x4 srdD = make_srd(wd, D_ * F_ * 2);
    const int l = lane < 56 ? lane : 55, P = (xcd & 1) ? (wid < 7 ? 9 + wid : 8) : wid;
    touch_b(L0 + K3_SCR, (l >> 3) * 2048 + (l & 7) * 128, srdD, (224 * P + 7 * idx) * 2048 + ((xcd >> 1) & 1) * 1024);
  }
  step(F1, F0, 3 * K3_SLOT, 0, 0, 0, 0, false, true);
  WAIT_VL(1); BARRIER();
#define K3_PRIO 0
  if (K3_PRIO == 1) asm volatile("s_cmp_eq_u32 %0, 0\n\ts_setprio 1\n\ts_cbranch_scc0 1f\n\ts_setprio 2\n1:" ::"s"(wn) : "scc");
  if (K3_PRIO == 2) asm volatile("s_cmp_eq_u32 %0, 0\n\ts_cbranch_scc0 1f\n\ts_setprio 2\n1:" ::"s"(wn) : "scc");
  step(F0, F1, 4 * K3_SLOT, 0, 0, 0, 0, false, true);
  step(F1, F0, 0, 0, 0, 0, 0, false, true);
  step(F0, F1, 1 * K3_SLOT, 0, 0, 0, 0, false, true);
  step(F1, F0, 0, 0, 0, 0, 0, false, false);
  if (K3_PRIO == 1) __builtin_amdgcn_s_setprio(0);
  STAMP_L1(2);
  float k1[4], k2[4], ir2[4];
#pragma unroll
  for (int m = 0; m < 4; ++m) {
    asm volatile("" : "+v"(ssq[m]));
    const f32x4 p = ssq[m];
    ir2[m] = red_add((p[0] + p[1]) + (p[2] + p[3])) * (1.0f / D_) + LN_EPS;
    const float r = __builtin_amdgcn_rsqf(ir2[m]);
    const float c1 = -2.0f * 0.7978845608028654f * 1.4426950408889634f, c2 = c1 * 0.044715f;
    k1[m] = c1 * r; k2[m] = c2 * r * r * r;
  }
  const int P = pn >> 1, w = 2 * (pn & 1) + wn, mt0 = pm * 16 + 4 * wm;
  char* ab = ws + WS_ACT + (size_t)(7 * P) * 131072 + (size_t)mt0 * 1024;
  unsigned xb[4][2];
#pragma unroll
  for (int m = 0; m < 4; ++m) {
    unsigned o[3][2];
#pragma unroll
    for (int c = 0; c < 3; ++c) {
      const f32x4 a = acc[m][2 * c], b = acc[m][2 * c + 1];
      o[c][0] = geglu2(a.xy, b.xy, k1[m], k2[m], ir2[m]);
      o[c][1] = geglu2(a.zw, b.zw, k1[m], k2[m], ir2[m]);
    }
    st16(ab + (size_t)w * 131072 + m * 1024 + lane * 16, (u32x4){o[0][0], o[0][1], o[1][0], o[1][1]});
    xb[m][0] = o[2][0]; xb[m][1] = o[2][1];
  }
  const int hf = lane >> 5, fq1 = fq & 1;
  unsigned xm[2][2];
#pragma unroll
  for (int mp = 0; mp < 2; ++mp) {
    swap32u(xb[2 * mp][0], xb[2 * mp + 1][0]); swap32u(xb[2 * mp][1], xb[2 * mp + 1][1]);
    const unsigned slot = (unsigned)((2 * (w & 1) + fq1) * 16 + fr) * 16 + (2 * mp + hf) * 1024;
    st16(ab + (size_t)(4 + (w >> 1)) * 131072 + slot, (u32x4){xb[2 * mp][0], xb[2 * mp][1], xb[2 * mp + 1][0], xb[2 * mp + 1][1]});
    f32x4 a = acc[2 * mp][6], b = acc[2 * mp + 1][6];
#pragma unroll
    for (int r = 0; r < 4; ++r) { float u = a[r], v = b[r]; swap32f(u, v); a[r] = u; b[r] = v; }
    const float q1 = hf ? k1[2 * mp + 1] : k1[2 * mp], q2 = hf ? k2[2 * mp + 1] : k2[2 * mp], qi = hf ? ir2[2 * mp + 1] : ir2[2 * mp];
    xm[mp][0] = geglu2(a.xy, b.xy, q1, q2, qi); xm[mp][1] = geglu2(a.zw, b.zw, q1, q2, qi);
  }
  swap16u(xm[0][0], xm[1][0]); swap16u(xm[0][1], xm[1][1]);
  st16(ab + (size_t)6 * 131072 + (2 * fq1 + hf) * 1024 + w * 256 + fr * 16, (u32x4){xm[0][0], xm[0][1], xm[1][0], xm[1][1]});
  BW_OUT(2);
  STAMP_END(2);
}

extern "C" __global__ void kz_pad() { asm volatile(".fill 4096, 4, 0xBF800000"); }

extern "C" void kernel_launch(void* const* d_in, const int* in_sizes, int n_in, void* d_out, int out_size, void* d_ws, size_t ws_size,
                              hipStream_t stream) {
  const u16* x = (const u16*)d_in[0];
  const u16* wqkv = (const u16*)d_in[1];
  const u16* wo = (const u16*)d_in[2];
  const u16* wg = (const u16*)d_in[4];
  const u16* wu = (const u16*)d_in[5];
  const u16* wd = (const u16*)d_in[6];
  char* ws = (char*)d_ws;
  u16* out = (u16*)d_out;
  k1_qkv_attn<<<256, 768, 0, stream>>>(x, wqkv, wo, wg, wu, ws);
  k2_wo<<<256, 576, 0, stream>>>(x, wo, ws);
  k3_gateup<<<256, 512, 0, stream>>>(wg, wu, wd, ws);
  k4_down<<<256, 832, 0, stream>>>(wd, ws, out);
}
```

```cpp
#include <hip/hip_runtime.h>
#include <hip/hip_bf16.h>
#include <stdint.h>

#pragma clang diagnostic ignored "-Winline-asm"
#pragma clang diagnostic ignored "-Wint-to-pointer-cast"

typedef unsigned short u16;
typedef __bf16 bf16x8 __attribute__((ext_vector_type(8)));
typedef __bf16 bf16x2 __attribute__((ext_vector_type(2)));
typedef short s16x4 __attribute__((ext_vector_type(4)));
typedef float f32x4 __attribute__((ext_vector_type(4)));
typedef float f32x2 __attribute__((ext_vector_type(2)));
typedef unsigned u32x4 __attribute__((ext_vector_type(4)));
typedef unsigned u32x2 __attribute__((ext_vector_type(2)));
#define LAS __attribute__((address_space(3)))
#define DI __device__ __forceinline__

constexpr int S_ = 2048, D_ = 1024, F_ = 3584;
constexpr float LN_EPS = 1e-5f;

constexpr size_t WS_O = 0;
constexpr size_t WS_ACT = 0;
constexpr size_t WS_HC = WS_ACT + (size_t)112 * 131072;
constexpr size_t WS_MUX = WS_HC + (size_t)33 * 131072;
constexpr size_t WS_MU = WS_MUX + 8192;
constexpr size_t WS_WTAB = WS_MU + 8192;
constexpr size_t WS_XCH = WS_WTAB + 8192;

DI u32x4 make_srd(const void* p, unsigned bytes) {
  uint64_t a = (uint64_t)p;
  u32x4 r; r.x = (unsigned)a; r.y = (unsigned)(a >> 32) & 0xffffu; r.z = bytes; r.w = 0x00020000u;
  return r;
}
DI void dma(unsigned lds, unsigned voff, u32x4 srd, unsigned soff) {
  asm volatile("s_mov_b32 m0, %0\n\ts_nop 0\n\tbuffer_load_dwordx4 %1, %2, %3 offen lds" ::"s"(lds), "v"(voff), "s"(srd), "s"(soff) : "m0");
}
DI void dma2(unsigned lds, unsigned so, unsigned voff, u32x4 srd, unsigned soff) {
  asm volatile("s_add_u32 m0, %0, %1\n\ts_nop 0\n\tbuffer_load_dwordx4 %2, %3, %4 offen lds" ::"s"(lds), "s"(so), "v"(voff), "s"(srd), "s"(soff) : "m0", "scc");
}
DI void dma_nt(unsigned lds, unsigned voff, u32x4 srd, unsigned soff) {
  asm volatile("s_mov_b32 m0, %0\n\ts_nop 0\n\tbuffer_load_dwordx4 %1, %2, %3 offen nt lds" ::"s"(lds), "v"(voff), "s"(srd), "s"(soff) : "m0");
}
#define dma1 dma
DI void touch_g(unsigned lds, unsigned voff, uint64_t base) {
  asm volatile("s_mov_b32 m0, %0\n\ts_nop 0\n\tglobal_load_lds_dword %1, %2" ::"s"(lds), "v"(voff), "s"(base) : "m0");
}
DI void touch_b(unsigned lds, unsigned voff, u32x4 srd, unsigned soff) {
  asm volatile("s_mov_b32 m0, %0\n\ts_nop 0\n\tbuffer_load_dword %1, %2, %3 offen lds" ::"s"(lds), "v"(voff), "s"(srd), "s"(soff) : "m0");
}
DI void touch_v(unsigned lds, uint64_t addr) {
  asm volatile("s_mov_b32 m0, %0\n\ts_nop 0\n\tglobal_load_lds_dword %1, off" ::"s"(lds), "v"(addr) : "m0");
}
#define CODE_ADDR(var, sym) uint64_t var; { int d_; asm volatile("s_getpc_b64 %0\n\ts_mov_b32 %1, " #sym "@rel32@lo+4" : "=s"(var), "=s"(d_)); var += (int64_t)d_; }
#define KARG_PAGE() ((uint64_t)__builtin_amdgcn_kernarg_segment_ptr() & ~(uint64_t)4095)
#define VADD(v, s) asm volatile("v_add_u32 %0, %1, %0" : "+v"(v) : "s"(s))
#define SB() __builtin_amdgcn_sched_barrier(0)
#define MEMFENCE() asm volatile("" ::: "memory")
#define WAIT_VL(V) do { SB(); MEMFENCE(); __builtin_amdgcn_s_waitcnt(((V) & 15) | (((V) >> 4) << 14) | 0x70); MEMFENCE(); SB(); } while (0)
#define WAIT_V(V) do { SB(); MEMFENCE(); __builtin_amdgcn_s_waitcnt(((V) & 15) | (((V) >> 4) << 14) | 0xF70); MEMFENCE(); SB(); } while (0)
#define WAIT_L0() do { SB(); MEMFENCE(); __builtin_amdgcn_s_waitcnt(0xC07F); MEMFENCE(); SB(); } while (0)
#define BARRIER() do { SB(); MEMFENCE(); __builtin_amdgcn_s_barrier(); MEMFENCE(); SB(); } while (0)
#define BW_DECL()
#define BW_OUT(kern)

DI u32x4 lds128(unsigned a) { return *(LAS u32x4*)(a); }
DI f32x4 lds128f(unsigned a) { return *(LAS f32x4*)(a); }
DI u32x2 ldstr(unsigned a) { return __builtin_bit_cast(u32x2, __builtin_amdgcn_ds_read_tr16_b64_v4i16((LAS s16x4*)(a))); }
DI f32x4 mfma(u32x4 a, u32x4 b, f32x4 c) {
  return __builtin_amdgcn_mfma_f32_16x16x32_bf16(__builtin_bit_cast(bf16x8, a), __builtin_bit_cast(bf16x8, b), c, 0, 0, 0);
}
DI u32x4 cat(u32x2 lo, u32x2 hi) { u32x4 r; r.x = lo.x; r.y = lo.y; r.z = hi.x; r.w = hi.y; return r; }
DI unsigned pk(float a, float b) { f32x2 v = {a, b}; return __builtin_bit_cast(unsigned, __builtin_convertvector(v, bf16x2)); }
DI float dot2(unsigned a, unsigned b, float c) {
  return __builtin_amdgcn_fdot2_f32_bf16(__builtin_bit_cast(bf16x2, a), __builtin_bit_cast(bf16x2, b), c, false);
}
DI float bflo(unsigned u) { return __uint_as_float(u << 16); }
DI float bfhi(unsigned u) { return __uint_as_float(u & 0xffff0000u); }
DI float red_add(float v) {
  auto a = __builtin_amdgcn_permlane32_swap(__float_as_uint(v), __float_as_uint(v), false, false);
  v = __uint_as_float(a[0]) + __uint_as_float(a[1]);
  auto b = __builtin_amdgcn_permlane16_swap(__float_as_uint(v), __float_as_uint(v), false, false);
  return __uint_as_float(b[0]) + __uint_as_float(b[1]);
}
DI float red_max(float v) {
  auto a = __builtin_amdgcn_permlane32_swap(__float_as_uint(v), __float_as_uint(v), false, false);
  v = fmaxf(__uint_as_float(a[0]), __uint_as_float(a[1]));
  auto b = __builtin_amdgcn_permlane16_swap(__float_as_uint(v), __float_as_uint(v), false, false);
  return fmaxf(__uint_as_float(b[0]), __uint_as_float(b[1]));
}
DI float row_add(float v) {
  v += __uint_as_float(__builtin_amdgcn_update_dpp(0u, __float_as_uint(v), 0x128, 0xf, 0xf, false));
  v += __uint_as_float(__builtin_amdgcn_update_dpp(0u, __float_as_uint(v), 0x124, 0xf, 0xf, false));
  v += __uint_as_float(__builtin_amdgcn_update_dpp(0u, __float_as_uint(v), 0x122, 0xf, 0xf, false));
  v += __uint_as_float(__builtin_amdgcn_update_dpp(0u, __float_as_uint(v), 0x121, 0xf, 0xf, false));
  return v;
}
DI void swap32f(float& a, float& b) {
  auto r = __builtin_amdgcn_permlane32_swap(__float_as_uint(a), __float_as_uint(b), false, false);
  a = __uint_as_float(r[0]); b = __uint_as_float(r[1]);
}
DI void swap32u(unsigned& a, unsigned& b) { auto r = __builtin_amdgcn_permlane32_swap(a, b, false, false); a = r[0]; b = r[1]; }
DI void swap16u(unsigned& a, unsigned& b) { auto r = __builtin_amdgcn_permlane16_swap(a, b, false, false); a = r[0]; b = r[1]; }
constexpr unsigned ONES2 = 0x3F803F80u;
template <int V> struct IC { static constexpr int value = V; };
template <int I, int E, class Fn> DI void sfor(Fn&& f) { if constexpr (I < E) { f(IC<I>{}); sfor<I + 1, E>(f); } }
typedef unsigned u32x6 __attribute__((ext_vector_type(6)));
DI float selm(float a, float b, unsigned long long m) { return __builtin_amdgcn_inverse_ballot_w64(m) ? b : a; }
DI float max2(float a, float b) { return __builtin_elementwise_maximum(a, b); }
DI float max3(float a, float b, float c) { return max2(max2(a, b), c); }
template <class Fn> constexpr unsigned long long lanes(Fn f) { unsigned long long m = 0; for (int l = 0; l < 64; ++l) if (f(l & 15, l >> 4)) m |= 1ull << l; return m; }
#define STMODE 3
DI void st16(void* p, u32x4 v) {
  asm volatile("global_store_dwordx4 %0, %1, off sc1\n\ts_nop 1" :: "v"(p), "v"(v) : "memory");
}
template <int OFF> DI void st16s(uint64_t base, unsigned voff, u32x4 v) {
  asm volatile("global_store_dwordx4 %0, %1, %2 offset:%3 sc1\n\ts_nop 1" ::"v"(voff), "v"(v), "s"(base), "n"(OFF) : "memory");
}
DI void st8(void* p, u32x2 v) {
  asm volatile("global_store_dwordx2 %0, %1, off sc1" :: "v"(p), "v"(v) : "memory");
}
#define STAMP_AT(kern, i)
#define STAMP_RT(kern, i)
#define STAMP_L0()
#define STAMP_L1(kern)
#define STAMP_END(kern)
#define STAMP_BEG(kern)
#define EST_DECL()
#define EST(i)
#define EST_OUT()

extern "C" __global__ void k1_qkv_attn(const u16*, const u16*, const u16*, const u16*, const u16*, char*);
extern "C" __global__ void k2_wo(const u16*, const u16*, const u16*, const u16*, char*);
extern "C" __global__ void k3_gateup(const u16*, const u16*, const u16*, char*);
extern "C" __global__ void k4_down(const u16*, char*, u16*);

constexpr int K1_STAGE = 43008, K1_B = 18432, K1_ST = 3 * K1_STAGE;
constexpr int K1_SCR = K1_ST + 144 * 8, K1_LDS = K1_SCR + 256;
#define PFD 3
#define GUL 64
#define W0R 3
#define K3_ORD 2
#define K1_SPREAD 1
#define K1_G1 8
#define PF0 PFD
struct K1Frag { u32x4 a[2]; u32x4 h; u32x2 b[6][2]; };

extern "C" __global__ __launch_bounds__(768, 3) void k1_qkv_attn(const u16* __restrict__ x, const u16* __restrict__ wqkv, const u16* __restrict__ wo,
                                                                 const u16* __restrict__ wg, const u16* __restrict__ wu, char* __restrict__ ws) {
  __shared__ __attribute__((aligned(1024))) char smem[K1_LDS];
  const unsigned L0 = (unsigned)(size_t)(LAS char*)smem;
  const int tid = threadIdx.x, lane = tid & 63;
  const int wid = __builtin_amdgcn_readfirstlane(tid >> 6);
  BW_DECL();
  const int xcd = blockIdx.x & 7, idx = blockIdx.x >> 3;
  const int pm = (xcd >> 1) * 4 + (idx >> 3), pn = (xcd & 1) * 8 + (idx & 7);
  const int s0 = pm * 128;
  const int l3 = lane >> 3, l7 = lane & 7;

  if (__builtin_expect(wid >= 8, 1)) {
    const int j = wid - 8;
    __builtin_amdgcn_s_setprio(3);
    const u32x4 srdX = make_srd(x, S_ * D_ * 2);
    const u32x4 srdW = make_srd(wqkv, D_ * 3 * D_ * 2);
    unsigned voffA[2], voffB[2];
#pragma unroll
    for (int i = 0; i < 2; ++i) {
      voffA[i] = (unsigned)(s0 + 32 * j + 8 * i + l3) * 2048 + (unsigned)(l7 ^ (4 * i + (lane >> 4))) * 16;
      voffB[i] = (unsigned)(16 * j + 8 * i + l3) * 6144 + pn * 128 + (unsigned)(l7 ^ ((((lane >> 4) & 1) | (i << 1)) << 1)) * 16;
    }
    auto tile = [&](int kt, unsigned so) {
#pragma unroll
      for (int i = 0; i < 4; ++i) dma(L0 + so + j * 4096 + i * 1024, voffA[i & 1], srdX, kt * 128 + (i >> 1) * 32768);
#pragma unroll
      for (int i = 0; i < 6; ++i) dma(L0 + so + K1_B + (i >> 1) * 8192 + j * 2048 + (i & 1) * 1024, voffB[i & 1], srdW, kt * (64 * 6144) + (i >> 1) * 2048);
    };
    tile(0, 0);
    unsigned voffE;
    const unsigned long long emask = j == 0 ? ~0xFFull : ~0ull;
    auto halo = [&](int kt, unsigned so) {
      asm volatile("s_mov_b32 m0, %0\n\ts_mov_b64 exec, %4\n\tbuffer_load_dwordx4 %1, %2, %3 offen lds\n\ts_mov_b64 exec, -1"
                   ::"s"(L0 + so + 16384 + j * 1024), "v"(voffE), "s"(srdX), "s"(kt * 128), "s"(emask) : "m0");
    };
    const unsigned vline = lane * 128;
    CODE_ADDR(self, k1_qkv_attn);
    if (j < 2) {
      const int r = s0 - 16 + 8 * j + l3;
      voffE = (unsigned)(r < 0 ? 0 : r) * 2048 + (unsigned)(l7 ^ (4 * j + (lane >> 4))) * 16;
      halo(0, 0);
      tile(1, K1_STAGE);
      halo(1, K1_STAGE);
      if (j == 0 && lane < 24) *(LAS u32x4*)(L0 + l3 * K1_STAGE + 16384 + l7 * 16) = (u32x4){ONES2, ONES2, ONES2, ONES2};
    } else {
      tile(1, K1_STAGE);
      touch_g(L0 + K1_SCR, vline, self + (j - 2) * 8192);
      voffE = j == 2 ? (blockIdx.x * 8 + l7) * 2048 : (blockIdx.x * 12 + (lane < 12 ? lane : 11)) * 128;
      const int q = idx * 6 + (lane & 7), ps = (xcd & 1) * 8 + ((3 - (int)((uint64_t)wqkv >> 7)) & 7);
      if (j == 3 && lane >= 16 && lane < 22) voffE = (q / 3) * 6144 + (q % 3) * 2048 + ps * 128;
#pragma unroll
      for (int t = PF0; t < PFD + 2; ++t) touch_b(L0 + K1_SCR, voffE, j == 2 ? srdX : srdW, j == 2 ? t * 128 : t * (64 * 6144));
    }
    if (j < 2) WAIT_VL(11); else WAIT_VL(11 + PFD + 2 - PF0);
    BARRIER();
    CODE_ADDR(nextk, k2_wo);
    unsigned so = 2 * K1_STAGE;
#pragma nounroll
    for (int kt = 2; kt < 16; ++kt) {
      if (j < 2) halo(kt, so);
      else if (kt >= 16 - PFD) touch_g(L0 + K1_SCR, vline, j == 2 ? self + 2688 : nextk);
      else touch_b(L0 + K1_SCR, voffE, j == 2 ? srdX : srdW, j == 2 ? (kt + PFD) * 128 : (kt + PFD) * (64 * 6144));
      tile(kt, so);
      so = so == 2 * K1_STAGE ? 0 : so + K1_STAGE;
      if (kt == 2 && j >= 2) WAIT_VL(12 + PFD + 2 - PF0); else WAIT_VL(11);
      BARRIER();
    }
    u32x4 hv[8];
    {
      const int r = s0 - 16 + 4 * j + (lane >> 4);
      const u16* p = x + (size_t)(r < 0 ? 0 : r) * D_ + (lane & 15) * 8;
#pragma unroll
      for (int i = 0; i < 8; ++i) asm volatile("global_load_dwordx4 %0, %1, off offset:%2" : "=v"(hv[i]) : "v"(p), "n"(i * 256));
    }
    u32x4 wrow0, wrow1;
    {
      const u16* p = wo + (size_t)(blockIdx.x * 4 + j) * D_ + lane * 8;
      asm volatile("global_load_dwordx4 %0, %1, off" : "=v"(wrow0) : "v"(p));
      asm volatile("global_load_dwordx4 %0, %1, off offset:1024" : "=v"(wrow1) : "v"(p));
      const u32x4 srdGU = make_srd((j & 1) ? wu : wg, D_ * F_ * 2);
      touch_b(L0 + K1_SCR, (lane < GUL ? lane : GUL - 1) * 128, srdGU, ((j >> 1) * 256 + blockIdx.x) * (GUL * 128));
      touch_b(L0 + K1_SCR, l3 * 2048 + l7 * 128, make_srd(wo, D_ * D_ * 2), (idx * 32 + j * 8) * 2048 + (xcd & 1) * 1024);
    }
    touch_g(L0 + K1_SCR, (lane & 31) * 128, KARG_PAGE());
    WAIT_V(13); BARRIER();
    BARRIER();
    WAIT_V(5);
    {
      float a[2] = {0.f, 0.f}, q[2] = {0.f, 0.f};
#pragma unroll
      for (int i = 0; i < 8; ++i) {
        asm volatile("" : "+v"(hv[i]));
#pragma unroll
        for (int e = 0; e < 4; ++e) { a[e & 1] = dot2(hv[i][e], ONES2, a[e & 1]); q[e & 1] = dot2(hv[i][e], hv[i][e], q[e & 1]); }
      }
      const float sa = row_add(a[0] + a[1]), sq = row_add(q[0] + q[1]);
      const float mean = sa * (1.0f / D_), var = fmaxf(sq * (1.0f / D_) - mean * mean, 0.f);
      if ((lane & 15) == 0) {
        *(LAS float*)(L0 + K1_ST + (4 * j + (lane >> 4)) * 4) = -mean;
        *(LAS float*)(L0 + K1_ST + 576 + (4 * j + (lane >> 4)) * 4) = __builtin_amdgcn_rsqf(var + LN_EPS);
      }
    }
    WAIT_L0();
    BARRIER();
    BARRIER();
    WAIT_V(3);
    asm volatile("" : "+v"(wrow0), "+v"(wrow1));
    float s = 0.f;
#pragma unroll
    for (int e = 0; e < 4; ++e) { s = dot2(wrow0[e], ONES2, s); s = dot2(wrow1[e], ONES2, s); }
    s = red_add(row_add(s)) * (1.0f / D_);
    const unsigned hi = pk(s, 0.f) & 0xffffu;
    const unsigned lo = pk(s - bflo(hi), 0.f) & 0xffffu;
    const int k = blockIdx.x * 4 + j, dd = k & 31;
    u16* t = (u16*)(ws + WS_WTAB) + (k >> 5) * 96 + ((dd >> 2) & 3) * 24 + 4 * (dd >> 4) + (dd & 3);
    if (lane < 3) t[lane * 8] = (u16)(lane == 0 ? hi : lane == 1 ? lo : 0u);
    BW_OUT(0);
    return;
  }

  const int g = wid & 3, hh = wid >> 2, fr = lane & 15, fq = lane >> 4;
  STAMP_BEG(0);
  f32x4 acc[2][6], accHT, accHV, accS, accQ;
  f32x4 bp, bc;
  sfor<0, 4>([&](auto rr) {
    constexpr int r = decltype(rr)::value;
    bp[r] = selm(-INFINITY, 0.f, lanes([](int f, int q) { const int d = f - 4 * q - r; return (d & 1) == 0 && d <= -2; }));
    bc[r] = selm(-INFINITY, 0.f, lanes([](int f, int q) { const int d = f - 4 * q - r; return (d & 1) == 0 && d >= 0 && d <= 14; }));
  });
  constexpr unsigned CT = 2 * K1_STAGE;
  constexpr unsigned CV = CT + 512;
  unsigned ca = L0 + CT + hh * 256 + (g & 1) * 64 + fq * 16, cb = L0 + CV + hh * 128 + (g & 1) * 64 + fr * 4;
  unsigned sa = L0 + K1_ST + g * 128 + fr * 4, sb = L0 + K1_ST + g * 128 + fq * 16;
  unsigned xb = L0 + hh * 10240 + g * 2048 + lane * 16;
  asm volatile("" : "+v"(bp), "+v"(bc), "+v"(ca), "+v"(cb), "+v"(sa), "+v"(sb), "+v"(xb));
  auto mainloop = [&](auto qh, auto hd) {
    constexpr int QH = decltype(qh)::value, HH = decltype(hd)::value;
    int ln = lane;
    asm volatile("" : "+v"(ln));
    const int fr = ln & 15, fq = ln >> 4;
    unsigned ra[2], rh[2], rb[6];
#pragma unroll
    for (int ks = 0; ks < 2; ++ks) {
      const unsigned c = (unsigned)((4 * ks + fq) ^ (fr >> 1)) * 16;
      ra[ks] = L0 + (unsigned)(32 * g + fr) * 128 + c;
      rh[ks] = L0 + 16384 + fr * 128 + c;
    }
    {
      const int q = fr >> 2, p = ln & 3, f = ((q >> 1) & 1) | ((fq & 1) << 1);
#pragma unroll
      for (int j = 0; j < 6; ++j)
        rb[j] = L0 + K1_B + (j >> 1) * 8192 + (unsigned)(8 * fq + q) * 128 + (unsigned)((2 * HH + ((j ^ g) & 1)) ^ f) * 32 + 8 * p;
    }
    accHT = accHV = accS = accQ = (f32x4){0, 0, 0, 0};
    u32x4 onesf = {ONES2, ONES2, ONES2, ONES2};
    asm volatile("" : "+v"(onesf));
#pragma unroll
    for (int m = 0; m < 2; ++m)
#pragma unroll
      for (int j = 0; j < 6; ++j) { acc[m][j] = (f32x4){0, 0, 0, 0}; asm volatile("" : "+v"(acc[m][j])); }
    asm volatile("" : "+v"(accHT), "+v"(accHV), "+v"(accS), "+v"(accQ));
    K1Frag F0, F1;
    auto rd = [&](K1Frag& F, int i, int ks) {
      if (i < 2) F.a[i] = lds128(ra[ks] + i * 2048);
      else if (i == 2) F.h = lds128(rh[ks]);
      else { const int e = i - 3, j = e >> 1, t = e & 1; F.b[j][t] = ldstr(rb[j] + ks * 4096 + t * 512); }
    };
    BARRIER();
#pragma unroll
    for (int i = 0; i < 15; ++i) rd(F0, i, 0);
    WAIT_L0();
    STAMP_L0();
    auto mm = [&](K1Frag& F, int i) {
      if (i < 12) {
        const int j = i >> 1, m = i & 1;
        const u32x4 b = cat(F.b[j][0], F.b[j][1]);
        if (j < 4) acc[m][j] = mfma(b, F.a[m], acc[m][j]); else acc[m][j] = mfma(F.a[m], b, acc[m][j]);
      } else if (i == 12) accHT = mfma(cat(F.b[2 * QH][0], F.b[2 * QH][1]), F.h, accHT);
      else if (i == 13) accS = mfma(onesf, F.a[HH], accS);
      else if (i == 14) accQ = mfma(F.a[HH], F.a[HH], accQ);
      else if (QH == HH) accHV = mfma(F.h, cat(F.b[4][0], F.b[4][1]), accHV);
    };
    unsigned sidx = 0;
#pragma nounroll
    for (int kt = 0; kt < 16; ++kt) {
      const unsigned dl = sidx == 2 ? (unsigned)(-2 * K1_STAGE) : (unsigned)K1_STAGE;
      sidx = sidx == 2 ? 0 : sidx + 1;
#pragma unroll
      for (int i = 0; i < 16; ++i) {
        mm(F0, i);
#pragma unroll
        for (int r = 0; r < 15; ++r) {
          if (r * K1_G1 / 15 == i) rd(F1, r, 1);
          if (r >= 4 && (r & 1) == 0 && (K1_G1 == 8 ? 6 + r / 2 : r * K1_G1 / 15 + 1) == i) VADD(rb[(r - 4) >> 1], dl);
        }
        if (i == 14) VADD(ra[0], dl);
        if (i == 15) VADD(rh[0], dl);
        SB();
      }
      WAIT_L0(); BARRIER();
#pragma unroll
      for (int i = 0; i < 16; ++i) {
        mm(F1, i);
        if (i < 15) rd(F0, i == 2 ? 3 : i == 3 ? 4 : i == 14 ? 2 : i < 2 ? i : i + 1, 0);
        if (i == 9) VADD(ra[1], dl);
        if (i == 13) VADD(rh[1], dl);
        SB();
      }
    }
  };
  if (g < 2) { if (hh == 0) mainloop(IC<0>{}, IC<0>{}); else mainloop(IC<0>{}, IC<1>{}); }
  else { if (hh == 0) mainloop(IC<1>{}, IC<0>{}); else mainloop(IC<1>{}, IC<1>{}); }
  STAMP_L1(0);
  EST_DECL(); EST(0);

  const bool vhalo = (g >> 1) == hh;
  {
    const float dq = selm(selm(accQ[0], accQ[1], lanes([](int r, int) { return (r & 3) == 1; })),
                          selm(accQ[2], accQ[3], lanes([](int r, int) { return (r & 3) == 3; })), lanes([](int r, int) { return (r & 2) != 0; }));
    constexpr unsigned long long DIAG = lanes([](int r, int q) { return q == (r >> 2); });
    const float nmean = accS[0] * (-1.0f / D_), var = max2(dq * (1.0f / D_) - nmean * nmean, 0.f);
    const float rstd = __builtin_amdgcn_rsqf(var + LN_EPS);
    const int tl = 16 * (2 * g + hh) + fr;
    asm volatile("s_mov_b64 exec, %7\n\tds_write_b128 %0, %1\n\ts_mov_b64 exec, %8\n\tds_write2_b32 %2, %3, %4 offset1:144\n\t"
                 "s_mov_b64 exec, %9\n\tds_write_b32 %5, %6\n\ts_mov_b64 exec, -1"
                 ::"v"(ca + (g >> 1) * 128), "v"(accHT), "v"(sa + 64 + hh * 64), "v"(nmean), "v"(rstd), "v"(cb), "v"(accHV[0]),
                   "s"(lanes([](int r, int) { return r == 0; })), "s"(DIAG), "s"(vhalo ? 0xFFFFull : 0ull) : "memory");
    if (pn == 0 && __builtin_amdgcn_inverse_ballot_w64(DIAG)) ((float*)(ws + WS_MUX))[s0 + tl] = -nmean;
  }
  WAIT_L0(); EST(1); BARRIER();
  EST(2);
  u32x4 qf[2], kf[3]; u32x6 vc[2]; float cq[2];
  {
    f32x4 ct[4]; float cv[2];
#pragma unroll
    for (int j = 0; j < 4; ++j) ct[j] = lds128f((ca ^ ((j & 1) * 64)) + (j >> 1) * 128);
#pragma unroll
    for (int j = 0; j < 2; ++j) cv[j] = *(LAS float*)(cb ^ (j * 64));
    float mn[2], rs[2]; f32x4 mv[2], rv[2];
#pragma unroll
    for (int m = 0; m < 2; ++m) {
      mn[m] = *(LAS float*)(sa + 64 + m * 64); rs[m] = *(LAS float*)(sa + 640 + m * 64);
      mv[m] = lds128f(sb + 64 + m * 64); rv[m] = lds128f(sb + 640 + m * 64);
    }
#pragma unroll
    for (int m = 1; m >= 0; --m) {
      const f32x4 k0 = (acc[m][2] + ct[2] * mn[m]) * rs[m], k1 = (acc[m][3] + ct[3] * mn[m]) * rs[m];
      kf[m + 1] = (u32x4){pk(k0[0], k0[1]), pk(k0[2], k0[3]), pk(k1[0], k1[1]), pk(k1[2], k1[3])};
#pragma unroll
      for (int j = 0; j < 2; ++j) {
        const f32x4 v = (acc[m][4 + j] + mv[m] * cv[j]) * rv[m];
        vc[j][2 + 2 * m] = pk(v[0], v[1]); vc[j][3 + 2 * m] = pk(v[2], v[3]);
      }
      if (m == 1) {
        *(LAS u32x2*)(xb + 2048 + 8) = kf[2].xy; *(LAS u32x2*)(xb + 2048) = kf[2].zw;
        *(LAS u32x2*)(xb + 3072 + 8) = (u32x2){vc[0][4], vc[0][5]}; *(LAS u32x2*)(xb + 3072) = (u32x2){vc[1][4], vc[1][5]};
      }
    }
    const unsigned xh = xb - g * 2048 + (g & 1) * 8;
    if (g >= 2) {
      const f32x4 v = (accHT + ct[2] * *(LAS float*)(sa - g * 128)) * *(LAS float*)(sa - g * 128 + 576);
      *(LAS u32x2*)(xh) = (u32x2){pk(v[0], v[1]), pk(v[2], v[3])};
    }
    if (vhalo) {
      const f32x4 v = (accHV + lds128f(sb - g * 128) * cv[0]) * lds128f(sb - g * 128 + 576);
      *(LAS u32x2*)(xh + 1024) = (u32x2){pk(v[0], v[1]), pk(v[2], v[3])};
    }
#pragma unroll
    for (int m = 0; m < 2; ++m) {
      const f32x4 q0 = acc[m][0] + ct[0] * mn[m], q1 = acc[m][1] + ct[1] * mn[m];
      qf[m] = (u32x4){pk(q0[0], q0[1]), pk(q0[2], q0[3]), pk(q1[0], q1[1]), pk(q1[2], q1[3])};
      cq[m] = rs[m] * (0.17677669529663687f * 1.4426950408889634f);
    }
  }
  f32x4 sc[2], sp[2];
  const f32x4 bp0 = bp + ((pm | g) != 0 ? 0.f : -INFINITY);
  sc[0] = mfma(kf[1], qf[0], bc); sp[1] = mfma(kf[1], qf[1], bp); sc[1] = mfma(kf[2], qf[1], bc);
  WAIT_L0(); EST(3); BARRIER();
  EST(4);
  {
    kf[0] = lds128(xb);
#pragma unroll
    for (int j = 0; j < 2; ++j) { const u32x2 t = *(volatile LAS u32x2*)(xb + 1024 + j * 8); vc[j][0] = t.x; vc[j][1] = t.y; }
    sp[0] = mfma(kf[0], qf[0], bp0);
    constexpr unsigned long long ODD = lanes([](int r, int) { return (r & 1) != 0; });
    float s[2][4], mx[2], sum[2];
#pragma unroll
    for (int m = 1; m >= 0; --m) {
      s[m][0] = selm(sp[m][0], sp[m][1], ODD); s[m][1] = selm(sp[m][2], sp[m][3], ODD);
      s[m][2] = selm(sc[m][0], sc[m][1], ODD); s[m][3] = selm(sc[m][2], sc[m][3], ODD);
      mx[m] = max2(max3(s[m][0], s[m][1], s[m][2]), s[m][3]);
    }
    {
      swap32f(mx[0], mx[1]);
      float t = max2(mx[0], mx[1]), u = t;
      unsigned a = __float_as_uint(t), b = __float_as_uint(u); swap16u(a, b);
      mx[0] = mx[1] = max2(__uint_as_float(a), __uint_as_float(b));
      swap32f(mx[0], mx[1]);
    }
    const unsigned sh = (lane & 1) * 16;
    u32x4 p[2];
#pragma unroll
    for (int m = 0; m < 2; ++m) {
      const float mc = mx[m] * cq[m];
#pragma unroll
      for (int e = 0; e < 4; ++e) { s[m][e] = __builtin_amdgcn_exp2f(s[m][e] * cq[m] - mc); p[m][e] = pk(s[m][e], 0.f) << sh; }
      sum[m] = (s[m][0] + s[m][1]) + (s[m][2] + s[m][3]);
    }
    f32x4 o[2][2];
    const f32x4 z = {0, 0, 0, 0};
#pragma unroll
    for (int j = 0; j < 2; ++j) {
      o[0][j] = mfma(__builtin_shufflevector(vc[j], vc[j], 0, 1, 2, 3), p[0], z);
      o[1][j] = mfma(__builtin_shufflevector(vc[j], vc[j], 2, 3, 4, 5), p[1], z);
    }
    {
      swap32f(sum[0], sum[1]);
      unsigned a = __float_as_uint(sum[0] + sum[1]), b = a; swap16u(a, b);
      sum[0] = sum[1] = __builtin_amdgcn_rcpf(__uint_as_float(a) + __uint_as_float(b));
      swap32f(sum[0], sum[1]);
    }
    char* of = ws + WS_O + (size_t)(2 * pn + hh) * 131072 + (size_t)(pm * 8 + 2 * g) * 1024 + lane * 16;
    u32x4 w[2];
#pragma unroll
    for (int m = 0; m < 2; ++m) {
      const f32x4 oa = o[m][0] * sum[m], ob = o[m][1] * sum[m];
      w[m] = (u32x4){pk(oa[0], oa[1]), pk(oa[2], oa[3]), pk(ob[0], ob[1]), pk(ob[2], ob[3])};
    }
    {
      unsigned a0 = w[0].x, a1 = w[0].y, a2 = w[0].z, a3 = w[0].w, b0 = w[1].x, b1 = w[1].y, b2 = w[1].z, b3 = w[1].w;
      asm volatile("s_bitcmp0_b32 %8, 0\n\ts_cbranch_scc1 1f\n\tv_swap_b32 %0, %2\n\tv_swap_b32 %1, %3\n\tv_swap_b32 %4, %6\n\tv_swap_b32 %5, %7\n1:"
                   : "+v"(a0), "+v"(a1), "+v"(a2), "+v"(a3), "+v"(b0), "+v"(b1), "+v"(b2), "+v"(b3) : "s"(g) : "scc");
      st16(of, (u32x4){a0, a1, a2, a3}); st16(of + 1024, (u32x4){b0, b1, b2, b3});
    }
  }
  EST(5); EST_OUT();
  BW_OUT(0);
  STAMP_END(0);
}

#define K4_LAG 2
#define K4_LEAD 3
#define K4_BLEAD 1
#define K2_BLEAD 1
#define K4_JIT 2
#define K4_SLEEP 2
#define K4_S 52
#define K4_POLL 3
#define K2_S 12
#define K2_POLL 2
constexpr int K4_SLOT = 16384, K4_NS = 8, K4_SCR = K4_SLOT * K4_NS, K4_LDS = K4_SCR + 256, K4_R = 112 - K4_S;
constexpr int K2_R = 32 - K2_S;
static_assert(K4_S >= 50 && K4_S <= 54 && K4_S % 2 == 0 && K2_S % 2 == 0, "");

DI void k4_loaders(const u16* __restrict__ wmat, char* __restrict__ ws, const unsigned L0, const int wid) {
  constexpr int S = K4_S, R = K4_R, BL = K4_BLEAD;
  const int lane = threadIdx.x & 63;
  BW_DECL();
  const int xcd = blockIdx.x & 7, idx = blockIdx.x >> 3;
  const int kh = xcd & 1, pm = (xcd >> 2) * 8 + (idx >> 2), pq = ((xcd >> 1) & 1) * 4 + (idx & 3);
  const int j = wid - 8, ca = idx & 3, cb = idx >> 2;
#define LPRIO 3
  __builtin_amdgcn_s_setprio(LPRIO);
  const u32x4 srdA = make_srd(ws + WS_ACT, 112 * 131072), srdB = make_srd(wmat, F_ * D_ * 2);
  const unsigned voffA = lane * 16 + pm * 8192;
  const unsigned voffB = (unsigned)(lane >> 3) * 2048 + pq * 256 + (unsigned)((lane & 7) ^ (((lane >> 4) & 3) << 1)) * 16;
  auto run = [&](auto na, auto pa, auto nb, auto lag, int qa, int qb) {
    constexpr int NA = decltype(na)::value, PA = decltype(pa)::value, NB = decltype(nb)::value, LG = decltype(lag)::value;
    constexpr int NF = NA + NB, NO = (NA + 1 - PA) / 2 + NB;
    static_assert(LG <= 3, "");
    unsigned va[NA + 1], la[NA + 1], vb[3][NB + 1], lb[NB + 1];
#pragma unroll
    for (int i = 0; i < NA; ++i) { const int q = (qa + i) & 7; va[i] = voffA + (q ^ kh) * 1024; la[i] = L0 + q * 1024; }
#pragma unroll
    for (int i = 0; i < NB; ++i) {
      const int q = (qb + i) & 7, r = q & 3;
      const unsigned v = voffB + (q >> 2) * 128;
      vb[0][i] = v + 8 * r * 2048; vb[1][i] = v + (56 * (r & 1) + 8 * (r >> 1)) * 2048; vb[2][i] = v + (112 * (r & 1) + 4 * (r >> 1) + 52 * (lane >> 5)) * 2048;
      lb[i] = L0 + 8192 + q * 1024;
    }
    unsigned so = 0, sa = kh * (63 * 131072), sb = kh * (9 * 224 * 2048);
    const unsigned voffJ = (unsigned)(7 * idx + (lane < 7 ? lane : 6)) * 2048 + ((xcd >> 1) & 1) * 1024 + ((3 - (int)((uint64_t)wmat >> 7)) & 7) * 128;
    auto slot = [&](auto tt, int kk) {
      constexpr int t = decltype(tt)::value;
      if (t < R) {
#pragma unroll
        for (int i = 0; i < NA; ++i) if (t < S || ((PA + i) & 1) == 0) dma2(la[i], so, va[i], srdA, sa);
        const unsigned sr = sb + (kk < 4 ? 56 * kk : kk < 6 ? 112 * kk - 416 : 48) * 2048;
#pragma unroll
        for (int i = 0; i < NB; ++i) dma2(lb[i], so, vb[kk < 4 ? 0 : kk < 6 ? 1 : 2][i], srdB, sr);
        if (NA == 2 && kk == 0 && t < 49 && t > 0) {
          const unsigned s2 = sb + 2 * 224 * 2048;
          touch_b(L0 + K4_SCR, voffJ, srdB, s2 >= 3584 * 2048 ? s2 - 9 * 224 * 2048 : s2);
        }
        so = (so + K4_SLOT) & (K4_NS * K4_SLOT - 1); sa += 131072;
        if (kk == 6) sb += 224 * 2048;
      }
      if (t > LG && (t - LG) % 2 == 1) {
        constexpr int lo = t - LG + 1, hi = t < R ? t : R - 1;
        constexpr int n = hi >= lo ? hi - lo + 1 : 0, nf = lo < S ? (hi < S ? n : S - lo) : 0;
        WAIT_V(nf * NF + (n - nf) * NO);
        BARRIER();
      }
    };
    sfor<0, LG + 2>([&](auto tt) { slot(tt, decltype(tt)::value); });
    if (NA == 2) { touch_b(L0 + K4_SCR, voffJ, srdB, sb + 224 * 2048); touch_b(L0 + K4_SCR, voffJ, srdB, sb + 448 * 2048); }
    constexpr int UN = NB ? 14 : 2;
#pragma nounroll
    for (int i = 0; i < 42 / UN; ++i) sfor<LG + 2, LG + 2 + UN>([&](auto tt) { slot(tt, decltype(tt)::value % 7); });
    sfor<LG + 44, 49>([&](auto tt) { slot(tt, decltype(tt)::value % 7); });
    if (kh || NB == 0) {
      sa = (kh ? R : 49) * 131072; sb = 8 * 224 * 2048;
      sfor<49, S>([&](auto tt) { slot(tt, decltype(tt)::value - 49 + R - 56); });
    } else sfor<49, S>([&](auto tt) { slot(tt, decltype(tt)::value - 49); });
    sa = S * 131072; sb = 7 * 224 * 2048;
    sfor<S, R + LG>([&](auto tt) { slot(tt, decltype(tt)::value % 7); });
  };
  if (j == 4) run(IC<2>{}, IC<0>{}, IC<BL>{}, IC<K4_LEAD>{}, 2 * ca, cb);
  else if (j == 0) run(IC<3>{}, IC<0>{}, IC<0>{}, IC<K4_LAG>{}, 2 * ca + 2, 0);
  else if (j == 1) run(IC<3>{}, IC<1>{}, IC<0>{}, IC<K4_LAG>{}, 2 * ca + 5, 0);
  else if (BL && j == 3) run(IC<0>{}, IC<0>{}, IC<3>{}, IC<K4_LAG>{}, 0, cb + 5);
  else run(IC<0>{}, IC<0>{}, IC<4>{}, IC<K4_LAG>{}, 0, BL ? cb + 1 : 4 * j);
  BW_OUT(3);
}
DI unsigned xword(unsigned v) { return v < 0xFFFFFFFEu ? v : 0xFFFFFFFEu; }
DI unsigned max4(u32x4 v) { return max(max(v.x, v.y), max(v.z, v.w)); }

constexpr int K2_TAB = K4_LDS, K2_X = K2_TAB + 6144, K2_LDS = K2_X + 16384;
struct K2Frag { u32x4 a[2]; u32x2 b[4][2]; u32x4 w; };
extern "C" __global__ __launch_bounds__(576, 3) void k2_wo(const u16* __restrict__ x, const u16* __restrict__ wo, const u16* __restrict__ wg,
                                                           const u16* __restrict__ wu, char* __restrict__ ws) {
  __shared__ __attribute__((aligned(1024))) char smem[K2_LDS];
  const unsigned L0 = (unsigned)(size_t)(LAS char*)smem;
  const int tid = threadIdx.x, lane = tid & 63;
  const int wid = __builtin_amdgcn_readfirstlane(tid >> 6);
  BW_DECL();
  const int xcd = blockIdx.x & 7, idx = blockIdx.x >> 3;
  const int pm = (xcd >> 1) * 4 + (idx >> 3), pn = (xcd & 1) * 8 + (idx & 7);
  if (__builtin_expect(wid >= 4, 1)) {
    const int j = wid - 4, ca = idx & 7, cb = idx >> 3;
    __builtin_amdgcn_s_setprio(LPRIO);
    const u32x4 srdA = make_srd(ws + WS_O, 32 * 131072), srdB = make_srd(wo, D_ * D_ * 2);
    const unsigned voffA = lane * 16 + pm * 8192;
    const unsigned voffB = (unsigned)(lane >> 3) * 2048 + pn * 128 + (unsigned)((lane & 7) ^ (((lane >> 4) & 3) << 1)) * 16;
    auto run = [&](auto na, auto nb, auto lag, int qa, int qb) {
      constexpr int NA = decltype(na)::value, NB = decltype(nb)::value, LG = decltype(lag)::value, N = NA + NB;
      unsigned va[NA + 1], la[NA + 1], vb[NB + 1], lb[NB + 1];
#pragma unroll
      for (int i = 0; i < NA; ++i) { const int q = (qa + i) & 7; va[i] = voffA + q * 1024; la[i] = L0 + q * 1024; }
#pragma unroll
      for (int i = 0; i < NB; ++i) { const int q = (qb + i) & 3; vb[i] = voffB + q * 16384; lb[i] = L0 + 8192 + q * 1024; }
      unsigned so = 0, sa = 0;
      auto issue = [&]() {
#pragma unroll
        for (int i = 0; i < NA; ++i) dma2(la[i], so, va[i], srdA, sa);
        const unsigned sr = sa >> 1;
#pragma unroll
        for (int i = 0; i < NB; ++i) dma2(lb[i], so, vb[i], srdB, sr);
        so = (so + K4_SLOT) & (K4_NS * K4_SLOT - 1); sa += 131072;
      };
#pragma unroll
      for (int u = 0; u <= LG; ++u) issue();
      WAIT_V(N * LG); BARRIER();
#pragma nounroll
      for (int i = 0; i < (30 - LG) / 2; ++i) { issue(); issue(); WAIT_V(N * LG); BARRIER(); }
      issue();
      WAIT_V(N * (LG - 1)); BARRIER();
      if (LG == 4) { WAIT_V(N); BARRIER(); }
      WAIT_V(0); BARRIER();
    };
    if (j == 4) run(IC<1>{}, IC<1>{}, IC<4>{}, ca, cb);
#define K2_LA 2
#define K2_LB 2
    else if (j == 0) run(IC<4>{}, IC<0>{}, IC<K2_LA>{}, ca + 1, 0);
    else if (j == 1) run(IC<3>{}, IC<0>{}, IC<K2_LA>{}, ca + 5, 0);
    else if (j == 2) run(IC<0>{}, IC<3>{}, IC<K2_LB>{}, 0, cb + 1);
    else {
      const u32x4 srdT = make_srd(ws + WS_WTAB, 6144), srdX = make_srd(x, S_ * D_ * 2);
#pragma unroll
      for (int q = 0; q < 6; ++q) dma(L0 + K2_TAB + q * 1024, lane * 16, srdT, q * 1024);
      WAIT_V(0); BARRIER();
      touch_b(L0 + K4_SCR, (unsigned)(32 * idx + (lane & 31)) * 2048 + ((xcd & 1) * 8 + ((3 - (int)((uint64_t)wo >> 7)) & 7)) * 128, srdB, 0);
      unsigned vx[2];
#pragma unroll
      for (int i = 0; i < 2; ++i) vx[i] = (unsigned)(pm * 128 + 8 * i + (lane >> 3)) * 2048 + pn * 128 + (unsigned)((lane & 7) ^ (4 * i + (lane >> 4))) * 16;
      BARRIER();
#pragma nounroll
      for (int i = 0; i < 8; ++i) { dma(L0 + K2_X + i * 2048, vx[0], srdX, i * 32768); dma(L0 + K2_X + i * 2048 + 1024, vx[1], srdX, i * 32768); BARRIER(); }
      CODE_ADDR(nextk, k3_gateup);
      touch_g(L0 + K4_SCR, lane * 128, nextk); touch_g(L0 + K4_SCR, lane * 128, nextk + 8192);
      BARRIER(); BARRIER();
      WAIT_V(2);
#pragma nounroll
      for (int i = 0; i < 5; ++i) BARRIER();
      touch_g(L0 + K4_SCR, (lane & 31) * 128, KARG_PAGE());
      {
        const int l = lane < 14 * W0R ? lane : 14 * W0R - 1, r = (l * 4682) >> 16;
        const unsigned v = (unsigned)(W0R * idx + r) * 7168 + (xcd & 3) * 1792 + (l - 14 * r) * 128;
        touch_b(L0 + K4_SCR, v, make_srd(wg, D_ * F_ * 2), 0); touch_b(L0 + K4_SCR, v, make_srd(wu, D_ * F_ * 2), 0);
      }
    }
    BW_OUT(1);
    return;
  }
  const int g = wid, fr = lane & 15, fq = lane >> 4;
  STAMP_BEG(1);
  const int tok = pm * 128 + 32 * g + fr;
  float rmu[2];
  {
    const float* q = (const float*)(ws + WS_MUX) + tok;
    asm volatile("global_load_dword %0, %1, off" : "=v"(rmu[0]) : "v"(q));
    asm volatile("global_load_dword %0, %1, off offset:64" : "=v"(rmu[1]) : "v"(q));
  }
  f32x4 acc[2][4], accW[2];
#pragma unroll
  for (int m = 0; m < 2; ++m) {
    accW[m] = (f32x4){0, 0, 0, 0};
    asm volatile("" : "+v"(accW[m]));
#pragma unroll
    for (int n = 0; n < 4; ++n) { acc[m][n] = (f32x4){0, 0, 0, 0}; asm volatile("" : "+v"(acc[m][n])); }
  }
  unsigned ra = L0 + g * 2048 + lane * 16, rb[4];
  {
    const int q = fr >> 2, p = lane & 3, sw = (2 * fq + (q >> 1)) & 3;
#pragma unroll
    for (int n = 0; n < 4; ++n) rb[n] = L0 + 8192 + (unsigned)(4 * fq + q) * 128 + (unsigned)(n ^ sw) * 32 + 8 * p;
  }
  unsigned rw = L0 + K2_TAB + fq * 48 + (fr < 2 ? fr : 2) * 16;
  K2Frag F0, F1;
  auto rd = [&](K2Frag& F, int i, int sl) {
    if (i < 2) F.a[i] = lds128(ra + sl * K4_SLOT + i * 1024);
    else if (i == 10) F.w = lds128(rw + sl * 192);
    else { const int e = i - 2; F.b[e >> 1][e & 1] = ldstr(rb[e >> 1] + sl * K4_SLOT + (e & 1) * 2048); }
  };
  auto step = [&](K2Frag& F, K2Frag& N, int sl, unsigned dl) {
#pragma unroll
    for (int i = 0; i < 10; ++i) {
      const int n = i >> 1, m = i & 1;
      if (n < 4) acc[m][n] = mfma(cat(F.b[n][0], F.b[n][1]), F.a[m], acc[m][n]);
      else accW[m] = mfma(F.w, F.a[m], accW[m]);
      if (sl >= 0 && i < 6) { rd(N, 2 * i, sl); if (i < 5) rd(N, 2 * i + 1, sl); }
      if (sl == 3 && i == 6) { VADD(ra, dl); VADD(rb[0], dl); }
      if (sl == 3 && i == 7) { VADD(rb[1], dl); VADD(rb[2], dl); }
      if (sl == 3 && i == 8) { const unsigned c = 768; VADD(rb[3], dl); VADD(rw, c); }
      SB();
    }
  };
  BARRIER();
#pragma unroll
  for (int i = 0; i < 11; ++i) rd(F0, i, 0);
  WAIT_L0(); BARRIER();
  STAMP_L0();
  unsigned dl = 4 * K4_SLOT;
#pragma nounroll
  for (int it = 0; it < 28; it += 4) {
    step(F0, F1, 1, 0);
    step(F1, F0, 2, 0); WAIT_L0(); BARRIER();
    step(F0, F1, 3, dl);
    step(F1, F0, 0, 0); WAIT_L0(); BARRIER();
    dl = 0u - dl;
  }
  step(F0, F1, 1, 0);
  {
    const unsigned rx = L0 + K2_X + g * 4096 + fr * 128 + (fq & 1) * 8;
#pragma unroll
    for (int m = 0; m < 2; ++m)
#pragma unroll
      for (int n = 0; n < 4; ++n) {
        const u32x2 v = *(LAS u32x2*)(rx + m * 2048 + (unsigned)((2 * n + (fq >> 1)) ^ (fr >> 1)) * 16);
        acc[m][n] += (f32x4){bflo(v.x), bfhi(v.x), bflo(v.y), bfhi(v.y)};
      }
    SB();
  }
  step(F1, F0, 2, 0); WAIT_L0(); BARRIER();
  step(F0, F1, 3, 0);
  step(F1, F0, -1, 0);
  WAIT_V(0);
  STAMP_L1(1);
  asm volatile("" : "+v"(rmu[0]), "+v"(rmu[1]));
#pragma unroll
  for (int m = 0; m < 2; ++m) {
    const float mu = rmu[m] + red_add(accW[m][0] + accW[m][1]);
    char* hb = ws + WS_HC + (size_t)(2 * pn) * 131072 + (size_t)(pm * 8 + 2 * g + m) * 1024 + lane * 16;
    float ss = 0.f;
#pragma unroll
    for (int c = 0; c < 2; ++c) {
      const f32x4 y0 = acc[m][2 * c] - mu, y1 = acc[m][2 * c + 1] - mu;
      const u32x4 hp = {pk(y0[0], y0[1]), pk(y0[2], y0[3]), pk(y1[0], y1[1]), pk(y1[2], y1[3])};
      st16(hb + c * 131072, hp);
#pragma unroll
      for (int e = 0; e < 4; ++e) ss = dot2(hp[e], hp[e], ss);
    }
    ss = red_add(ss);
    if (fq == 0) {
      ((float*)(ws + WS_HC + (size_t)32 * 131072))[(pm * 8 + 2 * g + m) * 256 + (pn >> 2) * 64 + fr * 4 + (pn & 3)] = ss;
      if (pn == 0) ((float*)(ws + WS_MU))[tok + 16 * m] = mu;
    }
  }
  BW_OUT(1);
  STAMP_END(1);
}

struct K4Frag { u32x4 a[4]; u32x2 b[2][2]; };
extern "C" __global__ __launch_bounds__(832, 4) void k4_down(const u16* __restrict__ wd, char* __restrict__ ws, u16* __restrict__ out) {
  __shared__ __attribute__((aligned(1024))) char smem[K4_LDS];
  const unsigned L0 = (unsigned)(size_t)(LAS char*)smem;
  const int tid = threadIdx.x, lane = tid & 63;
  const int wid = __builtin_amdgcn_readfirstlane(tid >> 6);
  if (__builtin_expect(wid >= 8, 1)) { k4_loaders(wd, ws, L0, wid); return; }
  BW_DECL();
  const int xcd = blockIdx.x & 7, idx = blockIdx.x >> 3;
  const int kh = xcd & 1, pm = (xcd >> 2) * 8 + (idx >> 2), pq = ((xcd >> 1) & 1) * 4 + (idx & 3);
  const int gm = wid & 1, gn = wid >> 1, fr = lane & 15, fq = lane >> 4;
  STAMP_BEG(3);
  const int mt = pm * 8 + 4 * gm + kh, tok = mt * 16 + fr;
  u32x4 rh[2]; float rmu[2];
  const char* rp = ws + WS_HC + (size_t)(4 * pq + gn) * 131072 + (size_t)mt * 1024;
  const char* rq = ws + WS_MU + mt * 64;
  unsigned rv = lane * 16;
  asm volatile("" : "=v"(rh[0]), "=v"(rh[1]), "=v"(rmu[0]), "=v"(rmu[1]));
  f32x4 acc[4][2];
#pragma unroll
  for (int m = 0; m < 4; ++m) { acc[m][0] = acc[m][1] = (f32x4){0, 0, 0, 0}; asm volatile("" : "+v"(acc[m][0]), "+v"(acc[m][1])); }
  unsigned ra = L0 + gm * 4096 + lane * 16, rb[2];
  {
    const int q = fr >> 2, p = lane & 3, sw = (2 * fq + (q >> 1)) & 3;
#pragma unroll
    for (int n = 0; n < 2; ++n) rb[n] = L0 + 8192 + (gn >> 1) * 4096 + (unsigned)(4 * fq + q) * 128 + (unsigned)((2 * (gn & 1) + n) ^ sw) * 32 + 8 * p;
  }
  K4Frag F[3];
  auto rd = [&](K4Frag& N, int i, int sl) {
    if (i < 4) N.b[i >> 1][i & 1] = ldstr(rb[i >> 1] + sl * K4_SLOT + (i & 1) * 2048);
    else { const int m = 2 * (i & 1) + ((i - 4) >> 1); N.a[m] = lds128(ra + sl * K4_SLOT + m * 1024); }
  };
  auto mm = [&](K4Frag& C, int i) {
    const int m = 2 * (i & 1) + (i >> 2), n = (i >> 1) & 1;
    acc[m][n] = mfma(cat(C.b[n][0], C.b[n][1]), C.a[m], acc[m][n]);
  };
  auto step = [&](int t, int NM, int ROT, int NR, unsigned dl) {
    const int sl = (t + 2) & 3;
#pragma unroll
    for (int i = 0; i < NM; ++i) {
      mm(F[t % 3], (i + ROT) & 7);
      const int r0 = NM == 8 ? i : i < 2 ? 2 * i : i + 2, r1 = NM == 8 || i >= 2 ? r0 : r0 + 1;
#pragma unroll
      for (int r = r0; r <= r1; ++r)
        if (r < NR) {
          rd(F[(t + 2) % 3], r, sl);
          if (sl == 3 && r == 1) VADD(rb[0], dl);
          if (sl == 3 && r == 3) VADD(rb[1], dl);
          if (sl == 3 && r == NR - 1) VADD(ra, dl);
        }
      SB();
    }
  };
#define WAIT_L(n) do { SB(); MEMFENCE(); __builtin_amdgcn_s_waitcnt(0xC07F | ((n) << 8)); MEMFENCE(); SB(); } while (0)
  BARRIER();
#pragma unroll
  for (int i = 0; i < 16; ++i) rd(F[i >> 3], i & 7, i >> 3);
  WAIT_L0(); BARRIER();
  STAMP_L0();
  unsigned dl = 4 * K4_SLOT;
#pragma nounroll
  for (int it = 0; it < 4; ++it) {
#pragma unroll
    for (int u = 0; u < 12; ++u) {
      step(u, 8, 0, 8, u == 5 ? 0u - dl : dl);
      if (u & 1) { WAIT_L(8); BARRIER(); }
    }
    dl = 0u - dl;
    asm volatile("s_cmp_lg_u32 %6, 0\n\ts_cbranch_scc1 1f\n\tglobal_load_dwordx4 %0, %4, %7\n\tglobal_load_dwordx4 %1, %4, %7 offset:2048\n\t"
                 "global_load_dword %2, %5, %8\n\tglobal_load_dword %3, %5, %8 offset:128\n1:"
                 : "+v"(rh[0]), "+v"(rh[1]), "+v"(rmu[0]), "+v"(rmu[1]) : "v"(rv), "v"((rv >> 2) & 60), "s"(it), "s"(rp), "s"(rq) : "scc");
  }
  char* xo = ws + WS_XCH + (size_t)(blockIdx.x * 8 + wid) * 2048 + lane * 16;
  u32x4 y[2];
  sfor<48, K4_R>([&](auto tt) {
    constexpr int t = decltype(tt)::value, NR = t + 2 < K4_S ? 8 : t + 2 < K4_R ? 6 : 0;
    step(t, t < K4_S ? 8 : 4, t + 1 == K4_S ? 4 : 0, NR, (t + 2) & 4 ? 0u - 4 * K4_SLOT : 4 * K4_SLOT);
    if (t + 1 == K4_S) {
#pragma unroll
      for (int c = 0; c < 2; ++c) {
        const f32x4 p0 = acc[2 * c + 1][0], p1 = acc[2 * c + 1][1];
        const u32x4 v = {xword(pk(p0[0], p0[1])), xword(pk(p0[2], p0[3])), xword(pk(p1[0], p1[1])), xword(pk(p1[2], p1[3]))};
        asm volatile("global_store_dwordx4 %0, %1, off offset:%2 sc1\n\ts_nop 1" ::"v"(xo), "v"(v), "n"(c * 1024) : "memory");
      }
      xo += (1 - 2 * kh) * 16384;
    }
    if (t == K4_S + 1) {
      WAIT_V(2);
      asm volatile("" : "+v"(rh[0]), "+v"(rh[1]), "+v"(rmu[0]), "+v"(rmu[1]));
#pragma unroll
      for (int c = 0; c < 2; ++c) {
        const u32x4 h = rh[c];
        acc[2 * c][0] += (f32x4){bflo(h.x), bfhi(h.x), bflo(h.y), bfhi(h.y)} + rmu[c];
        acc[2 * c][1] += (f32x4){bflo(h.z), bfhi(h.z), bflo(h.w), bfhi(h.w)} + rmu[c];
      }
      SB();
    }
    if (t == K4_R - 1 - K4_POLL)
      asm volatile("global_load_dwordx4 %0, %2, off sc1\n\tglobal_load_dwordx4 %1, %2, off offset:1024 sc1" : "=&v"(y[0]), "=&v"(y[1]) : "v"(xo) : "memory");
    if ((t & 1) && t + 3 < K4_R) { WAIT_L(NR); BARRIER(); }
  });
  static_assert(((K4_R - 1) & 7) < 4, "");
  unsigned tw = L0 + 4 * K4_SLOT + wid * 2048 + fr * 64 + (unsigned)((fq >> 1) ^ (fr >> 2)) * 16 + (fq & 1) * 8;
  unsigned tr = L0 + 4 * K4_SLOT + wid * 2048 + (lane >> 2) * 64 + (unsigned)((lane & 3) ^ (lane >> 4)) * 16;
  u16* op = out + (size_t)(mt * 16 + (lane >> 2)) * D_ + pq * 128 + gn * 32 + (lane & 3) * 8;
  asm volatile("" : "+v"(op), "+v"(tw), "+v"(tr));
  STAMP_L1(3);
  asm volatile("s_waitcnt vmcnt(0)" : "+v"(y[0]), "+v"(y[1])::"memory");
#pragma nounroll
  for (int spin = 0; spin < (1 << 18); ++spin) {
    if (__builtin_amdgcn_ballot_w64(max(max4(y[0]), max4(y[1])) == 0xFFFFFFFFu) == 0) break;
    __builtin_amdgcn_s_sleep(K4_SLEEP);
    asm volatile("global_load_dwordx4 %0, %2, off sc1\n\tglobal_load_dwordx4 %1, %2, off offset:1024 sc1\n\ts_waitcnt vmcnt(0)"
                 : "=&v"(y[0]), "=&v"(y[1]) : "v"(xo) : "memory");
  }
#pragma unroll
  for (int c = 0; c < 2; ++c) {
    const f32x4 y0 = (f32x4){bflo(y[c].x), bfhi(y[c].x), bflo(y[c].y), bfhi(y[c].y)} + acc[2 * c][0];
    const f32x4 y1 = (f32x4){bflo(y[c].z), bfhi(y[c].z), bflo(y[c].w), bfhi(y[c].w)} + acc[2 * c][1];
    *(LAS u32x2*)(tw + c * 1024) = (u32x2){pk(y0[0], y0[1]), pk(y0[2], y0[3])};
    *(LAS u32x2*)((tw ^ 32) + c * 1024) = (u32x2){pk(y1[0], y1[1]), pk(y1[2], y1[3])};
  }
  MEMFENCE();
#pragma unroll
  for (int c = 0; c < 2; ++c) *(u32x4*)(op + c * 32 * D_) = lds128(tr + c * 1024);
  BW_OUT(3);
  STAMP_END(3);
}

constexpr int K3_G = 16384, K3_U = K3_G + 7168 + 16, K3_SLOT = 30752, K3_NS = 5, K3_SCR = K3_SLOT * K3_NS, K3_LDS = K3_SCR + 256;
struct K3Frag { u32x4 a[4]; u32x2 b[7][2]; };

DI unsigned geglu2(f32x2 g, f32x2 u, float k1, float k2, float ir2) {
  const f32x2 z = g * (g * g * k2 + k1);
  f32x2 e; e.x = __builtin_amdgcn_exp2f(z.x); e.y = __builtin_amdgcn_exp2f(z.y);
  const f32x2 d = e * ir2 + ir2;
  f32x2 r; r.x = __builtin_amdgcn_rcpf(d.x); r.y = __builtin_amdgcn_rcpf(d.y);
  const f32x2 o = (g * u) * r;
  return pk(o.x, o.y);
}

extern "C" __global__ __launch_bounds__(512, 2) void k3_gateup(const u16* __restrict__ wg, const u16* __restrict__ wu, const u16* __restrict__ wd,
                                                               char* __restrict__ ws) {
  __shared__ __attribute__((aligned(1024))) char smem[K3_LDS];
  const unsigned L0 = (unsigned)(size_t)(LAS char*)smem;
  const int tid = threadIdx.x, lane = tid & 63;
  const int wid = __builtin_amdgcn_readfirstlane(tid >> 6);
  BW_DECL();
  const int wm = wid & 3, wn = wid >> 2, fr = lane & 15, fq = lane >> 4;
  const int xcd = blockIdx.x & 7, idx = blockIdx.x >> 3;
  const int pm = (xcd >> 2) * 4 + (idx >> 3), pn = (xcd & 3) * 8 + (idx & 7);
  STAMP_BEG(2);

  const u32x4 srdA = make_srd(ws + WS_HC, 32 * 131072);
  const u32x4 srdG = make_srd(wg, D_ * F_ * 2), srdU = make_srd(wu, D_ * F_ * 2);
  const unsigned voffA = lane * 16 + wid * 2048 + pm * 16384;
  unsigned voffB;
  const int wb = wid < 7 ? wid : 6;
  { const unsigned L = wb * 64 + lane, r = (L * 4682u) >> 16; voffB = r * 7168 + pn * 224 + (L - r * 14) * 16; }
  const unsigned ldA = L0 + wid * 2048, ldG = L0 + K3_G + wb * 1024, ldU = L0 + K3_U + wb * 1024;
  auto dA = [&](int j, unsigned so, int i) { dma(ldA + so + i * 1024, voffA, srdA, j * 131072 + i * 1024); };
  auto dG = [&](int j, unsigned so) { dma(ldG + so, voffB, srdG, j * (32 * 7168)); };
  auto dU = [&](int j, unsigned so) { dma(ldU + so, voffB, srdU, j * (32 * 7168)); };
#define K3_WAIT(slots, extra) do { WAIT_VL(4 * (slots) + (extra)); BARRIER(); } while (0)
#pragma unroll
  for (int j = 0; j < 3; ++j) {
    dA(j, j * K3_SLOT, 0); dA(j, j * K3_SLOT, 1); dG(j, j * K3_SLOT); dU(j, j * K3_SLOT);
    if (j < K3_ORD) BARRIER();
  }
  unsigned ra = L0 + wm * 4096 + lane * 16;
  unsigned rb = L0 + K3_G + (unsigned)(4 * fq + (fr >> 2)) * 224 + 112 * wn + (lane & 3) * 8;
  unsigned rm = L0 + K3_G + (unsigned)(4 * fq + (fr >> 2)) * 224 + 112 * wn + 96 + (lane & 1) * 8 + ((lane >> 1) & 1) * (K3_U - K3_G);

  f32x4 acc[4][7];
#pragma unroll
  for (int m = 0; m < 4; ++m)
#pragma unroll
    for (int n = 0; n < 7; ++n) acc[m][n] = (f32x4){0, 0, 0, 0};
  K3Frag F0, F1;
  auto rd = [&](K3Frag& F, int i) {
    if (i < 4) F.a[i] = lds128(ra + i * 1024);
    else {
      const int e = i - 4, n = e >> 1, t = e & 1;
      F.b[n][t] = n < 6 ? ldstr(rb + (n >> 1) * 32 + (n & 1) * (K3_U - K3_G) + t * 3584) : ldstr(rm + t * 3584);
    }
  };
  f32x4 ssq[4];
  float cn[3][6], pv[3];
  auto fill = [&](int k) {
#define PIN(d, e) do { float r_ = (e); asm volatile("" : "+v"(r_)); d = r_; } while (0)
    if (k < 12) {
      const int m = k & 3, t = k >> 2;
      PIN(ssq[m][t == 1 ? 2 : 0], ssq[m][t == 1 ? 2 : 0] + ssq[m][t == 0 ? 1 : t == 1 ? 3 : 2]);
    } else if (k < 14) { float a = ssq[2 * k - 24][0], b = ssq[2 * k - 23][0]; swap32f(a, b); ssq[2 * k - 24][0] = a; ssq[2 * k - 23][0] = b; }
    else if (k < 16) PIN(pv[k - 14], ssq[2 * k - 28][0] + ssq[2 * k - 27][0]);
    else if (k == 16) { unsigned a = __float_as_uint(pv[0]), b = __float_as_uint(pv[1]); swap16u(a, b); pv[0] = __uint_as_float(a); pv[1] = __uint_as_float(b); }
    else if (k == 17) PIN(pv[2], pv[0] + pv[1]);
    else if (k == 18) PIN(pv[2], pv[2] * (1.0f / D_) + LN_EPS);
    else if (k == 19) PIN(pv[1], __builtin_amdgcn_rsqf(pv[2]));
    else if (k == 20) PIN(cn[2][4], pv[2]);
    else if (k == 21) PIN(pv[0], pv[1] * (-2.0f * 0.7978845608028654f * 1.4426950408889634f));
    else if (k == 22) PIN(pv[1], pv[1] * pv[1]);
    else if (k == 23) PIN(pv[1], pv[1] * 0.044715f);
    else if (k == 24) PIN(pv[1], pv[1] * pv[0]);
    else if (k < 27) PIN(cn[k - 25][4], pv[k - 25]);
    else if (k < 30) { const int q = k - 27; unsigned a = __float_as_uint(cn[q][4]), b = __float_as_uint(pv[q]); swap16u(a, b); cn[q][4] = __uint_as_float(a); cn[q][5] = __uint_as_float(b); }
    else if (k < 42) { const int q = (k - 30) >> 2, m = (k - 30) & 3; PIN(cn[q][m], cn[q][4 + (m >> 1)]); }
    else if (k < 48) { const int q = (k - 42) >> 1, h = (k - 42) & 1; swap32f(cn[q][2 * h], cn[q][2 * h + 1]); }
  };
  auto step = [&](K3Frag& F, K3Frag& N, unsigned so1, int ND, int jd, unsigned sd, unsigned sd2, bool AONLY, bool RD, int FB = -1) {
    const unsigned dl = so1 == (K3_NS - 1) * K3_SLOT ? (unsigned)(-(K3_NS - 1) * K3_SLOT) : (unsigned)K3_SLOT;
#pragma unroll
    for (int i = 0; i < 28; ++i) {
      const int n = i >> 2, m = i & 3;
      acc[m][n] = mfma(cat(F.b[n][0], F.b[n][1]), F.a[m], acc[m][n]);
      if (RD && i < 18) rd(N, i);
      if (RD && i == 19) VADD(ra, dl);
      if (RD && i == 21) VADD(rb, dl);
      if (RD && i == 23) VADD(rm, dl);
      if (FB >= 0 && (!RD || i >= 18)) fill(FB + (RD ? i - 18 : i));
#define K3_DS 18
#define K3_DD 2
      const int s = ND == 2 ? 10 : K3_DS, dd = ND == 2 ? 2 : K3_DD;
      if (ND > 0) {
        if (i == s) dA(jd, sd, 0);
        if (i == s + dd) dG(jd, sd);
        if (i == s + 2 * dd) dA(jd, sd, 1);
        if (i == s + 3 * dd) dU(jd, sd);
      }
      if (ND > 1) {
        if (i == 18) dA(jd + 1, sd2, 0);
        if (i == 20) dA(jd + 1, sd2, 1);
        if (i == 22) dG(jd + 1, sd2);
        if (i == 24) dU(jd + 1, sd2);
      }
      SB();
    }
  };

  constexpr int NL = 56 - GUL / 2;
  auto rest = [&]() { touch_b(L0 + K3_SCR, (lane < NL ? lane : NL - 1) * 128, wn ? srdU : srdG, GUL * 65536 + (blockIdx.x * 4 + wm) * (NL * 128)); };
  K3_WAIT(2, 0);
#pragma unroll
  for (int i = 0; i < 18; ++i) rd(F0, i);
  { const unsigned dl = K3_SLOT; VADD(ra, dl); VADD(rb, dl); VADD(rm, dl); }
  K3_WAIT(1, 0);
  STAMP_L0();
  step(F0, F1, K3_SLOT, 2, 3, 3 * K3_SLOT, 4 * K3_SLOT, false, true);
  K3_WAIT(2, 0);
  step(F1, F0, 2 * K3_SLOT, 2, 5, 0, K3_SLOT, false, true);
  rest();
  K3_WAIT(3, 0);
  CODE_ADDR(nextk, k4_down);
  uint64_t here; asm volatile("s_getpc_b64 %0" : "=s"(here));
  unsigned so = 2 * K3_SLOT;
  for (int j = 2; j < 26; j += 2) {
    const unsigned so1 = so == (K3_NS - 1) * K3_SLOT ? 0 : so + K3_SLOT;
    const unsigned so2 = so1 == (K3_NS - 1) * K3_SLOT ? 0 : so1 + K3_SLOT;
    step(F0, F1, so1, 1, j + 5, so, 0, false, true);
    K3_WAIT(3, 0);
    step(F1, F0, so2, 1, j + 6, so1, 0, false, true);
    if (j == 24 - 2 * wid) {
      touch_g(L0 + K3_SCR, lane * 128, (wid & 2) ? here + (wid & 1) * 8192 : nextk + (wid == 4 ? 16384 : (wid & 1) * 8192));
      char* xc = ws + WS_XCH + (size_t)(blockIdx.x * 8 + wid) * 2048 + lane * 16;
      st16(xc, (u32x4){~0u, ~0u, ~0u, ~0u}); st16(xc + 1024, (u32x4){~0u, ~0u, ~0u, ~0u});
    }
    K3_WAIT(3, 0);
    so = so2;
  }
  {
    const char* q = ws + WS_HC + (size_t)32 * 131072 + (size_t)(pm * 16 + 4 * wm) * 1024 + lane * 16;
#pragma unroll
    for (int m = 0; m < 4; ++m) asm volatile("global_load_dwordx4 %0, %1, off offset:%2" : "=v"(ssq[m]) : "v"(q), "n"(m * 1024));
  }
  step(F0, F1, 2 * K3_SLOT, 1, 31, 1 * K3_SLOT, 0, false, true);
  K3_WAIT(3, 4);
  {
    const u32x4 srdD = make_srd(wd, D_ * F_ * 2);
    const int l = lane < 56 ? lane : 55, P = (xcd & 1) ? (wid < 7 ? 9 + wid : 8) : wid;
    touch_b(L0 + K3_SCR, (l >> 3) * 2048 + (l & 7) * 128, srdD, (224 * P + 7 * idx) * 2048 + ((xcd >> 1) & 1) * 1024);
  }
  touch_g(L0 + K3_SCR, (lane & 31) * 128, KARG_PAGE());
  step(F1, F0, 3 * K3_SLOT, 0, 0, 0, 0, false, true);
  WAIT_VL(2); BARRIER();
  asm volatile("" : "+v"(ssq[0]), "+v"(ssq[1]), "+v"(ssq[2]), "+v"(ssq[3]));
  step(F0, F1, 4 * K3_SLOT, 0, 0, 0, 0, false, true, 0);
  step(F1, F0, 0, 0, 0, 0, 0, false, true, 10);
  step(F0, F1, 1 * K3_SLOT, 0, 0, 0, 0, false, true, 20);
  step(F1, F0, 0, 0, 0, 0, 0, false, false, 30);
  STAMP_L1(2);
  const int P = pn >> 1, w = 2 * (pn & 1) + wn, mt0 = pm * 16 + 4 * wm;
  const uint64_t ab = (uint64_t)(ws + WS_ACT + (size_t)(7 * P) * 131072 + (size_t)mt0 * 1024);
  unsigned xb[4][2];
  sfor<0, 4>([&](auto mm_) {
    constexpr int m = decltype(mm_)::value;
    unsigned o[3][2];
#pragma unroll
    for (int c = 0; c < 3; ++c) {
      const f32x4 a = acc[m][2 * c], b = acc[m][2 * c + 1];
      o[c][0] = geglu2(a.xy, b.xy, cn[0][m], cn[1][m], cn[2][m]);
      o[c][1] = geglu2(a.zw, b.zw, cn[0][m], cn[1][m], cn[2][m]);
    }
    st16s<m * 1024>(ab + (size_t)w * 131072, lane * 16, (u32x4){o[0][0], o[0][1], o[1][0], o[1][1]});
    xb[m][0] = o[2][0]; xb[m][1] = o[2][1];
  });
  const int hf = lane >> 5, fq1 = fq & 1;
  unsigned xm[2][2];
  sfor<0, 2>([&](auto mp_) {
    constexpr int mp = decltype(mp_)::value;
    swap32u(xb[2 * mp][0], xb[2 * mp + 1][0]); swap32u(xb[2 * mp][1], xb[2 * mp + 1][1]);
    st16s<mp * 2048>(ab + (size_t)(4 + (w >> 1)) * 131072 + (w & 1) * 512, (lane & 31) * 16 + hf * 1024,
                     (u32x4){xb[2 * mp][0], xb[2 * mp][1], xb[2 * mp + 1][0], xb[2 * mp + 1][1]});
    f32x4 a = acc[2 * mp][6], b = acc[2 * mp + 1][6];
#pragma unroll
    for (int r = 0; r < 4; ++r) { float u = a[r], v = b[r]; swap32f(u, v); a[r] = u; b[r] = v; }
    xm[mp][0] = geglu2(a.xy, b.xy, cn[0][4 + mp], cn[1][4 + mp], cn[2][4 + mp]); xm[mp][1] = geglu2(a.zw, b.zw, cn[0][4 + mp], cn[1][4 + mp], cn[2][4 + mp]);
  });
  swap16u(xm[0][0], xm[1][0]); swap16u(xm[0][1], xm[1][1]);
  st16s<0>(ab + (size_t)6 * 131072 + w * 256, (2 * fq1 + hf) * 1024 + fr * 16, (u32x4){xm[0][0], xm[0][1], xm[1][0], xm[1][1]});
  BW_OUT(2);
  STAMP_END(2);
}

extern "C" __global__ void kz_pad() { asm volatile(".fill 4096, 4, 0xBF800000"); }

extern "C" void kernel_launch(void* const* d_in, const int* in_sizes, int n_in, void* d_out, int out_size, void* d_ws, size_t ws_size,
                              hipStream_t stream) {
  const u16* x = (const u16*)d_in[0];
  const u16* wqkv = (const u16*)d_in[1];
  const u16* wo = (const u16*)d_in[2];
  const u16* wg = (const u16*)d_in[4];
  const u16* wu = (const u16*)d_in[5];
  const u16* wd = (const u16*)d_in[6];
  char* ws = (char*)d_ws;
  u16* out = (u16*)d_out;
  k1_qkv_attn<<<256, 768, 0, stream>>>(x, wqkv, wo, wg, wu, ws);
  k2_wo<<<256, 576, 0, stream>>>(x, wo, wg, wu, ws);
  k3_gateup<<<256, 512, 0, stream>>>(wg, wu, wd, ws);
  k4_down<<<256, 832, 0, stream>>>(wd, ws, out);
}
```

```cpp
#include <hip/hip_runtime.h>
#include <hip/hip_bf16.h>
#include <stdint.h>

#pragma clang diagnostic ignored "-Winline-asm"
#pragma clang diagnostic ignored "-Wint-to-pointer-cast"

typedef unsigned short u16;
typedef __bf16 bf16x8 __attribute__((ext_vector_type(8)));
typedef __bf16 bf16x2 __attribute__((ext_vector_type(2)));
typedef short s16x4 __attribute__((ext_vector_type(4)));
typedef float f32x4 __attribute__((ext_vector_type(4)));
typedef float f32x2 __attribute__((ext_vector_type(2)));
typedef unsigned u32x4 __attribute__((ext_vector_type(4)));
typedef unsigned u32x2 __attribute__((ext_vector_type(2)));
#define LAS __attribute__((address_space(3)))
#define DI __device__ __forceinline__

#define SN 15
constexpr int S_ = 2048, D_ = 1024, F_ = 3584;
constexpr float LN_EPS = 1e-5f;

constexpr size_t WS_O = 0;
constexpr size_t WS_ACT = 0;
constexpr size_t WS_HC = WS_ACT + (size_t)112 * 131072;
constexpr size_t WS_MUX = WS_HC + (size_t)33 * 131072;
constexpr size_t WS_MU = WS_MUX + 8192;
constexpr size_t WS_CNT = WS_MU + 8192;
constexpr size_t WS_F1 = WS_CNT + 2048;
constexpr size_t WS_WTAB = WS_F1 + 2048;
constexpr size_t WS_STAB = WS_WTAB + 12288;
constexpr size_t WS_XCH = WS_STAB + 32768;

DI u32x4 make_srd(const void* p, unsigned bytes) {
  uint64_t a = (uint64_t)p;
  u32x4 r; r.x = (unsigned)a; r.y = (unsigned)(a >> 32) & 0xffffu; r.z = bytes; r.w = 0x00020000u;
  return r;
}
DI void dma(unsigned lds, unsigned voff, u32x4 srd, unsigned soff) {
  asm volatile("s_mov_b32 m0, %0\n\ts_nop 0\n\tbuffer_load_dwordx4 %1, %2, %3 offen lds" ::"s"(lds), "v"(voff), "s"(srd), "s"(soff) : "m0");
}
DI void dma2s(unsigned lds, unsigned so, unsigned voff, u32x4 srd, unsigned soff) {
  asm volatile("s_add_u32 m0, %0, %1\n\ts_nop 0\n\tbuffer_load_dwordx4 %2, %3, %4 offen sc1 lds" ::"s"(lds), "s"(so), "v"(voff), "s"(srd), "s"(soff) : "m0", "scc");
}
DI void dma2(unsigned lds, unsigned so, unsigned voff, u32x4 srd, unsigned soff) {
  asm volatile("s_add_u32 m0, %0, %1\n\ts_nop 0\n\tbuffer_load_dwordx4 %2, %3, %4 offen lds" ::"s"(lds), "s"(so), "v"(voff), "s"(srd), "s"(soff) : "m0", "scc");
}
DI void dma_to(unsigned lds) { asm volatile("s_mov_b32 m0, %0" ::"s"(lds) : "m0"); }
DI void dma_go(unsigned voff, u32x4 srd, unsigned soff) { asm volatile("buffer_load_dwordx4 %0, %1, %2 offen lds" ::"v"(voff), "s"(srd), "s"(soff)); }
DI void dma_go1(unsigned voff, u32x4 srd, unsigned soff) { asm volatile("buffer_load_dwordx4 %0, %1, %2 offen sc1 lds" ::"v"(voff), "s"(srd), "s"(soff)); }
DI void dma_nt(unsigned lds, unsigned voff, u32x4 srd, unsigned soff) {
  asm volatile("s_mov_b32 m0, %0\n\ts_nop 0\n\tbuffer_load_dwordx4 %1, %2, %3 offen nt lds" ::"s"(lds), "v"(voff), "s"(srd), "s"(soff) : "m0");
}
#define dma1 dma
DI void touch_g(unsigned lds, unsigned voff, uint64_t base) {
  asm volatile("s_mov_b32 m0, %0\n\ts_nop 0\n\tglobal_load_lds_dword %1, %2" ::"s"(lds), "v"(voff), "s"(base) : "m0");
}
DI void touch_b(unsigned lds, unsigned voff, u32x4 srd, unsigned soff) {
  asm volatile("s_mov_b32 m0, %0\n\ts_nop 0\n\tbuffer_load_dword %1, %2, %3 offen lds" ::"s"(lds), "v"(voff), "s"(srd), "s"(soff) : "m0");
}
template <int B> DI void touch_b_if(int a, unsigned lds, unsigned voff, u32x4 srd, unsigned soff) {
  asm volatile("s_cmp_lg_u32 %4, %5\n\ts_cbranch_scc1 1f\n\ts_mov_b32 m0, %0\n\ts_nop 0\n\tbuffer_load_dword %1, %2, %3 offen lds\n1:"
               ::"s"(lds), "v"(voff), "s"(srd), "s"(soff), "s"(a), "n"(B) : "m0", "scc");
}
template <int B> DI void touch_g_if(int a, unsigned lds, unsigned voff, uint64_t base) {
  asm volatile("s_cmp_lg_u32 %3, %4\n\ts_cbranch_scc1 1f\n\ts_mov_b32 m0, %0\n\ts_nop 0\n\tglobal_load_lds_dword %1, %2\n1:"
               ::"s"(lds), "v"(voff), "s"(base), "s"(a), "n"(B) : "m0", "scc");
}
template <int OFF, int B> DI void st16s_if(int a, uint64_t base, unsigned voff, u32x4 v) {
  asm volatile("s_cmp_lg_u32 %4, %5\n\ts_cbranch_scc1 1f\n\tglobal_store_dwordx4 %0, %1, %2 offset:%3 sc1\n1:"
               ::"v"(voff), "v"(v), "s"(base), "n"(OFF), "s"(a), "n"(B) : "scc", "memory");
}
DI void touch_v(unsigned lds, uint64_t addr) {
  asm volatile("s_mov_b32 m0, %0\n\ts_nop 0\n\tglobal_load_lds_dword %1, off" ::"s"(lds), "v"(addr) : "m0");
}
#define CODE_ADDR(var, sym) uint64_t var; { int d_; asm volatile("s_getpc_b64 %0\n\ts_mov_b32 %1, " #sym "@rel32@lo+4" : "=s"(var), "=s"(d_)); var += (int64_t)d_; }
#define KARG_PAGE() ((uint64_t)__builtin_amdgcn_kernarg_segment_ptr() & ~(uint64_t)4095)
#define VADD(v, s) asm volatile("v_add_u32 %0, %1, %0" : "+v"(v) : "s"(s))
#define SB() __builtin_amdgcn_sched_barrier(0)
#define ALN(k) do { if ((k) >= 0) { SB(); asm volatile(".p2align 5\n\t.rept %0\n\ts_nop 0\n\t.endr" ::"n"((k) < 0 ? 0 : (k))); SB(); } } while (0)
#define AL2 6
#define AL3 0
#define MEMFENCE() asm volatile("" ::: "memory")
#define WAIT_VL(V) do { SB(); MEMFENCE(); __builtin_amdgcn_s_waitcnt(((V) & 15) | (((V) >> 4) << 14) | 0x70); MEMFENCE(); SB(); } while (0)
#define WAIT_V(V) do { SB(); MEMFENCE(); __builtin_amdgcn_s_waitcnt(((V) & 15) | (((V) >> 4) << 14) | 0xF70); MEMFENCE(); SB(); } while (0)
#define WAIT_L0() do { SB(); MEMFENCE(); __builtin_amdgcn_s_waitcnt(0xC07F); MEMFENCE(); SB(); } while (0)
#define BARRIER() do { SB(); MEMFENCE(); __builtin_amdgcn_s_barrier(); MEMFENCE(); SB(); } while (0)
#define BW_DECL()
#define BW_OUT(kern)

#define DLY(p)
DI int lane_now() { int l; asm volatile("v_mbcnt_lo_u32_b32 %0, -1, 0\n\tv_mbcnt_hi_u32_b32 %0, -1, %0" : "=v"(l)); return l; }
DI void vadd(unsigned& v, unsigned d) { VADD(v, d); }
template <int OFF> DI void ld4s(float& d, unsigned voff, const void* base) {
  asm volatile("global_load_dword %0, %1, %2 offset:%3 sc1" : "=v"(d) : "v"(voff), "s"(base), "n"(OFF));
}
template <int OFF, class T> DI void ld16s(T& d, unsigned voff, const void* base) {
  asm volatile("global_load_dwordx4 %0, %1, %2 offset:%3 sc1" : "=v"(d) : "v"(voff), "s"(base), "n"(OFF));
}
DI u32x4 lds128(unsigned a) { return *(LAS u32x4*)(a); }
DI f32x4 lds128f(unsigned a) { return *(LAS f32x4*)(a); }
DI u32x2 ldstr(unsigned a) { return __builtin_bit_cast(u32x2, __builtin_amdgcn_ds_read_tr16_b64_v4i16((LAS s16x4*)(a))); }
DI f32x4 mfma(u32x4 a, u32x4 b, f32x4 c) {
  return __builtin_amdgcn_mfma_f32_16x16x32_bf16(__builtin_bit_cast(bf16x8, a), __builtin_bit_cast(bf16x8, b), c, 0, 0, 0);
}
DI void mfma_acc(f32x4& c, u32x4 a, u32x4 b) { asm volatile("v_mfma_f32_16x16x32_bf16 %0, %1, %2, %0" : "+v"(c) : "v"(a), "v"(b)); }
DI void mfma_new(f32x4& c, u32x4 a, u32x4 b) { asm volatile("v_mfma_f32_16x16x32_bf16 %0, %1, %2, 0" : "=v"(c) : "v"(a), "v"(b)); }
#define MFMA_DONE() do { SB(); asm volatile("s_nop 7\n\ts_nop 1"); SB(); } while (0)
DI u32x4 cat(u32x2 lo, u32x2 hi) { u32x4 r; r.x = lo.x; r.y = lo.y; r.z = hi.x; r.w = hi.y; return r; }
DI unsigned pk(float a, float b) { f32x2 v = {a, b}; return __builtin_bit_cast(unsigned, __builtin_convertvector(v, bf16x2)); }
DI float dot2(unsigned a, unsigned b, float c) {
  return __builtin_amdgcn_fdot2_f32_bf16(__builtin_bit_cast(bf16x2, a), __builtin_bit_cast(bf16x2, b), c, false);
}
DI float bflo(unsigned u) { return __uint_as_float(u << 16); }
DI float bfhi(unsigned u) { return __uint_as_float(u & 0xffff0000u); }
DI float red_add(float v) {
  auto a = __builtin_amdgcn_permlane32_swap(__float_as_uint(v), __float_as_uint(v), false, false);
  v = __uint_as_float(a[0]) + __uint_as_float(a[1]);
  auto b = __builtin_amdgcn_permlane16_swap(__float_as_uint(v), __float_as_uint(v), false, false);
  return __uint_as_float(b[0]) + __uint_as_float(b[1]);
}
DI float red_max(float v) {
  auto a = __builtin_amdgcn_permlane32_swap(__float_as_uint(v), __float_as_uint(v), false, false);
  v = fmaxf(__uint_as_float(a[0]), __uint_as_float(a[1]));
  auto b = __builtin_amdgcn_permlane16_swap(__float_as_uint(v), __float_as_uint(v), false, false);
  return fmaxf(__uint_as_float(b[0]), __uint_as_float(b[1]));
}
DI float row_add(float v) {
  v += __uint_as_float(__builtin_amdgcn_update_dpp(0u, __float_as_uint(v), 0x128, 0xf, 0xf, false));
  v += __uint_as_float(__builtin_amdgcn_update_dpp(0u, __float_as_uint(v), 0x124, 0xf, 0xf, false));
  v += __uint_as_float(__builtin_amdgcn_update_dpp(0u, __float_as_uint(v), 0x122, 0xf, 0xf, false));
  v += __uint_as_float(__builtin_amdgcn_update_dpp(0u, __float_as_uint(v), 0x121, 0xf, 0xf, false));
  return v;
}
DI void swap32f(float& a, float& b) {
  auto r = __builtin_amdgcn_permlane32_swap(__float_as_uint(a), __float_as_uint(b), false, false);
  a = __uint_as_float(r[0]); b = __uint_as_float(r[1]);
}
DI void swap32u(unsigned& a, unsigned& b) { auto r = __builtin_amdgcn_permlane32_swap(a, b, false, false); a = r[0]; b = r[1]; }
DI void swap16u(unsigned& a, unsigned& b) { auto r = __builtin_amdgcn_permlane16_swap(a, b, false, false); a = r[0]; b = r[1]; }
constexpr unsigned ONES2 = 0x3F803F80u;
template <int V> struct IC { static constexpr int value = V; };
template <int I, int E, class Fn> DI void sfor(Fn&& f) { if constexpr (I < E) { f(IC<I>{}); sfor<I + 1, E>(f); } }
typedef unsigned u32x6 __attribute__((ext_vector_type(6)));
DI float selm(float a, float b, unsigned long long m) { return __builtin_amdgcn_inverse_ballot_w64(m) ? b : a; }
DI float max2(float a, float b) { return __builtin_elementwise_maximum(a, b); }
DI float max3(float a, float b, float c) { return max2(max2(a, b), c); }
template <class Fn> constexpr unsigned long long lanes(Fn f) { unsigned long long m = 0; for (int l = 0; l < 64; ++l) if (f(l & 15, l >> 4)) m |= 1ull << l; return m; }
#define STMODE 3
DI void st16(void* p, u32x4 v) {
  asm volatile("global_store_dwordx4 %0, %1, off sc1\n\ts_nop 1" :: "v"(p), "v"(v) : "memory");
}
template <int OFF> DI void st16s(uint64_t base, unsigned voff, u32x4 v) {
  asm volatile("global_store_dwordx4 %0, %1, %2 offset:%3 sc1\n\ts_nop 1" ::"v"(voff), "v"(v), "s"(base), "n"(OFF) : "memory");
}
DI void st8(void* p, u32x2 v) {
  asm volatile("global_store_dwordx2 %0, %1, off sc1" :: "v"(p), "v"(v) : "memory");
}
#define STAMP_E1()
#define STAMP_E2(kern)
#define STAMP_END_ND(kern)
#define STAMP_DECL()
#define STAMP_BEG2()
#define STAMP_AT(kern, i)
#define STAMP_RT(kern, i)
#define STAMP_L0()
#define STAMP_L1(kern)
#define STAMP_END(kern)
#define STAMP_BEG(kern)
#define EST_DECL()
#define EST(i)
#define EST_OUT()

#define TS_DECL(k, base)
#define TS()
#define TS_OUT()
#define TS_EXTRA 0
extern "C" __global__ void k_all(const u16*, const u16*, const u16*, const u16*, const u16*, const u16*, char*, u16*);
constexpr int K3_G = 65536, K3_UG = 7168 + 16, K3_BS = 14352, K3_BP = 2 * K3_BS, K3_SCR = K3_G + 3 * K3_BP, K3_CNT = K3_SCR + 1024, K3_LDS = K3_CNT + 32;

constexpr int K1_STAGE = 43008, K1_B = 18432, K1_ST = 3 * K1_STAGE;
constexpr int K1_SCR = K3_SCR, K1_WR = K1_ST + 2048, K2_TAB = K3_G + 2 * K3_BP;
#define K1_BI 36
#define C0N 2
#define C0X 16384
#define K1_JI 32
#define GUP 0
#define K1_WK 4
#define K1_RL 0
#define K1_T14 1
#define K1_GT 14
#define K1_CODE 12288
#define PFD 3
#define GUL 28
#define W0R 3
#define GU2 0
#define GU2B 3
#define K3_ORD 2
#define K1_SPREAD 1
#define K1_G1 8
#define NKS 8192
#define K2_GM 1
#define PF0 2
constexpr int k1_op(bool last, int c) {
  const int s[27] = {0, 1, 2, 3, 24, 7, 6, 5, 4, 8, 9, 10, 11, 25, 26, 15, 14, 12, 13, 16, 17, 18, 19, 23, 22, 21, 20};
  const int l[27] = {0, 1, 2, 3, 24, 7, 6, 5, 4, 8, 9, 10, 11, 25, 26, 15, 14, 18, 19, 23, 22, 21, 17, 13, 12, 16, 20};
  return c < 0 ? -1 : last ? l[c] : s[c];
}

DI unsigned mark_of(unsigned L0) { return *(volatile LAS unsigned*)(L0 + K3_CNT + 20) * 0x9E3779B1u + 0x7F4A7C15u; }
DI void k1_part(const u16* __restrict__ x, const u16* __restrict__ wqkv, const u16* __restrict__ wo, const u16* __restrict__ wg, const u16* __restrict__ wu,
                char* __restrict__ ws, const u16* out, const unsigned L0, const int wid, float (&rmu)[2], unsigned long long t0_) {
  const int lane = threadIdx.x & 63;
  BW_DECL();
  const int xcd = blockIdx.x & 7, idx = blockIdx.x >> 3;
  const int pm = ((xcd >> 2) * 4 + (idx >> 3)) * 2 + (xcd & 1), xh = (xcd >> 1) & 1, pn = xh * 8 + (idx & 7);
  const int i7 = idx & 7;
  int sl, rk; bool slow;
  auto ranks = [&](uint64_t w) { sl = (3 - (int)(w >> 7)) & 7; rk = 7 * (idx >> 3) + (i7 > sl ? i7 - 1 : i7); slow = i7 == sl; };
  const int s0 = pm * 128;
  const int l3 = lane >> 3, l7 = lane & 7;
  const int th = wid & 1, hh = (wid >> 1) & 1, fr = lane & 15, fq = lane >> 4;
  const int g = 2 * th + (hh ^ (wid >> 2));
  constexpr unsigned HO = K1_STAGE;
  constexpr unsigned CT = HO + 49152;
  constexpr unsigned CV = CT + 512;
  f32x4 acc[4][6], accHK, accHV;
  STAMP_DECL();
  TS_DECL(0, K3_LDS);
  EST_DECL();

  f32x4 bp, bc, bp0;
  unsigned ca, cb, sa, sb, xb;
  auto early = [&]() __attribute__((always_inline)) {
  sfor<0, 4>([&](auto rr) {
    constexpr int r = decltype(rr)::value;
    bp[r] = selm(-INFINITY, 0.f, lanes([](int f, int q) { const int d = f - 4 * q - r; return (d & 1) == 0 && d <= -2; }));
    bc[r] = selm(-INFINITY, 0.f, lanes([](int f, int q) { const int d = f - 4 * q - r; return (d & 1) == 0 && d >= 0 && d <= 14; }));
  });
  ca = L0 + CT + hh * 256 + th * 64 + fq * 16; cb = L0 + CV + hh * 128 + th * 64 + fr * 4;
  sa = L0 + K1_ST + g * 128 + fr * 4; sb = L0 + K1_ST + g * 128 + fq * 16;
  xb = L0 + hh * 10240 + g * 2048 + lane * 16;
  bp0 = bp + ((pm | g) != 0 ? 0.f : -INFINITY);
  asm volatile("" : "+v"(bp), "+v"(bc), "+v"(bp0), "+v"(ca), "+v"(cb), "+v"(sa), "+v"(sb), "+v"(xb));
  };

  auto loaders = [&]() __attribute__((always_inline)) {
    const int j = wid - 4;
    __builtin_amdgcn_s_setprio(3);
    u32x4 srdX = make_srd(nullptr, 0), srdW = srdX;
    unsigned voffA[2], voffB[2];
#pragma unroll
    for (int i = 0; i < 2; ++i) {
      voffA[i] = (unsigned)(s0 + 32 * j + 8 * i + l3) * 2048 + (unsigned)(l7 ^ (4 * i + (lane >> 4))) * 16;
      voffB[i] = (unsigned)(16 * j + 8 * i + l3) * 6144 + pn * 128 + (unsigned)(l7 ^ ((((lane >> 4) & 1) | (i << 1)) << 1)) * 16;
    }
    auto tile = [&](int kt, unsigned so) {
#pragma unroll
      for (int i = 0; i < 4; ++i) dma(L0 + so + j * 4096 + i * 1024, voffA[i & 1], srdX, kt * 128 + (i >> 1) * 32768);
#pragma unroll
      for (int i = 0; i < 6; ++i) dma(L0 + so + K1_B + (i >> 1) * 8192 + j * 2048 + (i & 1) * 1024, voffB[i & 1], srdW, kt * (64 * 6144) + (i >> 1) * 2048);
    };
    uint64_t wp = 0;
#pragma nounroll
    for (int ps = 0; ps < 2; ++ps) {
      tile(0, 0);
      SB();
      uint64_t xp = (uint64_t)x; wp = (uint64_t)wqkv;
      asm volatile("" : "+s"(xp), "+s"(wp));
      srdX = make_srd((const void*)xp, S_ * D_ * 2); srdW = make_srd((const void*)wp, D_ * 3 * D_ * 2);
    }
    ranks(wp);
    unsigned voffE;
    const unsigned long long emask = j == 0 ? ~0xFFull : ~0ull;
    auto halo = [&](int kt, unsigned so) {
      asm volatile("s_mov_b32 m0, %0\n\ts_mov_b64 exec, %4\n\tbuffer_load_dwordx4 %1, %2, %3 offen lds\n\ts_mov_b64 exec, -1"
                   ::"s"(L0 + so + 16384 + j * 1024), "v"(voffE), "s"(srdX), "s"(kt * 128), "s"(emask) : "m0");
    };
    const unsigned vline = lane * 128;
    CODE_ADDR(self, k_all);
    if (j < 2) {
      const int r = s0 - 16 + 8 * j + l3;
      voffE = (unsigned)(r < 0 ? 0 : r) * 2048 + (unsigned)(l7 ^ (4 * j + (lane >> 4))) * 16;
      halo(0, 0);
      tile(1, K1_STAGE);
      halo(1, K1_STAGE);
      if (j == 0 && lane < 24) *(LAS u32x4*)(L0 + l3 * K1_STAGE + 16384 + l7 * 16) = (u32x4){ONES2, ONES2, ONES2, ONES2};
    } else {
#define SLOWX 2
#define SLOW0 1
      const int q0 = rk * 7 + (l7 < 7 ? l7 : 6), q = q0 < 192 ? q0 : 191;
      const unsigned vs = slow ? 0x80000000u : (q / 3) * 6144 + (q % 3) * 2048 + (xh * 8 + sl) * 128;
      if (SLOW0 && j == 3) touch_b(L0 + K1_SCR, vs + (l3 < PF0 + SLOWX ? l3 : 0) * (64 * 6144), srdW, 0);
      tile(1, K1_STAGE);
      if (!C0N) touch_g(L0 + K1_SCR, vline, self + (j - 2) * 8192); else touch_g(L0 + K1_SCR, vline, self + C0X + (j - 2) * 8192);
      const int ex = (rk * 8 + xcd) * 10 + (lane < 10 ? lane : 9), ew = (rk * 8 + xcd) * 14 + (lane < 14 ? lane : 13);
      voffE = slow ? 0x80000000u : j == 2 ? (ex < 2048 ? ex : 2047) * 2048 : (ew < 3072 ? ew : 3071) * 128;
      if (j == 3 && lane >= 16 && lane < 23) voffE = vs + SLOWX * (64 * 6144);
#pragma unroll
      for (int t = PF0; t < PFD + 2; ++t) touch_b(L0 + K1_SCR, voffE, j == 2 ? srdX : srdW, j == 2 ? t * 128 : t * (64 * 6144));
#define NK0 0
      if (NK0 && j == 2) touch_g(L0 + K1_SCR, vline, self + 16384);
    }
    auto row = [&]() {
      const u32x4 srdO = make_srd(wo, D_ * D_ * 2);
      dma(L0 + K1_WR + j * 2048, lane * 16, srdO, (blockIdx.x * 4 + j) * 2048); dma(L0 + K1_WR + j * 2048 + 1024, lane * 16, srdO, (blockIdx.x * 4 + j) * 2048 + 1024);
      asm volatile("s_mov_b32 m0, %0\n\ts_mov_b64 exec, 1\n\tglobal_load_lds_dword %1, %2 sc1\n\ts_mov_b64 exec, -1" ::"s"(L0 + K3_CNT + 20), "v"(0), "s"(ws + WS_WTAB + 8192) : "m0");
    };
    constexpr int RW = 0;
    if (j < 2) WAIT_VL(11 + RW); else if (j == 3) WAIT_VL(11 + RW + SLOW0 + PFD + 2 - PF0); else WAIT_VL(11 + RW + NK0 + PFD + 2 - PF0);
    BARRIER();
    unsigned so = 2 * K1_STAGE;
    auto iter = [&](int kt, auto xx, auto&& job) {
#define K1_TAFT 1
      auto warm = [&]() {
        if (kt >= 16 - PFD) touch_g(L0 + K1_SCR, vline, self + K1_CODE + (2 * (kt - 13) + j - 2) * 8192);
        else touch_b(L0 + K1_SCR, voffE, j == 2 ? srdX : srdW, j == 2 ? (kt + PFD) * 128 : (kt + PFD) * (64 * 6144));
      };
      if (j < 2) halo(kt, so); else if (!K1_TAFT) warm();
      tile(kt, so);
      if (j >= 2 && K1_TAFT) warm();
      so = so == 2 * K1_STAGE ? 0 : so + K1_STAGE;
      job();
      constexpr int X = xx.value;
      if (X == 0 && kt == 2 && j == 2) WAIT_VL(12 + NK0 + PFD + 2 - PF0); else if (X == 0 && kt == 2 && j == 3) WAIT_VL(12 + PFD + 2 - PF0);
      else if (j >= 2) WAIT_VL(12 + X); else WAIT_VL(11 + X);
      BARRIER();
    };
    auto nojob = []() {};
#pragma nounroll
    for (int kt = 2; kt < K1_WK; ++kt) iter(kt, IC<0>{}, nojob);
#define JPRIO 0
    u32x4 hv[4];
    const int hr = s0 + 8 * pn + 2 * j + (lane >> 5);
    iter(K1_WK, IC<4>{}, [&]() {
      row();
      asm volatile("s_mov_b64 exec, 0xffff\n\tglobal_store_dword %0, %1, %2 sc1\n\ts_mov_b64 exec, -1"
                   ::"v"(lane * 4), "v"(0), "s"(blockIdx.x < 16 ? ws + WS_CNT + (blockIdx.x * 4 + j) * 64 : ws + WS_XCH + (blockIdx.x * 4 + j) * 4096) : "memory");
    });
    iter(K1_WK + 1, IC<8>{}, [&]() {
      const u16* p = x + (size_t)hr * D_ + (lane & 31) * 8;
#pragma unroll
      for (int i = 0; i < 4; ++i) asm volatile("global_load_dwordx4 %0, %1, off offset:%2" : "=v"(hv[i]) : "v"(p), "n"(i * 512));
    });
#define PGT 0
    iter(K1_WK + 2, IC<4 + PGT>{}, [&]() {
      if (PGT) {
        const int ks = 7 * ((xcd & 3) * 4 + (i7 >> 1)) + (l7 < 7 ? l7 : 6), g3 = (xcd >> 2) * 4 + (idx >> 3);
        const unsigned own = (2 * pn + (lane & 1)) * 131072 + pm * 8192;
        const unsigned v = j == 0 ? (unsigned)WS_O + own : j == 1 ? (unsigned)WS_HC + own : j == 2 ? (ks < 32 ? (unsigned)WS_O + own : (unsigned)WS_ACT + ks * 131072 + g3 * 16384)
                                                                                         : (((xcd >> 2) * 8 + (idx >> 2)) * 128 + ((lane & 3) * 2 + (xcd & 1)) * 16) * 2048 + (((xcd >> 1) & 1) * 4 + (idx & 3)) * 256;
        touch_g(L0 + K1_SCR, v, j == 3 ? (uint64_t)out : (uint64_t)ws);
      }
    });
    unsigned mark;
    iter(K1_WK + 3, IC<1 + PGT>{}, [&]() {
      __builtin_amdgcn_s_setprio(JPRIO);
      const u32x4 w0 = lds128(L0 + K1_WR + j * 2048 + lane * 16), w1 = lds128(L0 + K1_WR + j * 2048 + 1024 + lane * 16);
      float s = 0.f;
#pragma unroll
      for (int e = 0; e < 4; ++e) { s = dot2(w0[e], ONES2, s); s = dot2(w1[e], ONES2, s); }
      s = red_add(row_add(s)) * (1.0f / D_);
      const unsigned hi = pk(s, 0.f) & 0xffffu;
      mark = mark_of(L0);
      const u32x2 v = {hi | (pk(s - bflo(hi), 0.f) << 16), mark};
      asm volatile("s_mov_b64 exec, 1\n\tglobal_store_dwordx2 %0, %1, %2 sc1\n\ts_mov_b64 exec, -1" ::"v"(0), "v"(v), "s"(ws + WS_WTAB + (blockIdx.x * 4 + j) * 8) : "memory");
      __builtin_amdgcn_s_setprio(3);
    });
    float a[2] = {0.f, 0.f}, q[2] = {0.f, 0.f};
    auto sums = [&](int i0, int i1) {
      __builtin_amdgcn_s_setprio(JPRIO);
#pragma unroll
      for (int i = i0; i < i1; ++i) {
        asm volatile("" : "+v"(hv[i]));
#pragma unroll
        for (int e = 0; e < 4; ++e) { a[e & 1] = dot2(hv[i][e], ONES2, a[e & 1]); q[e & 1] = dot2(hv[i][e], hv[i][e], q[e & 1]); }
      }
      asm volatile("" : "+v"(a[0]), "+v"(a[1]), "+v"(q[0]), "+v"(q[1]));
      __builtin_amdgcn_s_setprio(3);
    };
    iter(K1_WK + 4, IC<1>{}, [&]() { sums(0, 2); });
    iter(K1_WK + 5, IC<0>{}, [&]() { sums(2, 4); });
    iter(K1_WK + 6, IC<1>{}, [&]() {
      __builtin_amdgcn_s_setprio(JPRIO);
      unsigned sa = __float_as_uint(row_add(a[0] + a[1])), sb = sa, sq = __float_as_uint(row_add(q[0] + q[1])), sr = sq;
      swap16u(sa, sb); swap16u(sq, sr);
      const float mean = (__uint_as_float(sa) + __uint_as_float(sb)) * (1.0f / D_);
      const float var = fmaxf((__uint_as_float(sq) + __uint_as_float(sr)) * (1.0f / D_) - mean * mean, 0.f);
      const u32x4 t = {__float_as_uint(-mean), mark, __float_as_uint(__builtin_amdgcn_rsqf(var + LN_EPS)), mark};
      asm volatile("s_mov_b64 exec, %3\n\tglobal_store_dwordx4 %0, %1, %2 sc1\n\ts_mov_b64 exec, -1" ::"v"(hr * 16), "v"(t), "s"(ws + WS_STAB), "s"(0x100000001ull) : "memory");
      __builtin_amdgcn_s_setprio(3);
    });
    iter(K1_WK + 7, IC<1>{}, nojob);
#pragma nounroll
    for (int kt = K1_WK + 8; kt < 16; ++kt) iter(kt, IC<0>{}, nojob);
    u32x4 st;
    const int sr = s0 - 16 + 36 * j + (lane < 36 ? lane : 35);
    auto lds_ = [&]() { asm volatile("global_load_dwordx4 %0, %1, %2 sc1" : "=v"(st) : "v"((sr < 0 ? 0 : sr) * 16), "s"(ws + WS_STAB) : "memory"); };
    lds_();
    if (j >= 2 && K1_TAFT) WAIT_V(2); else WAIT_V(1);
    BARRIER();
    early();
    BARRIER();
    asm volatile("s_waitcnt vmcnt(0)" : "+v"(st)::"memory");
    {
#pragma nounroll
      for (int spin = 0; spin < (1 << 18); ++spin) {
        if (__builtin_expect(__builtin_amdgcn_ballot_w64(st.y != mark || st.w != mark) == 0, 1)) break;
        __builtin_amdgcn_s_sleep(16);
        lds_();
        asm volatile("s_waitcnt vmcnt(0)" : "+v"(st)::"memory");
      }
      if (lane < 36) { *(LAS unsigned*)(L0 + K1_ST + (36 * j + lane) * 4) = st.x; *(LAS unsigned*)(L0 + K1_ST + 576 + (36 * j + lane) * 4) = st.z; }
    }
    __builtin_amdgcn_s_setprio(0);
    STAMP_BEG2(); STAMP_L0(); STAMP_L1(0);
    EST(0);
    WAIT_L0(); EST(1);
    BARRIER();
#pragma unroll
    for (int i = 0; i < 12; ++i) { const int jj = (i + 2) % 6, m = 1 - i / 6; acc[m][jj] = lds128f(L0 + HO + j * 12288 + (m * 6 + jj) * 1024 + lane * 16); }
    accHK = accHV = (f32x4){0, 0, 0, 0};
  };
  auto products = [&]() __attribute__((always_inline)) {
    STAMP_BEG2();
    TS();
    if (wid == 0 && lane < 5) *(LAS unsigned*)(L0 + K3_CNT + lane * 4) = 0;
    { uint64_t wp = (uint64_t)wqkv; asm volatile("" : "+s"(wp)); ranks(wp); }
    f32x4 accHQ;
    unsigned ra[2][2], rh[2], rb[2];
#pragma unroll
    for (int ks = 0; ks < 2; ++ks) {
      const unsigned c = (unsigned)((4 * ks + fq) ^ (fr >> 1)) * 16;
      ra[0][ks] = L0 + (unsigned)(32 * g + fr) * 128 + c; ra[1][ks] = L0 + (unsigned)(32 * (g ^ 1) + fr) * 128 + c;
      rh[ks] = L0 + 16384 + fr * 128 + c;
    }
    {
      const int q = fr >> 2, p = lane & 3, f = ((q >> 1) & 1) | ((fq & 1) << 1);
#pragma unroll
      for (int j = 0; j < 2; ++j) rb[j] = L0 + K1_B + (unsigned)(8 * fq + q) * 128 + (unsigned)((2 * hh + (j ^ th)) ^ f) * 32 + 8 * p;
    }
    const unsigned one = (fr & 3) == 0 ? ONES2 : 0u;
    u32x4 onesf = {one, one, one, one};
    asm volatile("" : "+v"(onesf));
    const f32x4 z4 = {0, 0, 0, 0};
#pragma unroll
    for (int i = 0; i < 24; ++i) { acc[i & 3][i >> 2] = z4; asm volatile("" : "+v"(acc[i & 3][i >> 2])); }
    accHQ = accHK = accHV = z4;
    asm volatile("" : "+v"(accHQ), "+v"(accHK), "+v"(accHV));
    u32x4 fa[2][2][4], fh[2]; u32x2 fb[4][2][2];
    auto rdA = [&](int o, int i) { fa[o][i & 1][i >> 1] = lds128(ra[i >> 2][i & 1] + ((i >> 1) & 1) * 2048); };
    auto rdB = [&](int o, int j, int i) { fb[(j + 2 * o) & 3][i >> 1][i & 1] = ldstr(rb[j & 1] + (j >> 1) * 8192 + (i >> 1) * 4096 + (i & 1) * 512); };
    early();
    BARRIER();
#pragma unroll
    for (int i = 0; i < 4; ++i) { rdB(0, 0, i); rdA(0, i); }
#pragma unroll
    for (int i = 0; i < 4; ++i) rdA(0, 4 + i);
#pragma unroll
    for (int i = 0; i < 4; ++i) rdB(0, 1, i);
    fh[0] = lds128(rh[0]); fh[1] = lds128(rh[1]);
    STAMP_L0(); TS();
    unsigned sidx = 0;
    unsigned ho = L0 + HO + wid * 12288 + lane * 16;
    auto ktile = [&](auto oo, auto la) {
      constexpr int o = oo.value;
      constexpr bool LA = la.value == 1, TC = K1_T14 ? la.value == 2 : LA;
      const unsigned dl = sidx == 2 ? (unsigned)(-2 * K1_STAGE) : (unsigned)K1_STAGE;
      sidx = sidx == 2 ? 0 : sidx + 1;
      sfor<0, 54>([&](auto ii) {
        constexpr int i = ii.value, c = i >> 1, op = k1_op(LA, c), ks = (i ^ c) & 1, j = op >> 2, m = op & 3;
        auto B = [&](int t) { return cat(fb[(t + 2 * o) & 3][ks][0], fb[(t + 2 * o) & 3][ks][1]); };
        if constexpr (i == K1_BI) { WAIT_L0(); BARRIER(); TS(); }
        if constexpr (op < 16) mfma_acc(acc[m][j], B(j), fa[o][ks][m]);
        else if constexpr (op < 24) mfma_acc(acc[m][j], fa[o][ks][m], B(j));
        else if constexpr (op == 24) mfma_acc(accHQ, B(0), onesf);
        else if constexpr (op == 25) mfma_acc(accHK, B(2), fh[ks]);
        else mfma_acc(accHV, fh[ks], B(4));
        if constexpr (i < 8) rdB(o, 2 + (i >> 2), i & 3);
        if constexpr (i >= 10 && i < 14) rdB(o, 4, i - 10);
        if constexpr (i >= 18 && i < 22) rdB(o, 5, i - 18);
        if constexpr (!LA) {
          if constexpr (i >= 22 && i < 26) vadd(ra[(i >> 1) & 1][i & 1], dl);
          if constexpr (i >= 26 && i < 28) vadd(rh[i & 1], dl);
          if constexpr (i >= 28 && i < 30) vadd(rb[i & 1], dl);
          if constexpr (i >= K1_BI && i < K1_BI + 8) rdA(o ^ 1, i - K1_BI);
          if constexpr (i >= K1_BI + 8 && i < K1_BI + 16) rdB(o ^ 1, (i - K1_BI - 8) >> 2, (i - K1_BI) & 3);
          if constexpr (i == K1_BI + 16 || i == K1_BI + 17) fh[i & 1] = lds128(rh[i & 1]);
        }
        if constexpr (TC && i == K1_JI) {
          constexpr int GC = (GUL * 8 + 6) / 7;
          touch_b(L0 + K1_SCR, slow ? 0x80000000u : (lane < GC ? lane : GC - 1) * 128, make_srd((wid & 1) ? wu : wg, D_ * F_ * 2), ((wid >> 1) * 224 + rk * 8 + xcd) * (GC * 128));
        }
        if constexpr (TC && i == K1_JI + 2) touch_b(L0 + K1_SCR, l3 * 2048 + l7 * 128, make_srd(wo, D_ * D_ * 2), (idx * 32 + wid * 8) * 2048 + xh * 1024);
        if constexpr (!LA) {
        } else if constexpr (i & 1) {
          constexpr int d = k1_op(true, c - 4);
          if constexpr (d >= 0 && d < 24 && (d & 2)) *(LAS f32x4*)(ho + ((d & 1) * 6 + (d >> 2)) * 1024) = acc[d & 3][d >> 2];
          if constexpr (d == 26)
            asm volatile("s_mov_b64 exec, %5\n\tds_write_b128 %0, %1\n\tds_write_b128 %0, %2 offset:128\n\ts_mov_b64 exec, 0xffff\n\tds_write_b32 %3, %4\n\ts_mov_b64 exec, -1"
                         ::"v"(L0 + CT + hh * 256 + th * 64 + fq * 16), "v"(accHQ), "v"(accHK), "v"(L0 + CV + hh * 128 + th * 64 + fr * 4), "v"(accHV[0]),
                           "s"(lanes([](int r, int) { return r == 0; })) : "memory");
        }
        SB();
      });
    };
#pragma nounroll
    for (int kt = 0; kt < 14; kt += 2) { ktile(IC<0>{}, IC<0>{}); ktile(IC<1>{}, IC<0>{}); }
    ktile(IC<0>{}, IC<K1_T14 ? 2 : 0>{}); ktile(IC<1>{}, IC<1>{});
    MFMA_DONE();
    STAMP_L1(0);
    EST(0);
    WAIT_L0(); EST(1); TS(); BARRIER();
  };
  if (wid < 4) products(); else loaders();
  EST(2);
  DLY(5);
  u32x4 qf[2], kf[3]; u32x6 vc[2]; float cq[2];
  {
    f32x4 ct[4]; float cv[2];
#pragma unroll
    for (int j = 0; j < 4; ++j) ct[j] = lds128f((ca ^ ((j & 1) * 64)) + (j >> 1) * 128);
#pragma unroll
    for (int j = 0; j < 2; ++j) cv[j] = *(LAS float*)(cb ^ (j * 64));
    float mn[2], rs[2]; f32x4 mv[2], rv[2];
#pragma unroll
    for (int m = 0; m < 2; ++m) {
      mn[m] = *(LAS float*)(sa + 64 + m * 64); rs[m] = *(LAS float*)(sa + 640 + m * 64);
      mv[m] = lds128f(sb + 64 + m * 64); rv[m] = lds128f(sb + 640 + m * 64);
    }
    const float hm = *(LAS float*)(sa - g * 128), hs = *(LAS float*)(sa - g * 128 + 576);
    const f32x4 hmv = lds128f(sb - g * 128), hsv = lds128f(sb - g * 128 + 576);
#pragma unroll
    for (int m = 1; m >= 0; --m) {
      const f32x4 k0 = (acc[m][2] + ct[2] * mn[m]) * rs[m], k1 = (acc[m][3] + ct[3] * mn[m]) * rs[m];
      kf[m + 1] = (u32x4){pk(k0[0], k0[1]), pk(k0[2], k0[3]), pk(k1[0], k1[1]), pk(k1[2], k1[3])};
#pragma unroll
      for (int j = 0; j < 2; ++j) {
        const f32x4 v = (acc[m][4 + j] + mv[m] * cv[j]) * rv[m];
        vc[j][2 + 2 * m] = pk(v[0], v[1]); vc[j][3 + 2 * m] = pk(v[2], v[3]);
      }
      if (m == 1) {
        const unsigned x0 = xb + (g == 1 ? 8 : 0), x1 = x0 ^ 8;
        *(LAS u32x2*)(x0 + 2048) = kf[2].xy; *(LAS u32x2*)(x1 + 2048) = kf[2].zw;
        *(LAS u32x2*)(x0 + 3072) = (u32x2){vc[0][4], vc[0][5]}; *(LAS u32x2*)(x1 + 3072) = (u32x2){vc[1][4], vc[1][5]};
      }
    }
    if (wid < 4) {
      const unsigned xh = xb - g * 2048 + th * 8;
      const f32x4 k = (accHK + ct[2] * hm) * hs;
      *(LAS u32x2*)(xh) = (u32x2){pk(k[0], k[1]), pk(k[2], k[3])};
      const f32x4 v = (accHV + hmv * cv[0]) * hsv;
      *(LAS u32x2*)(xh + 1024) = (u32x2){pk(v[0], v[1]), pk(v[2], v[3])};
    }
#pragma unroll
    for (int m = 0; m < 2; ++m) {
      const f32x4 q0 = acc[m][0] + ct[0] * mn[m], q1 = acc[m][1] + ct[1] * mn[m];
      qf[m] = (u32x4){pk(q0[0], q0[1]), pk(q0[2], q0[3]), pk(q1[0], q1[1]), pk(q1[2], q1[3])};
      cq[m] = rs[m] * (0.17677669529663687f * 1.4426950408889634f);
    }
  }
  f32x4 sc[2], sp[2];
  sc[0] = mfma(kf[1], qf[0], bc); sp[1] = mfma(kf[1], qf[1], bp); sc[1] = mfma(kf[2], qf[1], bc);
  rmu[0] = -*(LAS float*)(L0 + K1_ST + 64 + (wid & 3) * 128 + fr * 4); rmu[1] = -*(LAS float*)(L0 + K1_ST + 128 + (wid & 3) * 128 + fr * 4);
  WAIT_L0(); EST(3); TS(); BARRIER();
  u32x4 wt[2];
  auto ldw = [&]() { asm volatile("global_load_dwordx4 %0, %2, %3 sc1\n\tglobal_load_dwordx4 %1, %2, %3 offset:16 sc1" : "=&v"(wt[0]), "=&v"(wt[1]) : "v"(lane * 32), "s"(ws + WS_WTAB + (wid & 3) * 2048) : "memory"); };
  if (wid < 4) ldw();
  EST(4);
  {
    kf[0] = lds128(xb);
#pragma unroll
    for (int j = 0; j < 2; ++j) { const u32x2 t = *(volatile LAS u32x2*)(xb + 1024 + j * 8); vc[j][0] = t.x; vc[j][1] = t.y; }
    sp[0] = mfma(kf[0], qf[0], bp0);
    constexpr unsigned long long ODD = lanes([](int r, int) { return (r & 1) != 0; });
    float s[2][4], mx[2], sum[2];
#pragma unroll
    for (int m = 1; m >= 0; --m) {
      s[m][0] = selm(sp[m][0], sp[m][1], ODD); s[m][1] = selm(sp[m][2], sp[m][3], ODD);
      s[m][2] = selm(sc[m][0], sc[m][1], ODD); s[m][3] = selm(sc[m][2], sc[m][3], ODD);
      mx[m] = max2(max3(s[m][0], s[m][1], s[m][2]), s[m][3]);
    }
    {
      swap32f(mx[0], mx[1]);
      float t = max2(mx[0], mx[1]), u = t;
      unsigned a = __float_as_uint(t), b = __float_as_uint(u); swap16u(a, b);
      mx[0] = mx[1] = max2(__uint_as_float(a), __uint_as_float(b));
      swap32f(mx[0], mx[1]);
    }
    const unsigned sh = (lane & 1) * 16;
    u32x4 p[2];
#pragma unroll
    for (int m = 0; m < 2; ++m) {
      const float mc = mx[m] * cq[m];
#pragma unroll
      for (int e = 0; e < 4; ++e) { s[m][e] = __builtin_amdgcn_exp2f(s[m][e] * cq[m] - mc); p[m][e] = pk(s[m][e], 0.f) << sh; }
      sum[m] = (s[m][0] + s[m][1]) + (s[m][2] + s[m][3]);
    }
    f32x4 o[2][2];
    const f32x4 z = {0, 0, 0, 0};
#pragma unroll
    for (int j = 0; j < 2; ++j) {
      o[0][j] = mfma(__builtin_shufflevector(vc[j], vc[j], 0, 1, 2, 3), p[0], z);
      o[1][j] = mfma(__builtin_shufflevector(vc[j], vc[j], 2, 3, 4, 5), p[1], z);
    }
    {
      swap32f(sum[0], sum[1]);
      unsigned a = __float_as_uint(sum[0] + sum[1]), b = a; swap16u(a, b);
      sum[0] = sum[1] = __builtin_amdgcn_rcpf(__uint_as_float(a) + __uint_as_float(b));
      swap32f(sum[0], sum[1]);
    }
    char* of = ws + WS_O + (size_t)(2 * pn + hh) * 131072 + (size_t)(pm * 8 + 2 * g) * 1024 + lane * 16;
    u32x4 w[2];
#pragma unroll
    for (int m = 0; m < 2; ++m) {
      const f32x4 oa = o[m][0] * sum[m], ob = o[m][1] * sum[m];
      w[m] = (u32x4){pk(oa[0], oa[1]), pk(oa[2], oa[3]), pk(ob[0], ob[1]), pk(ob[2], ob[3])};
    }
    {
      unsigned a0 = w[0].x, a1 = w[0].y, a2 = w[0].z, a3 = w[0].w, b0 = w[1].x, b1 = w[1].y, b2 = w[1].z, b3 = w[1].w;
      asm volatile("s_bitcmp0_b32 %8, 0\n\ts_cbranch_scc1 1f\n\tv_swap_b32 %0, %2\n\tv_swap_b32 %1, %3\n\tv_swap_b32 %4, %6\n\tv_swap_b32 %5, %7\n1:"
                   : "+v"(a0), "+v"(a1), "+v"(a2), "+v"(a3), "+v"(b0), "+v"(b1), "+v"(b2), "+v"(b3) : "s"(th) : "scc");
      st16(of, (u32x4){a0, a1, a2, a3}); st16(of + 1024, (u32x4){b0, b1, b2, b3});
    }
  }
  EST(5); EST_OUT();
  TS_OUT();
  BW_OUT(0);
  STAMP_E1();
  WAIT_V(0);
  if (wid < 4) {
    const unsigned mark = mark_of(L0);
    asm volatile("" : "+v"(wt[0]), "+v"(wt[1]));
#pragma nounroll
    for (int spin = 0; spin < (1 << 18); ++spin) {
      if (__builtin_expect(__builtin_amdgcn_ballot_w64(wt[0].y != mark || wt[0].w != mark || wt[1].y != mark || wt[1].w != mark) == 0, 1)) break;
      __builtin_amdgcn_s_sleep(16);
      ldw();
      asm volatile("s_waitcnt vmcnt(0)" : "+v"(wt[0]), "+v"(wt[1])::"memory");
    }
  }
  if (lane == 0 && __hip_atomic_fetch_add((LAS unsigned*)(L0 + K3_CNT + 12), 1u, __ATOMIC_RELAXED, __HIP_MEMORY_SCOPE_WORKGROUP) == 7)
    __hip_atomic_fetch_or((unsigned*)(ws + WS_F1 + pm * 128), 1u << pn, __ATOMIC_RELAXED, __HIP_MEMORY_SCOPE_AGENT);
  if (wid < 4) {
    const unsigned t = L0 + K2_TAB + (wid * 8 + l3) * 192 + (lane & 3) * 48 + (lane & 4) * 2;
    *(LAS u32x2*)(t) = (u32x2){__builtin_amdgcn_perm(wt[0].z, wt[0].x, 0x05040100u), __builtin_amdgcn_perm(wt[1].z, wt[1].x, 0x05040100u)};
    *(LAS u32x2*)(t + 16) = (u32x2){__builtin_amdgcn_perm(wt[0].z, wt[0].x, 0x07060302u), __builtin_amdgcn_perm(wt[1].z, wt[1].x, 0x07060302u)};
    *(LAS u32x2*)(t + 32) = (u32x2){0, 0};
    WAIT_L0();
    STAMP_E2(0);
  }
}

#define K4_LAG 2
#define K4_LEAD 3
#define K4_BLEAD 0
#define K2_BLEAD 1
#define K4_JIT 2
#define K4_SLEEP 2
#define K4_S 52
#define K4_POLL 3
#define K2_S 12
#define K2_POLL 2
constexpr int K4_SLOT = 16384, K4_NS = 8, K4_SCR = K3_SCR, K4_R = 112 - K4_S, K4_R0 = 6;
static_assert(K4_R0 * K4_SLOT >= K3_G + K3_BP && K4_NS * K4_SLOT <= K3_SCR, "");
#define K34_PM 2
#define K4_WL 8
#define K4_WI 8
#define K4_RI 1
#define K34_SLEEP 8
DI void k3_done(char* __restrict__ ws, const unsigned L0) {
  const int xcd = blockIdx.x & 7, idx = blockIdx.x >> 3;
  if (lane_now() == 0 && __hip_atomic_fetch_add((LAS unsigned*)(L0 + K3_CNT), 1u, __ATOMIC_RELAXED, __HIP_MEMORY_SCOPE_WORKGROUP) == 7)
    __hip_atomic_fetch_add((unsigned*)(ws + WS_CNT) + ((xcd >> 2) * 4 + (idx >> 3)) * 64 + 32 + (xcd & 3) * 4 + ((idx & 7) >> 1), 1u, __ATOMIC_RELAXED,
                           __HIP_MEMORY_SCOPE_AGENT);
}
constexpr int K2_R = 32 - K2_S;
static_assert(K4_S >= 50 && K4_S <= 54 && K4_S % 2 == 0 && K2_S % 2 == 0, "");

DI void k4_loaders(const u16* __restrict__ wmat, char* __restrict__ ws, const unsigned L0, const int wid) {
  constexpr int S = K4_S, R = K4_R, BL = K4_BLEAD;
  const int lane = lane_now();
  BW_DECL();
  const int xcd = blockIdx.x & 7, idx = blockIdx.x >> 3;
  const int kh = xcd & 1, pm = (xcd >> 2) * 8 + (idx >> 2), pq = ((xcd >> 1) & 1) * 4 + (idx & 3);
  const int j = wid, ca = idx & 3;
#define LPRIO 3
  __builtin_amdgcn_s_setprio(LPRIO);
  const u32x4 srdA = make_srd(ws + WS_ACT, 112 * 131072), srdB = make_srd(wmat, F_ * D_ * 2);
  const unsigned voffA = lane * 16 + pm * 8192;
  const unsigned voffB = (unsigned)(lane >> 3) * 2048 + pq * 256 + (unsigned)((lane & 7) ^ (((lane >> 4) & 3) << 1)) * 16;
  auto run = [&](auto na, auto pa, auto nb, auto lag, int qa, int qb) {
    constexpr int NA = decltype(na)::value, PA = decltype(pa)::value, NB = decltype(nb)::value, LG = decltype(lag)::value;
    constexpr int NF = NA + NB, NO = (NA + 1 - PA) / 2 + NB;
    static_assert(LG >= 2 && LG <= 4, "");
    unsigned va[NA + 1], la[NA + 1], vb[3][NB + 1], lb[NB + 1];
#pragma unroll
    for (int i = 0; i < NA; ++i) { const int q = (qa + i) & 7; va[i] = voffA + (q ^ kh) * 1024; la[i] = L0 + q * 1024; }
#pragma unroll
    for (int i = 0; i < NB; ++i) {
      const int q = (qb + i) & 7, r = q & 3;
      const unsigned v = voffB + (q >> 2) * 128;
      vb[0][i] = v + 8 * r * 2048; vb[1][i] = v + (56 * (r & 1) + 8 * (r >> 1)) * 2048; vb[2][i] = v + (112 * (r & 1) + 4 * (r >> 1) + 52 * (lane >> 5)) * 2048;
      lb[i] = L0 + 8192 + q * 1024;
    }
    unsigned so = K4_R0 * K4_SLOT, sa = kh * (63 * 131072), sb = kh * (9 * 224 * 2048);
    unsigned tc = 0, lim = 0;
    auto ask = [&]() {
#pragma nounroll
      for (int spin = 0; spin < (1 << 18); ++spin) {
        if (NA == 2) {
          unsigned cv;
          asm volatile("s_mov_b64 exec, 0xffff\n\tglobal_atomic_add %0, %1, %3, %2 sc0\n\ts_mov_b64 exec, -1\n\ts_waitcnt vmcnt(0)" : "=&v"(cv) : "v"((lane & 15) * 4), "s"(ws + WS_CNT + (pm >> 1) * 256 + 128), "v"(0) : "memory");
          const unsigned m = (unsigned)__builtin_amdgcn_ballot_w64(cv < 2u) & 0xFFFFu;
          const unsigned q = (kh ? ((m >> 9) & 127) | ((m >> 1) & 128) | ((m << 1) & 256) : m & 511) | (((m >> (((xcd ^ 1) & 3) * 4 + ((idx & 7) >> 1))) & 1) << 8);
          const unsigned r = __builtin_ctz(q | 512);
          lim = r == 9 ? 64 : kh && r == 8 ? 52 : 7 * r;
          if (lim > tc) { *(volatile LAS unsigned*)(L0 + K3_CNT + 8) = lim; break; }
          __builtin_amdgcn_s_sleep(K34_SLEEP);
        } else {
          lim = __builtin_amdgcn_readfirstlane(*(volatile LAS unsigned*)(L0 + K3_CNT + 8));
          if (lim > tc) break;
          __builtin_amdgcn_s_sleep(1);
        }
      }
    };
    if (NA) { WAIT_V(0); k3_done(ws, L0); }
    const unsigned voffJ = (unsigned)(7 * idx + (lane < 7 ? lane : 6)) * 2048 + ((xcd >> 1) & 1) * 1024 + ((3 - (int)((uint64_t)wmat >> 7)) & 7) * 128;
    auto slot = [&](auto tt, int kk) {
      constexpr int t = decltype(tt)::value;
      if (t < R) {
        if (NA) { if (__builtin_expect(tc >= lim, 0)) ask(); ++tc; }
#pragma unroll
        for (int i = 0; i < NA; ++i) if (t < S || ((PA + i) & 1) == 0) dma2s(la[i], so, va[i], srdA, sa);
        const unsigned sr = sb + (kk < 4 ? 56 * kk : kk < 6 ? 112 * kk - 416 : 48) * 2048;
#pragma unroll
        for (int i = 0; i < NB; ++i) dma2(lb[i], so, vb[kk < 4 ? 0 : kk < 6 ? 1 : 2][i], srdB, sr);
        if (NA == 2 && kk == 0 && t < 49 && t > 0) {
          const unsigned s2 = sb + 2 * 224 * 2048;
          touch_b(L0 + K4_SCR, voffJ, srdB, s2 >= 3584 * 2048 ? s2 - 9 * 224 * 2048 : s2);
        }
        so = (so + K4_SLOT) & (K4_NS * K4_SLOT - 1); sa += 131072;
        if (kk == 6) sb += 224 * 2048;
      }
      if (t == 3) {
        if (!NA) { WAIT_V(4 * NF); k3_done(ws, L0); }
        WAIT_V(2 * NF);
        if (lane == 0) __hip_atomic_fetch_add((LAS unsigned*)(L0 + K3_CNT + 4), 1u, __ATOMIC_RELAXED, __HIP_MEMORY_SCOPE_WORKGROUP);
        BARRIER();
      }
      if (t > LG + 1 && (t - LG) % 2 == 1) {
        constexpr int lo = t - LG + 1, hi = t < R ? t : R - 1;
        constexpr int n = hi >= lo ? hi - lo + 1 : 0, nf = lo < S ? (hi < S ? n : S - lo) : 0;
        WAIT_V(nf * NF + (n - nf) * NO);
        BARRIER();
      }
    };
    sfor<0, LG + 2>([&](auto tt) { slot(tt, decltype(tt)::value); });
    if (NA == 2) { touch_b(L0 + K4_SCR, voffJ, srdB, sb + 224 * 2048); touch_b(L0 + K4_SCR, voffJ, srdB, sb + 448 * 2048); }
    constexpr int UN = NB ? 14 : 2;
#pragma nounroll
    for (int i = 0; i < 42 / UN; ++i) sfor<LG + 2, LG + 2 + UN>([&](auto tt) { slot(tt, decltype(tt)::value % 7); });
    sfor<LG + 44, 49>([&](auto tt) { slot(tt, decltype(tt)::value % 7); });
    if (kh || NB == 0) {
      sa = (kh ? R : 49) * 131072; sb = 8 * 224 * 2048;
      sfor<49, S>([&](auto tt) { slot(tt, decltype(tt)::value - 49 + R - 56); });
    } else sfor<49, S>([&](auto tt) { slot(tt, decltype(tt)::value - 49); });
    sa = S * 131072; sb = 7 * 224 * 2048;
    sfor<S, R + LG>([&](auto tt) { slot(tt, decltype(tt)::value % 7); });
  };
  if (j == 3) run(IC<2>{}, IC<0>{}, IC<0>{}, IC<K4_LEAD>{}, 2 * ca, 0);
  else if (j == 0) run(IC<6>{}, IC<0>{}, IC<0>{}, IC<K4_LAG>{}, 2 * ca + 2, 0);
  else run(IC<0>{}, IC<0>{}, IC<4>{}, IC<K4_LAG>{}, 0, 4 * j);
  BW_OUT(3);
}
DI unsigned xword(unsigned v) { return v < 0xFFFFFFFEu ? v : 0xFFFFFFFEu; }
DI unsigned max4(u32x4 v) { return max(max(v.x, v.y), max(v.z, v.w)); }

DI void k4_mfma(const u16* __restrict__ wd, char* __restrict__ ws, u16* __restrict__ out, const unsigned L0, const int wid, float (&rmu)[2]) {
  const int lane = lane_now();
  BW_DECL();
  const int xcd = blockIdx.x & 7, idx = blockIdx.x >> 3;
  const int kh = xcd & 1, pm = (xcd >> 2) * 8 + (idx >> 2), pq = ((xcd >> 1) & 1) * 4 + (idx & 3);
  const int gm = wid & 1, gn = wid >> 1, fr = lane & 15, fq = lane >> 4;
  const int mt = pm * 8 + 4 * gm + kh;
  unsigned rv = lane * 16;
  STAMP_BEG(3);
  TS_DECL(3, K3_LDS); TS();
  u32x4 rh[2][2];
  const char* rp = ws + WS_HC + (size_t)(4 * pq + 2 * gn) * 131072 + (size_t)mt * 1024;
  f32x4 acc[4][4];
  unsigned ra = L0 + 4 * K4_SLOT + gm * 4096 + lane * 16, rb[4];
  {
    const int q = fr >> 2, p = lane & 3, sw = (2 * fq + (q >> 1)) & 3;
#pragma unroll
    for (int n = 0; n < 4; ++n) rb[n] = L0 + 4 * K4_SLOT + 8192 + gn * 4096 + (unsigned)(4 * fq + q) * 128 + (unsigned)(n ^ sw) * 32 + 8 * p;
  }
  u32x4 sel[2];
  {
    const unsigned one = 0x3F80u << ((fr & 1) * 16);
#pragma unroll
    for (int c = 0; c < 2; ++c)
#pragma unroll
      for (int e = 0; e < 4; ++e) sel[c][e] = fq == (fr >> 2) && e == 2 * c + ((fr >> 1) & 1) ? one : 0u;
    asm volatile("" : "+v"(sel[0]), "+v"(sel[1]));
  }
  u32x4 fa[2][4]; u32x2 fb[4][2][2];
  auto rdA = [&](int o, int d, int m, int h) { fa[h][d] = lds128(ra + (2 * o + h) * K4_SLOT + m * 1024); };
  auto rdB = [&](int o, int n, int i) { fb[n][i >> 1][i & 1] = ldstr(rb[n] + (2 * o + (i >> 1)) * K4_SLOT + (i & 1) * 2048); };
  auto pair = [&](auto oo, auto fl, unsigned dl, auto&& at) {
    constexpr int o = oo.value;
    constexpr bool F0 = fl.value & 1, LS = fl.value & 2;
    sfor<0, 32>([&](auto ii) {
      constexpr int i = ii.value, n = (i >> 2) & 3, m = 2 * ((((i >> 1) + 1) >> 1) & 1) + (i < 16), h = (i ^ (i >> 1)) & 1;
      if constexpr (i == 16) { WAIT_L0(); BARRIER(); TS(); }
      if constexpr (F0 && i < 16 && (i & 1) == 0) mfma_new(acc[m][n], cat(fb[n][h][0], fb[n][h][1]), fa[h][m]);
      else mfma_acc(acc[m][n], cat(fb[n][h][0], fb[n][h][1]), fa[h][m]);
      if constexpr (i < 4) { rdB(o, 2, i); rdA(o, (i >> 1) * 2, (i >> 1) * 2, i & 1); }
      if constexpr (i >= 4 && i < 8) rdB(o, 3, i - 4);
      if constexpr (o == 1 && i >= 8 && i < 13) vadd(i == 12 ? ra : rb[(i - 8) & 3], dl);
      if constexpr (i >= 16 && i < 20) rdA(o ^ 1, ((i - 16) >> 1) * 2 + 1, ((i - 16) >> 1) * 2 + !LS, i & 1);
      if constexpr (i >= 20 && i < 28) rdB(o ^ 1, (i - 20) >> 2, i & 3);
      at(ii);
      SB();
    });
  };
  auto own = [&](auto oo, auto nx, unsigned dl, auto&& pre, auto&& at) {
    constexpr int o = oo.value;
    sfor<0, 16>([&](auto ii) {
      constexpr int i = ii.value, n = i >> 2, m = 2 * ((((i >> 1) + 1) >> 1) & 1), h = (i ^ (i >> 1)) & 1;
      if constexpr (i == 8 && nx.value) { pre(); WAIT_L0(); BARRIER(); TS(); }
      mfma_acc(acc[m][n], cat(fb[n][h][0], fb[n][h][1]), fa[h][m + o]);
      if constexpr (i < 4) { rdB(o, 2 + (i >> 1), 2 * (i & 1)); rdB(o, 2 + (i >> 1), 2 * (i & 1) + 1); }
      if constexpr (nx.value) {
        if constexpr (o == 1 && i >= 4 && i < 8) { vadd(rb[i - 4], dl); if (i == 4) vadd(ra, dl); }
        constexpr int r = i - 8;
        if constexpr (r == 0 || r == 1) { rdB(o ^ 1, 0, 2 * r); rdB(o ^ 1, 0, 2 * r + 1); rdA(o ^ 1, o ^ 1, 0, r); }
        if constexpr (r == 2 || r == 3) { rdA(o ^ 1, 3 - o, 2, r & 1); rdB(o ^ 1, 1, 2 * (r & 1)); rdB(o ^ 1, 1, 2 * (r & 1) + 1); }
      }
      at(ii);
      SB();
    });
  };
  auto none = [](auto) {};
  static_assert(K4_S == 52 && K4_R0 == 6, "");
#pragma unroll
  for (int n = 0; n < 4; ++n) {
    acc[0][n] = rmu[0]; acc[2][n] = rmu[1];
    asm volatile("" : "+v"(acc[0][n]), "+v"(acc[2][n]));
  }
  const bool late = __builtin_amdgcn_readfirstlane(*(volatile LAS unsigned*)(L0 + K3_CNT + 4)) >= 4u;
  if (!late) { WAIT_V(0); k3_done(ws, L0); }
  BARRIER();
  rdB(1, 0, 0); rdB(1, 0, 1); rdA(1, 1, 1, 0); rdB(1, 0, 2); rdB(1, 0, 3); rdA(1, 1, 1, 1); rdA(1, 3, 3, 1); rdA(1, 3, 3, 0);
#pragma unroll
  for (int i = 0; i < 4; ++i) rdB(1, 1, i);
  STAMP_L0(); TS();
  pair(IC<1>{}, IC<1>{}, 0u - 4 * K4_SLOT, none);
#define K34_SI 4
  pair(IC<0>{}, IC<0>{}, 0, [&](auto ii) { if constexpr (ii.value == K34_SI) { if (late) { WAIT_V(0); k3_done(ws, L0); } }
    if constexpr (K4_WL < 8 && ii.value == K4_WI) {
      const int l = lane < 56 ? lane : 55, w = wid + 4, P = kh ? (w < 7 ? 9 + w : 8) : w;
      if (w >= K4_WL) touch_b(L0 + K4_SCR, (l >> 3) * 2048 + (l & 7) * 128, make_srd(wd, D_ * F_ * 2), (224 * P + 7 * idx) * 2048 + ((xcd >> 1) & 1) * 1024);
    }
  });
  pair(IC<1>{}, IC<0>{}, 4 * K4_SLOT, [&](auto ii) {
    if constexpr (ii.value == 14) ld16s<0>(rh[0][0], rv, rp);
    if constexpr (ii.value == 15) ld16s<2048>(rh[1][0], rv, rp);
    if constexpr (ii.value == 28) ld16s<0>(rh[0][1], rv, rp + 131072);
    if constexpr (ii.value == 30) ld16s<2048>(rh[1][1], rv, rp + 131072);
  });
  unsigned dl = 0u - 4 * K4_SLOT;
#pragma nounroll
  for (int it = 0; it < 11; ++it) {
    pair(IC<0>{}, IC<0>{}, dl, none);
    pair(IC<1>{}, IC<0>{}, dl, none);
    dl = 0u - dl;
  }
  const uint64_t xo = (uint64_t)(ws + WS_XCH) + (size_t)(blockIdx.x * 4 + wid) * 4096, xi = xo + (1 - 2 * kh) * 16384;
  u32x4 y[4], xs[2];
  pair(IC<0>{}, IC<2>{}, 0, [&](auto ii) {
    constexpr int j = ii.value - 16;
    if constexpr (j >= 0) {
      constexpr int n = j >> 2, mi = (j >> 1) & 1;
      const f32x4 v = acc[2 * mi + 1][n];
      xs[mi][2 * (n & 1) + (j & 1)] = xword(pk(v[2 * (j & 1)], v[2 * (j & 1) + 1]));
      if ((j & 5) == 5) st16s<(2 * mi + (n >> 1)) * 1024>(xo, rv, xs[mi]);
    }
  });
  auto res = [&](int i) { mfma_acc(acc[2 * (i >> 2)][i & 3], sel[i & 1], rh[i >> 2][(i >> 1) & 1]); };
  WAIT_V(4);
  asm volatile("" : "+v"(rh[0][0]), "+v"(rh[0][1]), "+v"(rh[1][0]), "+v"(rh[1][1]));
  own(IC<1>{}, IC<1>{}, 4 * K4_SLOT, [&]() { if (K4_RI) { res(2); res(6); } }, none);
  auto ldy = [&]() {
    asm volatile("global_load_dwordx4 %0, %4, %5 sc1\n\tglobal_load_dwordx4 %1, %4, %5 offset:1024 sc1\n\t"
                 "global_load_dwordx4 %2, %4, %5 offset:2048 sc1\n\tglobal_load_dwordx4 %3, %4, %5 offset:3072 sc1"
                 : "=&v"(y[0]), "=&v"(y[1]), "=&v"(y[2]), "=&v"(y[3]) : "v"(rv), "s"(xi) : "memory");
  };
#define K4_PI 0
  own(IC<0>{}, IC<1>{}, 0, [&]() { if (K4_RI) { res(3); res(7); } }, none);
  own(IC<1>{}, IC<1>{}, 0u - 4 * K4_SLOT, [&]() { if (K4_RI) { res(0); res(4); } }, [&](auto ii) { if (ii.value == K4_PI) ldy(); });
  own(IC<0>{}, IC<0>{}, 0, []() {}, [&](auto ii) { if (ii.value + 16 == K4_PI) ldy(); });
  unsigned tw = L0 + 2 * K4_SLOT + wid * 4096 + fr * 128 + (unsigned)((fq >> 1) ^ (fr & 7)) * 16 + (fq & 1) * 8;
  unsigned tr = L0 + 2 * K4_SLOT + wid * 4096 + (lane >> 3) * 128 + (unsigned)((lane & 7) ^ (lane >> 3)) * 16;
  unsigned tx[4], ov = (lane >> 3) * 2048 + (lane & 7) * 16;
  const u16* op = out + (size_t)mt * 16 * D_ + pq * 128 + gn * 64;
#pragma unroll
  for (int n = 0; n < 4; ++n) { tx[n] = tw ^ (n * 32); asm volatile("" : "+v"(tx[n])); }
  asm volatile("" : "+v"(ov), "+v"(tr));
  if (K4_RI) { res(1); res(5); } else sfor<0, 8>([&](auto ii) { res(ii.value); });
  STAMP_L1(3);
  asm volatile("s_waitcnt vmcnt(0)" : "+v"(y[0]), "+v"(y[1]), "+v"(y[2]), "+v"(y[3])::"memory");
#pragma nounroll
  for (int spin = 0; spin < (1 << 18); ++spin) {
    if (__builtin_amdgcn_ballot_w64(max(max(max4(y[0]), max4(y[1])), max(max4(y[2]), max4(y[3]))) == 0xFFFFFFFFu) == 0) break;
    __builtin_amdgcn_s_sleep(K4_SLEEP);
    ldy();
    asm volatile("s_waitcnt vmcnt(0)" : "+v"(y[0]), "+v"(y[1]), "+v"(y[2]), "+v"(y[3])::"memory");
  }
  sfor<0, 8>([&](auto ii) { constexpr int i = ii.value; mfma_acc(acc[2 * (i >> 2)][i & 3], sel[i & 1], y[i >> 1]); });
  u32x4 r[4];
#pragma unroll
  for (int mi = 0; mi < 2; ++mi) {
    SB(); asm volatile("s_nop 3"); SB();
#pragma unroll
    for (int n = 0; n < 4; ++n) {
      const f32x4 v = acc[2 * mi][n];
      *(LAS u32x2*)(tx[n] + mi * 2048) = (u32x2){pk(v[0], v[1]), pk(v[2], v[3])};
    }
    MEMFENCE();
    r[2 * mi] = lds128(tr + mi * 2048); r[2 * mi + 1] = lds128(tr + mi * 2048 + 1024);
  }
#pragma unroll
  for (int c = 0; c < 4; ++c) asm volatile("global_store_dwordx4 %0, %1, %2 sc1" ::"v"(ov), "v"(r[c]), "s"(op + ((c >> 1) * 32 + (c & 1) * 8) * D_) : "memory");
  TS_OUT();
  BW_OUT(3);
  STAMP_END(3);
}

#define SA 0
#define K2_RA 3
#define K2_RE 6
#define K2_XI 4
#define K2_TI 0
#define K4_CODE 16384
#define K12_SLEEP 2
#define FIXF1 1
#define K23_SLEEP 1
#define K3_B2 0
#define K3_POS 41, 43, 45, 47, 12, 28, 50, 53
constexpr int k3_at(bool first, int i) {
  constexpr int pos0[8] = {K3_POS}, pos[8] = {K3_POS};
  for (int k = 0; k < 8; ++k) if (i == (first ? pos0[k] : pos[k])) return k;
  return -1;
}
DI unsigned geglu2(f32x2 g, f32x2 u, float k1, float k2, float ir2) {
  const f32x2 z = g * (g * g * k2 + k1);
  f32x2 e; e.x = __builtin_amdgcn_exp2f(z.x); e.y = __builtin_amdgcn_exp2f(z.y);
  const f32x2 d = e * ir2 + ir2;
  f32x2 r; r.x = __builtin_amdgcn_rcpf(d.x); r.y = __builtin_amdgcn_rcpf(d.y);
  const f32x2 o = (g * u) * r;
  return pk(o.x, o.y);
}

constexpr int K2_SLOT = 12288, K2_NS = 10, K2_R0 = 2, K2_X = K2_TAB + 6144;
static_assert(K2_TAB >= K2_SLOT * K2_NS && K2_X + 16384 <= K3_SCR && K2_TAB % 16 == 0, "");
extern "C" __global__ __launch_bounds__(512, 2) void k_all(const u16* __restrict__ x, const u16* __restrict__ wqkv, const u16* __restrict__ wo,
                                                           const u16* __restrict__ wg, const u16* __restrict__ wu, const u16* __restrict__ wd,
                                                           char* __restrict__ ws, u16* __restrict__ out) {
  __shared__ __attribute__((aligned(1024))) char smem[K3_LDS + TS_EXTRA];
  unsigned long long t0_ = 0;
  const unsigned L0 = (unsigned)(size_t)(LAS char*)smem;
  const int wid = __builtin_amdgcn_readfirstlane(threadIdx.x >> 6);
  if (wid < C0N) {
    CODE_ADDR(self, k_all);
    touch_g(L0 + K3_SCR, (threadIdx.x & 255) * 128, self);
  }
  SB();
  float mux[2];
  DLY(0);
  k1_part(x, wqkv, wo, wg, wu, ws, out, L0, wid, mux, t0_);
  const int lane = lane_now();
  BW_DECL();
  const int wm = wid & 3, wn = wid >> 2, fr = lane & 15, fq = lane >> 4;
  const int xcd = blockIdx.x & 7, idx = blockIdx.x >> 3;
  const int pm3 = (xcd >> 2) * 4 + (idx >> 3), pn3 = (xcd & 3) * 8 + (idx & 7);
  const u32x4 srdG = make_srd(wg, D_ * F_ * 2), srdU = make_srd(wu, D_ * F_ * 2);
  DLY(1);
  if (wid >= 4) {
    const int pm = 2 * pm3 + (xcd & 1), pn = ((xcd >> 1) & 1) * 8 + (idx & 7);
    const int j = wid - 4, ca = idx & 7, cb = idx >> 3;
    __builtin_amdgcn_s_setprio(LPRIO);
    const u32x4 srdA = make_srd(ws + WS_O, 32 * 131072), srdB = make_srd(wo, D_ * D_ * 2);
    const unsigned voffA = lane * 16 + pm * 8192;
    const unsigned voffB = (unsigned)(lane >> 3) * 2048 + pn * 128 + (unsigned)((lane & 7) ^ (((lane >> 4) & 3) << 1)) * 16;
    auto run = [&](auto na, auto nb, auto lag, int qa, int qb) {
      constexpr int NA = decltype(na)::value, NB = decltype(nb)::value, LG = decltype(lag)::value, N = NA + NB;
      static_assert(LG <= K2_NS - 4, "");
      unsigned va[NA + 1], la[NA + 1], vb[NB + 1], lb[NB + 1];
#pragma unroll
      for (int i = 0; i < NA; ++i) { const int q = (qa + i) & 7; va[i] = voffA + q * 1024; la[i] = L0 + q * 1024; }
#pragma unroll
      for (int i = 0; i < NB; ++i) { const int q = (qb + i) & 3; vb[i] = voffB + q * 16384; lb[i] = L0 + 8192 + q * 1024; }
      unsigned so = K2_R0 * K2_SLOT, sa = 0;
      if (NA) {
#pragma nounroll
        for (int spin = 0; spin < (1 << 18); ++spin) {
          if (NA == 1 && FIXF1 && __builtin_amdgcn_readfirstlane(*(volatile LAS unsigned*)(L0 + K3_CNT + 12)) < 8u) {
            __builtin_amdgcn_s_sleep(1);
          } else if (NA == 1) {
            unsigned cv;
            asm volatile("s_mov_b64 exec, 1\n\tglobal_atomic_or %0, %1, %1, %2 sc0\n\ts_mov_b64 exec, -1\n\ts_waitcnt vmcnt(0)" : "=&v"(cv) : "v"(0), "s"(ws + WS_F1 + pm * 128) : "memory");
            if (__builtin_amdgcn_readfirstlane(cv) == 0xFFFFu) { *(volatile LAS unsigned*)(L0 + K3_CNT + 16) = 1; break; }
            __builtin_amdgcn_s_sleep(K12_SLEEP);
          } else {
            if (__builtin_amdgcn_readfirstlane(*(volatile LAS unsigned*)(L0 + K3_CNT + 16))) break;
            __builtin_amdgcn_s_sleep(1);
          }
        }
      }
      auto issue = [&]() {
#pragma unroll
        for (int i = 0; i < NA; ++i) dma2s(la[i], so, va[i], srdA, sa);
        const unsigned sr = sa >> 1;
#pragma unroll
        for (int i = 0; i < NB; ++i) dma2(lb[i], so, vb[i], srdB, sr);
        so = so == (K2_NS - 1) * K2_SLOT ? 0 : so + K2_SLOT; sa += 131072;
      };
      constexpr int R0 = (NA == 1 ? K2_RE : K2_RA) < LG ? (NA == 1 ? K2_RE : K2_RA) : LG;
#pragma unroll
      for (int u = 0; u < R0 + 2; ++u) issue();
      WAIT_V(N * R0); BARRIER();
#pragma unroll
      for (int u = R0; u < LG; ++u) issue();
#pragma nounroll
      for (int i = 1; i < (32 - LG) / 2; ++i) { issue(); issue(); WAIT_V(N * LG); BARRIER(); }
      if (LG & 1) issue();
      sfor<(32 - LG) / 2, 16>([&](auto b) { WAIT_V(N * (30 - 2 * b.value)); BARRIER(); });
    };
#define K2_LL 6
#define K2_LA 3
#define K2_LB 3
    if (j == 3) run(IC<1>{}, IC<1>{}, IC<K2_LL>{}, ca, cb);
    else if (j == 0) run(IC<4>{}, IC<0>{}, IC<K2_LA>{}, ca + 1, 0);
    else if (j == 1) run(IC<3>{}, IC<0>{}, IC<K2_LA>{}, ca + 5, 0);
    else run(IC<0>{}, IC<3>{}, IC<K2_LB>{}, 0, cb + 1);
    __builtin_amdgcn_s_setprio(0);
    const u32x4 srdW = (j & 1) ? srdU : srdG;
    const unsigned lw = L0 + K3_G + (j >> 1) * K3_BS + (j & 1) * K3_UG;
#pragma unroll
    for (int i = 0; i < 7; ++i) {
      const unsigned L = i * 64 + lane, r = (L * 4682u) >> 16;
      dma(lw + i * 1024, r * 6944 + L * 16 + pn3 * 224, srdW, (j >> 1) * (32 * 7168));
    }
  } else {
    const int pm = 2 * pm3 + (xcd & 1), pn = ((xcd >> 1) & 1) * 8 + (idx & 7);
    const int g = wid;
    STAMP_BEG(1);
    TS_DECL(1, K3_LDS);
    const int tok = pm * 128 + 32 * g + fr;
    f32x4 acc[2][4], accW[2];
  #pragma unroll
    for (int m = 0; m < 2; ++m) {
      accW[m] = (f32x4){0, 0, 0, 0};
      asm volatile("" : "+v"(accW[m]));
  #pragma unroll
      for (int n = 0; n < 4; ++n) { acc[m][n] = (f32x4){0, 0, 0, 0}; asm volatile("" : "+v"(acc[m][n])); }
    }
    unsigned ra = L0 + K2_R0 * K2_SLOT + g * 2048 + lane * 16, rb[4];
    {
      const int q = fr >> 2, p = lane & 3, sw = (2 * fq + (q >> 1)) & 3;
  #pragma unroll
      for (int n = 0; n < 4; ++n) rb[n] = L0 + K2_R0 * K2_SLOT + 8192 + (unsigned)(4 * fq + q) * 128 + (unsigned)(n ^ sw) * 32 + 8 * p;
    }
    unsigned rw = L0 + K2_TAB + fq * 48 + (fr < 2 ? fr : 2) * 16;
    const unsigned rx = L0 + K2_X + g * 4096 + fr * 128;
    u32x4 sel[2];
    {
      const unsigned one = 0x3F80u << ((fr & 1) * 16);
  #pragma unroll
      for (int c = 0; c < 2; ++c)
  #pragma unroll
        for (int e = 0; e < 4; ++e) sel[c][e] = fq == 2 * c + (fr >> 3) && e == ((fr >> 1) & 3) ? one : 0u;
      asm volatile("" : "+v"(sel[0]), "+v"(sel[1]));
    }
    u32x4 fa[2][2][2], fw[2], fx[4]; u32x2 fb[4][2][2];
    auto rdA = [&](int o, int i) { fa[o][i & 1][i >> 1] = lds128(ra + (i & 1) * K2_SLOT + (i >> 1) * 1024); };
    auto rdB = [&](int n, int i) { fb[n][i >> 1][i & 1] = ldstr(rb[n] + (i >> 1) * K2_SLOT + (i & 1) * 2048); };
    unsigned pl = K2_R0 / 2;
    auto pair = [&](auto oo, auto nx, auto&& at) {
      constexpr int o = oo.value;
      const unsigned dl = pl == K2_NS / 2 - 1 ? (unsigned)(-(K2_NS - 2) * K2_SLOT) : 2u * K2_SLOT;
      pl = pl == K2_NS / 2 - 1 ? 0 : pl + 1;
      sfor<0, 20>([&](auto ii) {
        constexpr int i = ii.value, n = i >> 2, m = (((i >> 1) + 1) >> 1) & 1, h = (i ^ (i >> 1)) & 1;
        if constexpr (i == 12 && nx.value) { WAIT_L0(); BARRIER(); TS(); }
        if constexpr (n < 4) mfma_acc(acc[m][n], cat(fb[n][h][0], fb[n][h][1]), fa[o][h][m]);
        else mfma_acc(accW[m], fw[h], fa[o][h][m]);
        if constexpr (i < 4) { rdB(2, i); if (i < 2) rdB(3, i); }
        if constexpr (i == 4 || i == 5) { rdB(3, i - 2); fw[i - 4] = lds128(rw + (i - 4) * 192); }
        if constexpr (nx.value) {
          if constexpr (i >= 6 && i < 10) vadd(rb[i - 6], dl);
          if constexpr (i == 10) vadd(ra, dl);
          if constexpr (i == 11) vadd(rw, 384);
          if constexpr (i >= 12 && i < 16) { rdB(0, i - 12); rdA(o ^ 1, i - 12); }
          if constexpr (i >= 16) rdB(1, i - 16);
        }
        at(ii);
        SB();
      });
    };
    auto none = [](auto) {};
    unsigned vx[2];
  #pragma unroll
    for (int i = 0; i < 2; ++i) vx[i] = (unsigned)(pm * 128 + 32 * g + 8 * i + (lane >> 3)) * 2048 + pn * 128 + (unsigned)((lane & 7) ^ (4 * i + (lane >> 4))) * 16;
    const uint64_t ta = (uint64_t)wo + (32 * idx + 8 * g) * 2048 + ((pn & 8) + ((3 - (int)((uint64_t)wo >> 7)) & 7)) * 128;
    const unsigned tv = (lane & 7) * 2048;
    ALN(AL2);
    BARRIER();
  #pragma unroll
    for (int i = 0; i < 4; ++i) { rdB(0, i); rdA(0, i); }
  #pragma unroll
    for (int i = 0; i < 4; ++i) rdB(1, i);
    const u32x4 srdX = make_srd(x, S_ * D_ * 2);
    STAMP_L0(); TS();
  #pragma nounroll
    for (int it = 0; it < 7; ++it) {
      const unsigned u = it - K2_XI;
      auto side = [&](int o) {
        asm volatile("s_cmp_gt_u32 %4, 1\n\ts_cbranch_scc1 1f\n\ts_mov_b32 m0, %0\n\ts_nop 0\n\tbuffer_load_dwordx4 %1, %2, %3 offen lds\n1:"
                     ::"s"(L0 + K2_X + g * 4096 + u * 2048 + o * 1024), "v"(vx[o]), "s"(srdX), "s"(u * 32768), "s"(u) : "m0", "scc");
      };
      pair(IC<0>{}, IC<1>{}, [&](auto ii) { if (ii.value == 2) side(0); if (ii.value == 7) touch_g_if<K2_TI>(it, L0 + K3_SCR, tv, ta); });
      pair(IC<1>{}, IC<1>{}, [&](auto ii) { if (ii.value == 2) side(1); });
    }
    pair(IC<0>{}, IC<1>{}, none);
    pair(IC<1>{}, IC<0>{}, [&](auto ii) {
      constexpr int r = ii.value - 8;
      if constexpr (r == -1) WAIT_V(0);
      if constexpr (r >= 0 && r < 4) fx[r] = lds128(rx + (r >> 1) * 2048 + (unsigned)((4 * (r & 1) + fq) ^ (fr >> 1)) * 16);
    });
    sfor<0, 8>([&](auto ii) { constexpr int i = ii.value; mfma_acc(acc[i >> 2][i & 3], sel[i & 1], fx[i >> 1]); });
    MFMA_DONE();
    DLY(6);
    STAMP_L1(1);
    float sq[2];
  #pragma unroll
    for (int m = 0; m < 2; ++m) {
      const float mu = mux[m] + red_add(accW[m][0] + accW[m][1]);
      char* hb = ws + WS_HC + (size_t)(2 * pn) * 131072 + (size_t)(pm * 8 + 2 * g + m) * 1024 + lane * 16;
      float ss = 0.f;
  #pragma unroll
      for (int c = 0; c < 2; ++c) {
        const f32x4 y0 = acc[m][2 * c] - mu, y1 = acc[m][2 * c + 1] - mu;
        const u32x4 hp = {pk(y0[0], y0[1]), pk(y0[2], y0[3]), pk(y1[0], y1[1]), pk(y1[2], y1[3])};
        st16(hb + c * 131072, hp);
  #pragma unroll
        for (int e = 0; e < 4; ++e) ss = dot2(hp[e], hp[e], ss);
      }
      sq[m] = red_add(ss);
      if (fq == 0 && pn == 0) asm volatile("global_store_dword %0, %1, off sc1" ::"v"((float*)(ws + WS_MU) + tok + 16 * m), "v"(mu) : "memory");
    }
    if (lane < 32)
      asm volatile("global_store_dword %0, %1, off sc1" ::"v"(ws + WS_HC + (size_t)32 * 131072 + (pm * 16 + pn) * 512 + g * 128 + lane * 4), "v"(fq == 0 ? sq[0] : sq[1]) : "memory");
    STAMP_END(1);
    WAIT_V(0);
  }
  BARRIER();
  unsigned* cnt = (unsigned*)(ws + WS_CNT + pm3 * 256);
#define K23_AT 1
  if (K23_AT && wid == 7) {
    unsigned d = 1;
#pragma nounroll
    for (int spin = 0; spin < (1 << 18); ++spin) {
      unsigned v;
      asm volatile("s_mov_b64 exec, 1\n\tglobal_atomic_add %0, %1, %2, %3 sc0\n\ts_mov_b64 exec, -1\n\ts_waitcnt vmcnt(0)" : "=&v"(v) : "v"(0), "v"(d), "s"(cnt) : "memory");
      if (__builtin_amdgcn_readfirstlane(v) + d >= 32u) break;
      d = 0;
      __builtin_amdgcn_s_sleep(K23_SLEEP);
    }
  }
  if (!K23_AT && wid == 0 && lane == 0) __hip_atomic_fetch_add(cnt, 1u, __ATOMIC_RELAXED, __HIP_MEMORY_SCOPE_AGENT);
  DLY(2);
  const int pm = pm3, pn = pn3;
  STAMP_BEG(2);
  TS_DECL(2, K3_LDS); TS();

  const u32x4 srdA = make_srd(ws + WS_HC, 32 * 131072);
  const unsigned voffA = lane * 16 + wid * 2048 + pm * 16384;
#define K3_PF 6
  unsigned voffB;
  { const unsigned L = (wid < 7 ? wid : 6) * 64 + lane, r = (L * 4682u) >> 16; voffB = r * 7168 + pn * 224 + (L - r * 14) * 16; }
  const unsigned ldA = L0 + wid * 2048, ldG = wid < 7 ? L0 + K3_G + wid * 1024 : L0 + K3_SCR, wz = wid < 7 ? ~0u : 0u;
  auto tA = [&](int pa, int i) { return ldA + pa * 32768 + (i >> 1) * 16384 + (i & 1) * 1024; };
  auto tB = [&](unsigned ob, int i) { return ldG + ((ob + (i >> 1) * K3_BS + (i & 1) * K3_UG) & wz); };
  auto gA = [&](int j, int i) { dma_go1(voffA, srdA, j * 262144 + (i >> 1) * 131072 + (i & 1) * 1024); };
  auto gB = [&](int j, int i) { dma_go(voffB, (i & 1) ? srdU : srdG, (2 * j + (i >> 1)) * (32 * 7168)); };
  auto dA = [&](int j, int pa, int i) { dma_to(tA(pa, i)); asm volatile("s_nop 0"); dma_go1(voffA, srdA, j * 262144 + (i >> 1) * 131072 + (i & 1) * 1024); };
  auto dB = [&](int j, unsigned ob, int i) { dma(tB(ob, i), voffB, (i & 1) ? srdU : srdG, (2 * j + (i >> 1)) * (32 * 7168)); };
  if (!K23_AT || wid < 7) {
#pragma unroll
    for (int i = 0; i < 8; ++i) dB(1 + (i >> 2), (1 + (i >> 2)) * K3_BP, i & 3);
  }
  if (!K23_AT && wid == 0) {
#pragma nounroll
    for (int spin = 0; spin < (1 << 20); ++spin) {
      if (__hip_atomic_load(cnt, __ATOMIC_RELAXED, __HIP_MEMORY_SCOPE_AGENT) >= 32u) break;
      __builtin_amdgcn_s_sleep(K23_SLEEP);
    }
  }
  BARRIER();
#pragma unroll
  for (int t = 0; t < 4; ++t) {
    dA(t >> 1, t >> 1, 2 * (t & 1)); dA(t >> 1, t >> 1, 2 * (t & 1) + 1);
    if (t < K3_ORD) BARRIER();
  }
  const unsigned ra = L0 + wm * 4096 + lane * 16;
  unsigned rb = L0 + K3_G + (unsigned)(4 * fq + (fr >> 2)) * 224 + 112 * wn + (lane & 3) * 8;
  unsigned rm = L0 + K3_G + (unsigned)(4 * fq + (fr >> 2)) * 224 + 112 * wn + 96 + (lane & 1) * 8 + ((lane >> 1) & 1) * K3_UG;

  f32x4 acc[4][7];
  u32x4 fa[2][2][4]; u32x2 fb[7][2][2];
  auto rdA = [&](int pa, int i) { fa[pa][i >> 2][i & 3] = lds128(ra + pa * 32768 + (i >> 2) * 16384 + (i & 3) * 1024); };
  auto rdB = [&](int n, int i) {
    fb[n][i >> 1][i & 1] = n < 6 ? ldstr(rb + (n >> 1) * 32 + (n & 1) * K3_UG + (i >> 1) * K3_BS + (i & 1) * 3584) : ldstr(rm + (i >> 1) * K3_BS + (i & 1) * 3584);
  };
  f32x4 ssq[4];
  float cn[3][6], pv[3];
  auto fill = [&](int k) {
#define PIN(d, e) do { float r_ = (e); asm volatile("" : "+v"(r_)); d = r_; } while (0)
    if (k < 12) {
      const int m = k & 3, t = k >> 2;
      PIN(ssq[m][t == 1 ? 2 : 0], ssq[m][t == 1 ? 2 : 0] + ssq[m][t == 0 ? 1 : t == 1 ? 3 : 2]);
    } else if (k < 14) { float a = ssq[2 * k - 24][0], b = ssq[2 * k - 23][0]; swap32f(a, b); ssq[2 * k - 24][0] = a; ssq[2 * k - 23][0] = b; }
    else if (k < 16) PIN(pv[k - 14], ssq[2 * k - 28][0] + ssq[2 * k - 27][0]);
    else if (k == 16) { unsigned a = __float_as_uint(pv[0]), b = __float_as_uint(pv[1]); swap16u(a, b); pv[0] = __uint_as_float(a); pv[1] = __uint_as_float(b); }
    else if (k == 17) PIN(pv[2], pv[0] + pv[1]);
    else if (k == 18) PIN(pv[2], pv[2] * (1.0f / D_) + LN_EPS);
    else if (k == 19) PIN(pv[1], __builtin_amdgcn_rsqf(pv[2]));
    else if (k == 20) PIN(cn[2][4], pv[2]);
    else if (k == 21) PIN(pv[0], pv[1] * (-2.0f * 0.7978845608028654f * 1.4426950408889634f));
    else if (k == 22) PIN(pv[1], pv[1] * pv[1]);
    else if (k == 23) PIN(pv[1], pv[1] * 0.044715f);
    else if (k == 24) PIN(pv[1], pv[1] * pv[0]);
    else if (k < 27) PIN(cn[k - 25][4], pv[k - 25]);
    else if (k < 30) { const int q = k - 27; unsigned a = __float_as_uint(cn[q][4]), b = __float_as_uint(pv[q]); swap16u(a, b); cn[q][4] = __uint_as_float(a); cn[q][5] = __uint_as_float(b); }
    else if (k < 42) { const int q = (k - 30) >> 2, m = (k - 30) & 3; PIN(cn[q][m], cn[q][4 + (m >> 1)]); }
    else if (k < 48) { const int q = (k - 42) >> 1, h = (k - 42) & 1; swap32f(cn[q][2 * h], cn[q][2 * h + 1]); }
  };
  unsigned ob = 0, obp = 2 * K3_BP;
  auto pair = [&](int j, auto fl, auto VM, auto&& at) {
    constexpr int pa = fl.value & 1;
    constexpr bool DA = fl.value & 2, DB = fl.value & 4, B0 = fl.value & 8, NX = fl.value & 16;
    const unsigned ob0 = ob;
    sfor<0, 56>([&](auto ii) {
      constexpr int i = ii.value, n = i >> 3, e = i & 7, q = (i >> 1) & 3;
      constexpr bool ST = B0 && i < 16;
      constexpr int mn = ST ? ((i + 4) >> 3) & 1 : n, mm = ST ? ((i & 4) ? 3 - (i & 3) : i & 3) : (n & 1) ? 3 - q : q, h = ST ? i >> 3 : (i ^ (i >> 1)) & 1;
      if constexpr (i == 40 && NX) { WAIT_VL(VM.value); BARRIER(); TS(); }
      if constexpr (B0 && (i < 16 ? i < 8 : (i & 1) == 0)) mfma_new(acc[mm][mn], cat(fb[mn][h][0], fb[mn][h][1]), fa[pa][h][mm]);
      else mfma_acc(acc[mm][mn], cat(fb[mn][h][0], fb[mn][h][1]), fa[pa][h][mm]);
      constexpr int k = k3_at(B0, i), k1 = k3_at(B0, i + 1);
      if constexpr (k >= 0) {
        SB();
        if (k < 4) { if (DA) gA(j + 2, k); }
        else if (k < 6) { if (DA && !B0) gB(j + 2, k - 2); }
        else if (K3_B2) { if (DA && !B0) gB(j + 2, k - 6); }
        else if (DB) gB(j + 3, k - 6);
      }
#define K3_R6 32
      if constexpr (n < 4 && e < 4) rdB(n + 2, e);
      if constexpr (i >= K3_R6 && i < K3_R6 + 4) rdB(6, i - K3_R6);
      if constexpr (NX && i == 37) { const unsigned dl = ob == 2 * K3_BP ? (unsigned)(-2 * K3_BP) : (unsigned)K3_BP; ob += dl; vadd(rb, dl); vadd(rm, dl); }
      if constexpr (NX && n == 5) { if (e < 4) rdB(0, e); rdA(pa ^ 1, e); }
      if constexpr (NX && n == 6 && e < 4) rdB(1, e);
      at(ii);
      if constexpr (k1 >= 0) {
        if (k1 < 4) { if (DA) dma_to(tA(pa, k1)); }
        else if (k1 < 6) { if (DA && !B0) dma_to(tB(obp, k1 - 2)); }
        else if (K3_B2) { if (DA && !B0) dma_to(tB(obp, k1 - 6)); }
        else if (DB) dma_to(tB(ob0, k1 - 6));
      }
      SB();
    });
    obp = ob0;
  };
#define WAIT_L4() do { SB(); MEMFENCE(); __builtin_amdgcn_s_waitcnt(0xC47F); MEMFENCE(); SB(); } while (0)
  auto none = [](auto) {};
  ALN(AL3);
  WAIT_V(6); BARRIER();
  rdB(0, 0); rdB(0, 1);
#pragma unroll
  for (int i = 0; i < 4; ++i) rdA(0, i);
  rdB(1, 0); rdB(1, 1);
  constexpr int NL = 28 - GUL / 4;
  auto rest = [&]() {
    const int lane = lane_now();
    const unsigned v = (lane < NL ? lane : NL - 1) * 128, so = GUL * 65536 + (wid * 256 + blockIdx.x) * (NL * 128);
    touch_b(L0 + K3_SCR, v, srdG, so); touch_b(L0 + K3_SCR, v, srdU, so);
  };
#define K3_WD 2
#define K3_XJ 4
  auto side = [&](int j) {
    if (__builtin_expect(j + 1 == wid || j == wid + K3_XJ, 0)) {
      if (j + 1 == wid) rest();
      else {
        const uint64_t xc = (uint64_t)(ws + WS_XCH) + (size_t)blockIdx.x * 16384 - pm * 16384;
        if (blockIdx.x == 0 && wid == 0 && lane_now() == 0)
          __hip_atomic_store((unsigned*)(ws + WS_WTAB + 8192), mark_of(L0), __ATOMIC_RELAXED, __HIP_MEMORY_SCOPE_AGENT);
        st16s<0>(xc, voffA, (u32x4){~0u, ~0u, ~0u, ~0u}); st16s<1024>(xc, voffA, (u32x4){~0u, ~0u, ~0u, ~0u});
        if (K3_WD == 3 || K3_WD == 5) {
          const int lane = lane_now(), l = lane < 56 ? lane : 55, P = (xcd & 1) ? (wid < 7 ? 9 + wid : 8) : wid;
          touch_b(L0 + K3_SCR, (l >> 3) * 2048 + (l & 7) * 128, make_srd(wd, D_ * F_ * 2), (224 * P + 7 * idx) * 2048 + ((xcd >> 1) & 1) * 1024);
        }
      }
    }
  };
  WAIT_VL(4);
  BARRIER();
  STAMP_L0(); TS();
  rdB(1, 2); rdB(1, 3);
#pragma unroll
  for (int i = 4; i < 8; ++i) rdA(0, i);
  rdB(0, 2); rdB(0, 3);
  pair(0, IC<30>{}, IC<0>{}, none);
  if (wid == 0) rest();
  side(0);
#pragma nounroll
  for (int j = 1; j < 13; j += 2) {
    pair(j, IC<23>{}, IC<4>{}, none);
    side(j);
    pair(j + 1, IC<22>{}, IC<4>{}, none);
    side(j + 1);
  }
  float rmu[2];
  const char* mup = ws + WS_MU + (((xcd >> 2) * 8 + (idx >> 2)) * 8 + 4 * (wid & 1) + (xcd & 1)) * 64;
  {
    const u32x4 srdD = make_srd(wd, D_ * F_ * 2);
    const int lane = lane_now(), l = lane < 56 ? lane : 55, P = (xcd & 1) ? (wid < 7 ? 9 + wid : 8) : wid;
    const unsigned vd = (l >> 3) * 2048 + (l & 7) * 128, sd = (224 * P + 7 * idx) * 2048 + ((xcd >> 1) & 1) * 1024;
    const unsigned vq = (lane >> 4) * 2048 + (lane & 15) * 4;
    const char* sqb = ws + WS_HC + (size_t)32 * 131072 + (2 * pm + (wm >> 1)) * 8192 + (wm & 1) * 256;
    pair(13, IC<19>{}, IC<4>{}, [&](auto ii) {
      constexpr int i = ii.value;
      if constexpr (i >= 40) {
        float v;
        asm volatile("global_load_dword %0, %1, %2 offset:%3 sc1" : "=v"(v) : "v"(vq), "s"(sqb), "n"(((i - 40) & 3) * 512 + ((i - 40) >> 2) * 64));
        ssq[(i - 40) >> 2][(i - 40) & 3] = v;
      }
      if constexpr (K3_WD == 0 && i >= 46 && i < 54) touch_b_if<i - 46>(wid, L0 + K3_SCR, vd, srdD, sd);
      if constexpr (K3_WD == 4 && i < 16 && (i & 1) == 0) touch_b_if<(i >> 1)>(wid, L0 + K3_SCR, vd, srdD, sd);
    });
    pair(14, IC<16>{}, IC<(K3_WD == 2 && K4_WL == 8) || K3_WD == 5>{}, [&](auto ii) {
      constexpr int i = ii.value;
      if constexpr ((K3_WD == 2 || K3_WD == 5) && i >= (K4_WL == 8 ? 0 : 41) && i < (K4_WL == 8 ? 0 : 41) + 2 * K4_WL && ((i & 1) == 0) == (K4_WL == 8)) touch_b_if<((i - (K4_WL == 8 ? 0 : 41)) >> 1)>(wid, L0 + K3_SCR, vd, srdD, sd);
      if constexpr (i == 42) ld4s<0>(rmu[0], (lane_now() & 15) * 4, mup);
      if constexpr (i == 44) ld4s<128>(rmu[1], (lane_now() & 15) * 4, mup);
    });
  }
  asm volatile("" : "+v"(ssq[0]), "+v"(ssq[1]), "+v"(ssq[2]), "+v"(ssq[3]));
  pair(15, IC<1>{}, IC<0>{}, [&](auto ii) { if (ii.value < 48) fill(ii.value); });
  MFMA_DONE();
  STAMP_L1(2); TS();
  DLY(4);
  const int ln = lane_now();
  const int P = pn >> 1, w = 2 * (pn & 1) + wn, mt0 = pm * 16 + 4 * wm;
  const uint64_t ab = (uint64_t)(ws + WS_ACT + (size_t)(7 * P) * 131072 + (size_t)mt0 * 1024);
  unsigned xb[4][2];
  sfor<0, 4>([&](auto mm_) {
    constexpr int m = decltype(mm_)::value;
    unsigned o[3][2];
#pragma unroll
    for (int c = 0; c < 3; ++c) {
      const f32x4 a = acc[m][2 * c], b = acc[m][2 * c + 1];
      o[c][0] = geglu2(a.xy, b.xy, cn[0][m], cn[1][m], cn[2][m]);
      o[c][1] = geglu2(a.zw, b.zw, cn[0][m], cn[1][m], cn[2][m]);
    }
    st16s<m * 1024>(ab + (size_t)w * 131072, ln * 16, (u32x4){o[0][0], o[0][1], o[1][0], o[1][1]});
    xb[m][0] = o[2][0]; xb[m][1] = o[2][1];
  });
  const int hf = ln >> 5, fq1 = (ln >> 4) & 1;
  unsigned xm[2][2];
  sfor<0, 2>([&](auto mp_) {
    constexpr int mp = decltype(mp_)::value;
    swap32u(xb[2 * mp][0], xb[2 * mp + 1][0]); swap32u(xb[2 * mp][1], xb[2 * mp + 1][1]);
    st16s<mp * 2048>(ab + (size_t)(4 + (w >> 1)) * 131072 + (w & 1) * 512, (ln & 31) * 16 + hf * 1024,
                     (u32x4){xb[2 * mp][0], xb[2 * mp][1], xb[2 * mp + 1][0], xb[2 * mp + 1][1]});
    f32x4 a = acc[2 * mp][6], b = acc[2 * mp + 1][6];
#pragma unroll
    for (int r = 0; r < 4; ++r) { float u = a[r], v = b[r]; swap32f(u, v); a[r] = u; b[r] = v; }
    xm[mp][0] = geglu2(a.xy, b.xy, cn[0][4 + mp], cn[1][4 + mp], cn[2][4 + mp]); xm[mp][1] = geglu2(a.zw, b.zw, cn[0][4 + mp], cn[1][4 + mp], cn[2][4 + mp]);
  });
  swap16u(xm[0][0], xm[1][0]); swap16u(xm[0][1], xm[1][1]);
  st16s<0>(ab + (size_t)6 * 131072 + w * 256, (2 * fq1 + hf) * 1024 + (ln & 15) * 16, (u32x4){xm[0][0], xm[0][1], xm[1][0], xm[1][1]});
  TS_OUT();
  BW_OUT(2);
  STAMP_END_ND(2);
  DLY(3);
  WAIT_V(7);
  asm volatile("" : "+v"(rmu[0]), "+v"(rmu[1]));
  if (wid < 4) {
    k4_loaders(wd, ws, L0, wid);
  } else k4_mfma(wd, ws, out, L0, wid - 4, rmu);
}

extern "C" __global__ void kz_pad() { asm volatile(".fill 4096, 4, 0xBF800000"); }

extern "C" void kernel_launch(void* const* d_in, const int* in_sizes, int n_in, void* d_out, int out_size, void* d_ws, size_t ws_size,
                              hipStream_t stream) {
  const u16* x = (const u16*)d_in[0];
  const u16* wqkv = (const u16*)d_in[1];
  const u16* wo = (const u16*)d_in[2];
  const u16* wg = (const u16*)d_in[4];
  const u16* wu = (const u16*)d_in[5];
  const u16* wd = (const u16*)d_in[6];
  char* ws = (char*)d_ws;
  u16* out = (u16*)d_out;
  k_all<<<256, 512, 0, stream>>>(x, wqkv, wo, wg, wu, wd, ws, out);
}
```
